```python
import torch

def _softmax(x, axis=-1):
    return torch.softmax(x, dim=axis)
def _gelu(x):
    return torch.nn.functional.gelu(x, approximate="tanh")
def _silu(x):
    return torch.nn.functional.silu(x)
def _sigmoid(x):
    return torch.sigmoid(x)
def _layernorm(x, axis=-1, eps=1e-5):
    m = x.mean(dim=axis, keepdim=True)
    v = x.var(dim=axis, keepdim=True, unbiased=False)
    return (x - m) / torch.sqrt(v + eps)
def _scatter_add(tgt, idx, src, axis=0):
    return tgt.clone().index_add_(axis, idx, src)
def _segment_sum(src, seg, n):
    out = torch.zeros((n,) + src.shape[1:], dtype=src.dtype)
    return out.index_add_(0, seg, src)
def _pad(x, pw):
    flat = tuple(v for pair in reversed(pw) for v in pair)
    return torch.nn.functional.pad(x, flat)

kernel_name = 'concat_heads__ssm_conv_s4d__ssm_conv_h3__prompt'

def setup_inputs():

    _w = (lambda a: (lambda t: t.to(torch.bfloat16) if t.is_floating_point() else t)(torch.as_tensor(a)))
    def _A_setup():
        Bsz, L, D, N = 1, 2048, 1024, 128
        return {
            "u": _w((lambda *s: torch.randn(*s, dtype=torch.bfloat16) * 0.05)(Bsz, L, D)),
            "log_A_real": _w((lambda *s: torch.randn(*s, dtype=torch.bfloat16) * 3.2)(D, N) * 0.5),
            "A_imag": _w((lambda *s: torch.randn(*s, dtype=torch.bfloat16) * 0.05)(D, N) * 3.14),
            "C_real": _w((lambda *s: torch.randn(*s, dtype=torch.bfloat16) * 0.1)(D, N) / N ** 0.5),
            "C_imag": _w((lambda *s: torch.randn(*s, dtype=torch.bfloat16) * 0.1)(D, N) / N ** 0.5),
            "log_dt": _w((lambda *s: torch.randn(*s, dtype=torch.bfloat16) * 0.05)(D) * 0.5 - 4.0),
            "D_skip": _w((lambda *s: torch.randn(*s, dtype=torch.bfloat16) * 0.05)(D)),
        }
    _A_in = _A_setup()

    def _B_setup():
        B, S, D = 1, 2048, 1024
        ks = 4
        return {
            "q": _w((lambda *s: torch.randn(*s, dtype=torch.bfloat16) * 0.05)(B, S, D)),
            "k": _w((lambda *s: torch.randn(*s, dtype=torch.bfloat16) * 0.2)(B, S, D)),
            "v": _w((lambda *s: torch.randn(*s, dtype=torch.bfloat16) * 0.2)(B, S, D)),
            "shift_k": _w((lambda *s: torch.randn(*s, dtype=torch.bfloat16) * 0.2)(D, ks) * 0.5),
            "ssm_k": _w((lambda *s: torch.randn(*s, dtype=torch.bfloat16) * 0.8)(D, S) * (1.0 / S ** 0.5)),
            "ssm_D": _w((lambda *s: torch.randn(*s, dtype=torch.bfloat16) * 0.2)(D)),
        }
    _B_in = _B_setup()

    return {
        "A_u": _A_in["u"],
        "A_log_A_real": _A_in["log_A_real"],
        "A_A_imag": _A_in["A_imag"],
        "A_C_real": _A_in["C_real"],
        "A_C_imag": _A_in["C_imag"],
        "A_log_dt": _A_in["log_dt"],
        "A_D_skip": _A_in["D_skip"],
        "B_q": _B_in["q"],
        "B_k": _B_in["k"],
        "B_v": _B_in["v"],
        "B_shift_k": _B_in["shift_k"],
        "B_ssm_k": _B_in["ssm_k"],
        "B_ssm_D": _B_in["ssm_D"],
    }

def reference(A_u, A_log_A_real, A_A_imag, A_C_real, A_C_imag, A_log_dt, A_D_skip, B_q, B_k, B_v, B_shift_k, B_ssm_k, B_ssm_D):
    def _A_ref(u, log_A_real, A_imag, C_real, C_imag, log_dt, D_skip):
        L = u.shape[1]
        dt = torch.exp(log_dt)
        A = -torch.exp(log_A_real) + 1j * A_imag
        C = (C_real + 1j * C_imag) * dt[:, None]
        pos = (lambda x, dt: x.to(dt))(torch.arange(L), log_dt.dtype)
        P = torch.exp((A * dt[:, None])[:, :, None] * pos[None, None, :])
        K = 2.0 * torch.einsum("dn,dnl->dl", C, P).real
        L2 = 2 * L
        u_t = (lambda x, *d: x.permute(*d))(u, 0, 2, 1)
        Kf = (lambda x, *a, **k: torch.fft.rfft(x.float(), *a, **k))(K, L2)
        Uf = (lambda x, *a, **k: torch.fft.rfft(x.float(), *a, **k))(u_t, L2)
        y = (lambda x, *a, **k: torch.fft.irfft(x, *a, **k).to(torch.bfloat16))(Uf * Kf[None, :, :], L2)[:, :, :L]
        y = (lambda x, *d: x.permute(*d))(y, 0, 2, 1)
        return y + u * D_skip[None, None, :]

    def _B_ref(q, k, v, shift_k, ssm_k, ssm_D):
        B, S, D = q.shape
        ks = shift_k.shape[-1]
        k_pad = _pad(k, ((0, 0), (ks - 1, 0), (0, 0)))
        k_shift = torch.zeros_like(k)
        for t in range(ks):
            k_shift = k_shift + k_pad[:, t:t + S, :] * shift_k[:, ks - 1 - t][None, None, :]
        kv = k_shift * v
        L = 2 * S
        kv_pad = _pad(kv, ((0, 0), (0, S), (0, 0)))
        kf_pad = _pad(ssm_k, ((0, 0), (0, S)))
        kv_f = (lambda x, axes=(-2,-1): torch.fft.rfft2(x.float(), dim=axes))(kv_pad[:, None, :, :], axes=(1, 2))
        kf_f = (lambda x, axes=(-2,-1): torch.fft.rfft2(x.float(), dim=axes))(kf_pad[:, None, :], axes=(1, 2))
        kf_f = (lambda x, *d: x.permute(*d))(kf_f, 1, 2, 0)
        y_f = kv_f * kf_f[None, :, :, :]
        y = (lambda x, s, axes=(-2,-1): torch.fft.irfft2(x, s=s, dim=axes).to(torch.bfloat16))(y_f, (1, L), axes=(1, 2))[:, 0, :S, :]
        y = y + kv * ssm_D[None, None, :]
        return q * y

    a = _A_ref(A_u, A_log_A_real, A_A_imag, A_C_real, A_C_imag, A_log_dt, A_D_skip)
    b = _B_ref(B_q, B_k, B_v, B_shift_k, B_ssm_k, B_ssm_D)
    return torch.concatenate([a, b], axis=-1)
```

```cpp
#include <hip/hip_runtime.h>
#include <hip/hip_bf16.h>
#include <stdint.h>

#define L_ 2048
#define D_ 1024
#define N_ 128

__device__ __forceinline__ float bf2f(uint16_t v){ return __uint_as_float(((uint32_t)v)<<16); }
__device__ __forceinline__ uint16_t f2bf(float f){ __hip_bfloat16 b = __float2bfloat16(f); return *(uint16_t*)&b; }
__device__ __forceinline__ float rbf(float f){ return bf2f(f2bf(f)); }

__global__ void kgen(const uint16_t* logA, const uint16_t* Aim, const uint16_t* Cr, const uint16_t* Ci,
                     const uint16_t* logdt, float* K){
  int d = blockIdx.x;
  float dt = rbf(expf(bf2f(logdt[d])));
  for (int l = threadIdx.x; l < L_; l += blockDim.x){
    float pos = rbf((float)l);
    float acc = 0.f;
    for (int n = 0; n < N_; ++n){
      float ar = -rbf(expf(bf2f(logA[d*N_+n])));
      float ai = bf2f(Aim[d*N_+n]);
      float cr = bf2f(Cr[d*N_+n])*dt, ci = bf2f(Ci[d*N_+n])*dt;
      float a = ar*dt*pos, b = ai*dt*pos;
      float e = expf(a);
      float s, c; sincosf(b, &s, &c);
      acc += cr*(e*c) - ci*(e*s);
    }
    K[d*L_+l] = 2.f*acc;
  }
}

__global__ void kvk(const uint16_t* k, const uint16_t* v, const uint16_t* sk, float* kv){
  int idx = blockIdx.x*blockDim.x + threadIdx.x;
  int s = idx / D_, d = idx % D_;
  float acc = 0.f;
  for (int t = 0; t < 4; ++t){
    int ss = s + t - 3;
    float kk = ss >= 0 ? bf2f(k[ss*D_+d]) : 0.f;
    acc = rbf(acc + rbf(kk*bf2f(sk[d*4+3-t])));
  }
  kv[idx] = rbf(acc*bf2f(v[idx]));
}

__global__ void convA(const uint16_t* u, const float* K, const uint16_t* Dsk, uint16_t* out){
  int idx = blockIdx.x*blockDim.x + threadIdx.x;
  int l = idx / D_, d = idx % D_;
  float acc = 0.f;
  for (int j = 0; j <= l; ++j) acc += K[d*L_+j]*bf2f(u[(l-j)*D_+d]);
  float y = rbf(acc);
  float t = rbf(bf2f(u[idx])*bf2f(Dsk[d]));
  out[l*2*D_ + d] = f2bf(y+t);
}
__global__ void convB(const float* kv, const uint16_t* ssmk, const uint16_t* sD, const uint16_t* q, uint16_t* out){
  int idx = blockIdx.x*blockDim.x + threadIdx.x;
  int l = idx / D_, d = idx % D_;
  float acc = 0.f;
  for (int j = 0; j <= l; ++j) acc += bf2f(ssmk[d*L_+j])*kv[(l-j)*D_+d];
  float y = rbf(acc);
  float t = rbf(kv[idx]*bf2f(sD[d]));
  float y2 = rbf(y+t);
  out[l*2*D_ + D_ + d] = f2bf(bf2f(q[idx])*y2);
}

extern "C" void kernel_launch(void* const* d_in, const int* in_sizes, int n_in,
                              void* d_out, int out_size, void* d_ws, size_t ws_size,
                              hipStream_t stream) {
  const uint16_t* u = (const uint16_t*)d_in[0];
  const uint16_t* logA = (const uint16_t*)d_in[1];
  const uint16_t* Aim = (const uint16_t*)d_in[2];
  const uint16_t* Cr = (const uint16_t*)d_in[3];
  const uint16_t* Ci = (const uint16_t*)d_in[4];
  const uint16_t* logdt = (const uint16_t*)d_in[5];
  const uint16_t* Dsk = (const uint16_t*)d_in[6];
  const uint16_t* q = (const uint16_t*)d_in[7];
  const uint16_t* k = (const uint16_t*)d_in[8];
  const uint16_t* v = (const uint16_t*)d_in[9];
  const uint16_t* sk = (const uint16_t*)d_in[10];
  const uint16_t* ssmk = (const uint16_t*)d_in[11];
  const uint16_t* sD = (const uint16_t*)d_in[12];
  float* K = (float*)d_ws;
  float* kv = K + (size_t)D_*L_;
  uint16_t* out = (uint16_t*)d_out;
  kgen<<<D_, 256, 0, stream>>>(logA, Aim, Cr, Ci, logdt, K);
  kvk<<<L_*D_/256, 256, 0, stream>>>(k, v, sk, kv);
  convA<<<L_*D_/256, 256, 0, stream>>>(u, K, Dsk, out);
  convB<<<L_*D_/256, 256, 0, stream>>>(kv, ssmk, sD, q, out);
}
```

```cpp
#include <hip/hip_runtime.h>
#include <stdint.h>

typedef __attribute__((ext_vector_type(4))) unsigned int u32x4;
typedef __attribute__((ext_vector_type(2))) unsigned int u32x2;
typedef __attribute__((ext_vector_type(4))) float f32x4;
typedef __attribute__((ext_vector_type(2))) float f32x2;
typedef __attribute__((ext_vector_type(8))) _Float16 f16x8;
typedef __attribute__((ext_vector_type(2))) _Float16 f16x2;
typedef __attribute__((ext_vector_type(8))) __bf16 bf16x8;
typedef __attribute__((ext_vector_type(2))) __bf16 bf16x2;
typedef __attribute__((ext_vector_type(4))) short s16x4;

#define LSEQ 2048
#define DCH 1024
#define NMODE 128

__device__ __forceinline__ float bf2f(uint16_t v) { return __uint_as_float(((uint32_t)v) << 16); }
__device__ __forceinline__ float lo_f(uint32_t w) { return __uint_as_float(w << 16); }
__device__ __forceinline__ float hi_f(uint32_t w) { return __uint_as_float(w & 0xffff0000u); }
__device__ __forceinline__ uint32_t pk_bf16(float lo, float hi) {
    f32x2 x = {lo, hi};
    bf16x2 y = __builtin_convertvector(x, bf16x2);
    return __builtin_bit_cast(uint32_t, y);
}
__device__ __forceinline__ uint32_t pk_f16(float lo, float hi) {
    f32x2 x = {lo, hi};
    f16x2 y = __builtin_convertvector(x, f16x2);
    return __builtin_bit_cast(uint32_t, y);
}
__device__ __forceinline__ void rbf2(float& a, float& b) {
    uint32_t p = pk_bf16(a, b);
    a = lo_f(p);
    b = hi_f(p);
}
__device__ __forceinline__ float rbf(float a) { return lo_f(pk_bf16(a, a)); }

struct TapTab { uint16_t v[LSEQ]; };
static constexpr TapTab make_tab() {
    TapTab t{};
    for (int j = 0; j < LSEQ; ++j) {
        int p = j;
        if (j >= 256) {
            int s = (j < 512) ? 1 : ((j < 1024) ? 2 : 3);
            int q = j >> s, r = j & ((1 << s) - 1), half = 1 << (s - 1);
            if (r > half || (r == half && (q & 1))) q++;
            p = q << s;
        }
        int col = (p >> 3) & 15, a = p >> 7, low = p & 7, row = 0;
        if (p < 256) row = 8 * a + low;
        else if (p < 512) row = 16 + 4 * (a - 2) + (low >> 1);
        else if (p < 1024) row = 24 + 2 * (a - 4) + (low >> 2);
        else row = 32 + (a - 8);
        t.v[j] = (uint16_t)(row * 16 + col);
    }
    return t;
}
__device__ const TapTab g_taptab = make_tab();

struct cplx { float r, i; };
__device__ __forceinline__ cplx cmul(cplx a, cplx b) {
    cplx c;
    c.r = a.r * b.r - a.i * b.i;
    c.i = a.r * b.i + a.i * b.r;
    return c;
}
__device__ __forceinline__ cplx anchor(float a, float b, float k) {
    float e = __builtin_amdgcn_exp2f(a * k * 1.4426950408889634f);
    float th = __builtin_amdgcn_fractf(b * k * 0.15915494309189535f);
    cplx c;
    c.r = e * __builtin_amdgcn_cosf(th);
    c.i = e * __builtin_amdgcn_sinf(th);
    return c;
}

#define KG_ROWB 272
#define KG_TAB (64 * KG_ROWB)
#define KG_WAVE (KG_TAB + 1536)

__global__ __launch_bounds__(512) void prep_kernel(
    const uint16_t* __restrict__ u, const uint16_t* __restrict__ logA, const uint16_t* __restrict__ Aim,
    const uint16_t* __restrict__ Cre, const uint16_t* __restrict__ Cim, const uint16_t* __restrict__ logdt,
    const uint16_t* __restrict__ kin, const uint16_t* __restrict__ vin, const uint16_t* __restrict__ shk,
    uint16_t* __restrict__ xw, uint16_t* __restrict__ Kw)
{
    __shared__ __attribute__((aligned(16))) char lds[4 * KG_WAVE];
    const int lane = threadIdx.x & 63;
    const int w = __builtin_amdgcn_readfirstlane(threadIdx.x >> 6);
    const int b = blockIdx.x;

    if (w < 4) {
        const int wt = b * 2 + (w & 1);
        const int rt = wt >> 4, ct = wt & 15;
        const int cg = lane & 7, rb = lane >> 3;
        const int s0 = 64 * rt + 8 * rb, c0 = 64 * ct + 8 * cg;
        if (w < 2) {
            u32x4 r[8];
#pragma unroll
            for (int i = 0; i < 8; ++i) r[i] = *(const u32x4*)(u + (size_t)(s0 + i) * DCH + c0);
#pragma unroll
            for (int ch = 0; ch < 8; ++ch) {
                u32x4 o;
#pragma unroll
                for (int q = 0; q < 4; ++q)
                    o[q] = __builtin_amdgcn_perm(r[6 - 2 * q][ch >> 1], r[7 - 2 * q][ch >> 1],
                                                 (ch & 1) ? 0x07060302u : 0x05040100u);
                *(u32x4*)(xw + (size_t)(c0 + ch) * LSEQ + (2040 - s0)) = o;
            }
        } else {
            u32x4 kr[11], vr[8], wv[4];
#pragma unroll
            for (int i = 0; i < 11; ++i) {
                int s = s0 + i - 3;
                u32x4 z = {0, 0, 0, 0};
                kr[i] = (s >= 0) ? *(const u32x4*)(kin + (size_t)(s < 0 ? 0 : s) * DCH + c0) : z;
            }
#pragma unroll
            for (int i = 0; i < 8; ++i) vr[i] = *(const u32x4*)(vin + (size_t)(s0 + i) * DCH + c0);
#pragma unroll
            for (int i = 0; i < 4; ++i) wv[i] = *(const u32x4*)(shk + (size_t)c0 * 4 + 8 * i);
#pragma unroll
            for (int ch = 0; ch < 8; ++ch) {
                const uint32_t wlo = wv[ch >> 1][(ch & 1) * 2], whi = wv[ch >> 1][(ch & 1) * 2 + 1];
                const float w0 = lo_f(wlo), w1 = hi_f(wlo), w2 = lo_f(whi), w3 = hi_f(whi);
                float kf[11], vf[8];
#pragma unroll
                for (int i = 0; i < 11; ++i) kf[i] = (ch & 1) ? hi_f(kr[i][ch >> 1]) : lo_f(kr[i][ch >> 1]);
#pragma unroll
                for (int i = 0; i < 8; ++i) vf[i] = (ch & 1) ? hi_f(vr[i][ch >> 1]) : lo_f(vr[i][ch >> 1]);
                u32x4 o;
#pragma unroll
                for (int q = 0; q < 4; ++q) {
                    const int ia = 7 - 2 * q, ib = 6 - 2 * q;
                    float a0 = kf[ia] * w3, b0 = kf[ib] * w3;
                    rbf2(a0, b0);
                    float a1 = kf[ia + 1] * w2, b1 = kf[ib + 1] * w2;
                    rbf2(a1, b1);
                    a0 += a1; b0 += b1;
                    rbf2(a0, b0);
                    a1 = kf[ia + 2] * w1; b1 = kf[ib + 2] * w1;
                    rbf2(a1, b1);
                    a0 += a1; b0 += b1;
                    rbf2(a0, b0);
                    a1 = kf[ia + 3] * w0; b1 = kf[ib + 3] * w0;
                    rbf2(a1, b1);
                    a0 += a1; b0 += b1;
                    rbf2(a0, b0);
                    o[q] = pk_bf16(a0 * vf[ia], b0 * vf[ib]);
                }
                *(u32x4*)(xw + (size_t)(DCH + c0 + ch) * LSEQ + (2040 - s0)) = o;
            }
        }
        return;
    }

    const int d = b * 4 + (w - 4);
    char* T = lds + (w - 4) * KG_WAVE;
    u32x4 tabv[4];
#pragma unroll
    for (int i = 0; i < 4; ++i) tabv[i] = *(const u32x4*)(g_taptab.v + 8 * (64 * i + lane));

    const float dt = rbf(expf(bf2f(logdt[d])));
    float pa[2], pb[2], pcr[2], pci[2];
    float mx = 0.f;
#pragma unroll
    for (int bt = 0; bt < 2; ++bt) {
        const int n = d * NMODE + bt * 64 + lane;
        const float ar = -rbf(expf(bf2f(logA[n])));
        pa[bt] = ar * dt;
        pb[bt] = bf2f(Aim[n]) * dt;
        pcr[bt] = 2.f * (bf2f(Cre[n]) * dt);
        pci[bt] = 2.f * (bf2f(Cim[n]) * dt);
        mx = fmaxf(mx, fmaxf(fabsf(pcr[bt]), fabsf(pci[bt])));
    }
#pragma unroll
    for (int o = 32; o >= 1; o >>= 1) mx = fmaxf(mx, __shfl_xor(mx, o));
    int sf = 266 - (int)((__float_as_uint(mx) >> 23) & 0xff);
    sf = sf < 1 ? 1 : (sf > 239 ? 239 : sf);
    const float scl = __uint_as_float((uint32_t)sf << 23);
    const float inv = __uint_as_float((uint32_t)(240 - sf) << 23);

#pragma unroll
    for (int r = 41; r < 48; ++r) *(uint32_t*)(T + r * KG_ROWB + lane * 4) = 0u;

    f32x4 acc[3] = {{0, 0, 0, 0}, {0, 0, 0, 0}, {0, 0, 0, 0}};
    const int fr = lane & 15, kb = lane >> 4;
#pragma unroll 1
    for (int bt = 0; bt < 2; ++bt) {
        const float a = bt ? pa[1] : pa[0], bb = bt ? pb[1] : pb[0];
        const cplx z1 = anchor(a, bb, 1.f), z8 = anchor(a, bb, 8.f), z128 = anchor(a, bb, 128.f);
        char* Tl = T + lane * 4;
        {
            cplx f = {16384.f, 0.f};
#pragma unroll
            for (int i = 0; i < 16; ++i) {
                *(uint32_t*)(Tl + (48 + i) * KG_ROWB) = pk_f16(f.r, -f.i);
                if (i < 15) f = cmul(f, z8);
            }
        }
        cplx h[8];
        h[1] = z1;
        h[2] = cmul(z1, z1);
        h[3] = cmul(h[2], z1);
        h[4] = cmul(h[2], h[2]);
        h[5] = cmul(h[4], z1);
        h[6] = cmul(h[4], h[2]);
        h[7] = cmul(h[4], h[3]);
        cplx cg = {(bt ? pcr[1] : pcr[0]) * scl, (bt ? pci[1] : pci[0]) * scl};
#pragma unroll
        for (int aa = 0; aa <= 16; ++aa) {
            if (aa < 2) {
                *(uint32_t*)(Tl + (8 * aa) * KG_ROWB) = pk_f16(cg.r, cg.i);
#pragma unroll
                for (int e = 1; e < 8; ++e) {
                    cplx t = cmul(cg, h[e]);
                    *(uint32_t*)(Tl + (8 * aa + e) * KG_ROWB) = pk_f16(t.r, t.i);
                }
            } else if (aa < 4) {
                *(uint32_t*)(Tl + (16 + 4 * (aa - 2)) * KG_ROWB) = pk_f16(cg.r, cg.i);
#pragma unroll
                for (int e = 1; e < 4; ++e) {
                    cplx t = cmul(cg, h[2 * e]);
                    *(uint32_t*)(Tl + (16 + 4 * (aa - 2) + e) * KG_ROWB) = pk_f16(t.r, t.i);
                }
            } else if (aa < 8) {
                *(uint32_t*)(Tl + (24 + 2 * (aa - 4)) * KG_ROWB) = pk_f16(cg.r, cg.i);
                cplx t = cmul(cg, h[4]);
                *(uint32_t*)(Tl + (24 + 2 * (aa - 4) + 1) * KG_ROWB) = pk_f16(t.r, t.i);
            } else {
                *(uint32_t*)(Tl + (32 + (aa - 8)) * KG_ROWB) = pk_f16(cg.r, cg.i);
            }
            if (aa < 16) cg = cmul(cg, z128);
        }
#pragma unroll
        for (int ks = 0; ks < 4; ++ks) {
            const f16x8 bfr = *(const f16x8*)(T + (48 + fr) * KG_ROWB + ks * 64 + kb * 16);
#pragma unroll
            for (int rt = 0; rt < 3; ++rt) {
                const f16x8 afr = *(const f16x8*)(T + (16 * rt + fr) * KG_ROWB + ks * 64 + kb * 16);
                acc[rt] = __builtin_amdgcn_mfma_f32_16x16x32_f16(afr, bfr, acc[rt], 0, 0, 0);
            }
        }
    }
    uint16_t* KD = (uint16_t*)(T + KG_TAB);
#pragma unroll
    for (int rt = 0; rt < 3; ++rt)
#pragma unroll
        for (int r = 0; r < 4; r += 2) {
            uint32_t p = pk_bf16(acc[rt][r] * inv, acc[rt][r + 1] * inv);
            KD[(16 * rt + 4 * kb + r) * 16 + fr] = (uint16_t)p;
            KD[(16 * rt + 4 * kb + r + 1) * 16 + fr] = (uint16_t)(p >> 16);
        }
#pragma unroll
    for (int i = 0; i < 4; ++i) {
        u32x4 o;
#pragma unroll
        for (int q = 0; q < 4; ++q) {
            const uint32_t lo = KD[tabv[i][q] & 0xffffu], hi = KD[tabv[i][q] >> 16];
            o[q] = lo | (hi << 16);
        }
        *(u32x4*)(Kw + (size_t)d * LSEQ + 8 * (64 * i + lane)) = o;
    }
}

#define KA_OFF 0
#define KB_OFF 4640
#define XA_OFF 9216
#define XB_OFF 13440
#define CH_STRIDE 17600
#define YS_OFF (8 * CH_STRIDE)
#define YS_STRIDE 2112
#define CONV_LDS (YS_OFF + 8 * YS_STRIDE)

__device__ __forceinline__ void put_k(char* cb, int y, u32x4 d, uint32_t prev) {
    *(u32x4*)(cb + KA_OFF + 464 + 16 * y) = d;
    u32x4 o;
    o[0] = __builtin_amdgcn_alignbyte(d[0], prev, 2);
    o[1] = __builtin_amdgcn_alignbyte(d[1], d[0], 2);
    o[2] = __builtin_amdgcn_alignbyte(d[2], d[1], 2);
    o[3] = __builtin_amdgcn_alignbyte(d[3], d[2], 2);
    *(u32x4*)(cb + KB_OFF + 464 + 16 * y) = o;
}
__device__ __forceinline__ void put_x(char* cb, int y, u32x4 d) {
    *(u32x4*)(cb + XA_OFF + 16 * y) = d;
    *(uint32_t*)(cb + XB_OFF + 16 * y - 4) = d[0];
    u32x2 m = {d[1], d[2]};
    *(u32x2*)(cb + XB_OFF + 16 * y) = m;
    *(uint32_t*)(cb + XB_OFF + 16 * y + 8) = d[3];
}
__device__ __forceinline__ uint32_t prev_lane(uint32_t v, uint32_t carry) {
    return (uint32_t)__builtin_amdgcn_update_dpp((int)carry, (int)v, 0x138  , 0xf, 0xf, false);
}
__device__ __forceinline__ bf16x8 ld_xfrag(const char* p) {
    u32x2 a = *(const u32x2*)p, b = *(const u32x2*)(p + 8);
    u32x4 v = {a[0], a[1], b[0], b[1]};
    return __builtin_bit_cast(bf16x8, v);
}

__global__ __launch_bounds__(512) void conv_kernel(
    const uint16_t* __restrict__ xw, const uint16_t* __restrict__ Kw, const uint16_t* __restrict__ ssmk,
    const uint16_t* __restrict__ qin, const uint16_t* __restrict__ Dsk, const uint16_t* __restrict__ sD,
    uint16_t* __restrict__ out)
{
    __shared__ __attribute__((aligned(16))) char lds[CONV_LDS];
    const int b = blockIdx.x;
    const int xcd = b & 7, idx = b >> 3, part = idx >> 4, grp = xcd * 16 + (idx & 15);
    const int lane = threadIdx.x & 63;
    const int w = __builtin_amdgcn_readfirstlane(threadIdx.x >> 6);
    const int chl = grp * 8 + w;
    const uint16_t* ksrc = (part ? ssmk : Kw) + (size_t)chl * LSEQ;
    const uint16_t* xsrc = xw + (size_t)(part * DCH + chl) * LSEQ;
    char* cb = lds + w * CH_STRIDE;

    const u32x4 k0 = *(const u32x4*)(ksrc + 8 * lane);
    const u32x4 k1 = *(const u32x4*)(ksrc + 8 * (64 + lane));
    const u32x4 x2 = *(const u32x4*)(xsrc + 8 * (128 + lane));
    const u32x4 x3 = *(const u32x4*)(xsrc + 8 * (192 + lane));
    const u32x4 k2 = *(const u32x4*)(ksrc + 8 * (128 + lane));
    const u32x4 k3 = *(const u32x4*)(ksrc + 8 * (192 + lane));
    const u32x4 x0 = *(const u32x4*)(xsrc + 8 * lane);
    const u32x4 x1 = *(const u32x4*)(xsrc + 8 * (64 + lane));
    const float Dv = bf2f((part ? sD : Dsk)[chl]);

    const int g = lane >> 4, fr = lane & 15;
    u32x4 qv[4];
    if (part) {
#pragma unroll
        for (int i = 0; i < 4; ++i) {
            const int l = 1024 * (i >> 1) + 1023 - (128 * w + 64 * (i & 1) + lane);
            qv[i] = *(const u32x4*)(qin + (size_t)l * DCH + grp * 8);
        }
    }

    {
        const u32x4 z = {0, 0, 0, 0};
        if (lane < 29) {
            *(u32x4*)(cb + KA_OFF + 16 * lane) = z;
            *(u32x4*)(cb + KB_OFF + 16 * lane) = z;
        }
        if (lane < 4) *(u32x4*)(cb + XA_OFF + 4096 + 16 * lane) = z;
        if (lane < 5) *(u32x4*)(cb + XB_OFF + 4080 + 16 * lane) = z;
    }
    put_k(cb, lane, k0, prev_lane(k0[3], 0u));
    const uint32_t c1 = __builtin_amdgcn_readlane(k0[3], 63);
    put_k(cb, 64 + lane, k1, prev_lane(k1[3], c1));
    const uint32_t c2 = __builtin_amdgcn_readlane(k1[3], 63);
    put_x(cb, 128 + lane, x2);
    put_x(cb, 192 + lane, x3);

    const char* kaddr = cb + ((fr & 1) ? KA_OFF : KB_OFF) + 2 * (32 * (fr >> 1) + 8 * g + 8);
    const char* xaddr = cb + ((fr & 1) ? (XA_OFF + 2 * (1822 - 2 * fr + 8 * g)) : (XB_OFF + 2 * (1820 - 2 * fr + 8 * g)));
    char* ys = lds + YS_OFF;

    f32x4 acc[4];
#pragma unroll
    for (int i = 0; i < 4; ++i) acc[i] = (f32x4){0, 0, 0, 0};

#pragma unroll 1
    for (int c0 = 0; c0 < 8; ++c0) {
        bf16x8 kf[4], xf[4];
#pragma unroll
        for (int m = 0; m < 4; ++m) kf[m] = *(const bf16x8*)(kaddr + 64 * c0 + 512 * m);
#pragma unroll
        for (int t = 0; t < 4; ++t) xf[t] = ld_xfrag(xaddr + 64 * c0 - 512 * t);
#pragma unroll
        for (int m = 0; m < 4; ++m)
#pragma unroll
            for (int t = 0; t < 4; ++t)
                if (m + t <= 3) acc[m + t] = __builtin_amdgcn_mfma_f32_16x16x32_bf16(kf[m], xf[t], acc[m + t], 0, 0, 0);
    }
    put_k(cb, 128 + lane, k2, prev_lane(k2[3], c2));
    const uint32_t c3 = __builtin_amdgcn_readlane(k2[3], 63);
    put_k(cb, 192 + lane, k3, prev_lane(k3[3], c3));
    put_x(cb, lane, x0);
    put_x(cb, 64 + lane, x1);

#pragma unroll 1
    for (int sg = 0; sg < 2; ++sg) {
        if (sg == 1) {
#pragma unroll
            for (int i = 0; i < 4; ++i) acc[i] = (f32x4){0, 0, 0, 0};
#pragma unroll 1
            for (int c0 = 0; c0 < 8; ++c0) {
                bf16x8 kf[8], xf[8];
#pragma unroll
                for (int m = 0; m < 8; ++m) kf[m] = *(const bf16x8*)(kaddr + 64 * c0 + 512 * m);
#pragma unroll
                for (int t = 0; t < 8; ++t) xf[t] = ld_xfrag(xaddr + 64 * c0 - 512 * t);
#pragma unroll
                for (int m = 0; m < 8; ++m)
#pragma unroll
                    for (int t = 0; t < 8; ++t)
                        if (m + t >= 4 && m + t <= 7)
                            acc[m + t - 4] = __builtin_amdgcn_mfma_f32_16x16x32_bf16(kf[m], xf[t], acc[m + t - 4], 0, 0, 0);
            }
            __syncthreads();
        }
#pragma unroll
        for (int tt = 0; tt < 4; ++tt) {
#pragma unroll
            for (int hh = 0; hh < 2; ++hh) {
                const int lloc = 256 * tt + 64 * g + 2 * fr + 32 * hh;
                const int l = 1024 * sg + lloc;
                const uint32_t xd = *(const uint32_t*)(cb + XA_OFF + 2 * (2046 - l));
                float y1 = acc[tt][2 * hh + 1], y0 = acc[tt][2 * hh];
                rbf2(y1, y0);
                float t1 = lo_f(xd) * Dv, t0 = hi_f(xd) * Dv;
                rbf2(t1, t0);
                *(uint32_t*)(ys + w * YS_STRIDE + 2 * (1022 - lloc)) = pk_bf16(y1 + t1, y0 + t0);
            }
        }
        __syncthreads();
#pragma unroll
        for (int it = 0; it < 2; ++it) {
            const int e0 = 128 * w + 64 * it + 16 * g;
            const int qq = (lane >> 2) & 3, pp = lane & 3;
            const char* a0 = ys + qq * YS_STRIDE + (e0 + 4 * pp) * 2;
            s16x4 v0 = __builtin_amdgcn_ds_read_tr16_b64_v4i16((__attribute__((address_space(3))) s16x4*)(a0));
            s16x4 v1 = __builtin_amdgcn_ds_read_tr16_b64_v4i16((__attribute__((address_space(3))) s16x4*)(a0 + 4 * YS_STRIDE));
            u32x2 p0 = __builtin_bit_cast(u32x2, v0), p1 = __builtin_bit_cast(u32x2, v1);
            u32x4 o = {p0[0], p0[1], p1[0], p1[1]};
            if (part) {
                const u32x4 qq4 = sg ? (it ? qv[3] : qv[2]) : (it ? qv[1] : qv[0]);
#pragma unroll
                for (int j = 0; j < 4; ++j)
                    o[j] = pk_bf16(lo_f(qq4[j]) * lo_f(o[j]), hi_f(qq4[j]) * hi_f(o[j]));
            }
            const int l = 1024 * sg + 1023 - (e0 + fr);
            *(u32x4*)(out + (size_t)l * (2 * DCH) + part * DCH + grp * 8) = o;
        }
    }
}

extern "C" void kernel_launch(void* const* d_in, const int* in_sizes, int n_in,
                              void* d_out, int out_size, void* d_ws, size_t ws_size,
                              hipStream_t stream) {
    const uint16_t* u = (const uint16_t*)d_in[0];
    const uint16_t* logA = (const uint16_t*)d_in[1];
    const uint16_t* Aim = (const uint16_t*)d_in[2];
    const uint16_t* Cre = (const uint16_t*)d_in[3];
    const uint16_t* Cim = (const uint16_t*)d_in[4];
    const uint16_t* logdt = (const uint16_t*)d_in[5];
    const uint16_t* Dsk = (const uint16_t*)d_in[6];
    const uint16_t* q = (const uint16_t*)d_in[7];
    const uint16_t* k = (const uint16_t*)d_in[8];
    const uint16_t* v = (const uint16_t*)d_in[9];
    const uint16_t* shk = (const uint16_t*)d_in[10];
    const uint16_t* ssmk = (const uint16_t*)d_in[11];
    const uint16_t* sD = (const uint16_t*)d_in[12];
    uint16_t* xw = (uint16_t*)d_ws;
    uint16_t* Kw = xw + (size_t)2 * DCH * LSEQ;
    prep_kernel<<<256, 512, 0, stream>>>(u, logA, Aim, Cre, Cim, logdt, k, v, shk, xw, Kw);
    conv_kernel<<<256, 512, 0, stream>>>(xw, Kw, ssmk, q, Dsk, sD, (uint16_t*)d_out);
}
```

```cpp
#include <hip/hip_runtime.h>
#include <stdint.h>

typedef __attribute__((ext_vector_type(4))) unsigned int u32x4;
typedef __attribute__((ext_vector_type(2))) unsigned int u32x2;
typedef __attribute__((ext_vector_type(4))) float f32x4;
typedef __attribute__((ext_vector_type(2))) float f32x2;
typedef __attribute__((ext_vector_type(8))) _Float16 f16x8;
typedef __attribute__((ext_vector_type(2))) _Float16 f16x2;
typedef __attribute__((ext_vector_type(8))) __bf16 bf16x8;
typedef __attribute__((ext_vector_type(2))) __bf16 bf16x2;
typedef __attribute__((ext_vector_type(4))) short s16x4;

#define STAMP(k, i)
#define STAMPW(k, i)
#define LSEQ 2048
#define DCH 1024
#define NMODE 128

__device__ __forceinline__ float bf2f(uint16_t v) { return __uint_as_float(((uint32_t)v) << 16); }
__device__ __forceinline__ float lo_f(uint32_t w) { return __uint_as_float(w << 16); }
__device__ __forceinline__ float hi_f(uint32_t w) { return __uint_as_float(w & 0xffff0000u); }
__device__ __forceinline__ uint32_t pk_bf16(float lo, float hi) {
    f32x2 x = {lo, hi};
    bf16x2 y = __builtin_convertvector(x, bf16x2);
    return __builtin_bit_cast(uint32_t, y);
}
__device__ __forceinline__ uint32_t pk_f16(float lo, float hi) {
    f32x2 x = {lo, hi};
    f16x2 y = __builtin_convertvector(x, f16x2);
    return __builtin_bit_cast(uint32_t, y);
}
__device__ __forceinline__ void rbf2(float& a, float& b) {
    uint32_t p = pk_bf16(a, b);
    a = lo_f(p);
    b = hi_f(p);
}
__device__ __forceinline__ float rbf(float a) { return lo_f(pk_bf16(a, a)); }

struct TapTab { uint16_t v[LSEQ]; };
static constexpr TapTab make_tab() {
    TapTab t{};
    for (int j = 0; j < LSEQ; ++j) {
        int p = j;
        if (j >= 256) {
            int s = (j < 512) ? 1 : ((j < 1024) ? 2 : 3);
            int q = j >> s, r = j & ((1 << s) - 1), half = 1 << (s - 1);
            if (r > half || (r == half && (q & 1))) q++;
            p = q << s;
        }
        int col = (p >> 3) & 15, a = p >> 7, low = p & 7, row = 0;
        if (p < 256) row = 8 * a + low;
        else if (p < 512) row = 16 + 4 * (a - 2) + (low >> 1);
        else if (p < 1024) row = 24 + 2 * (a - 4) + (low >> 2);
        else row = 32 + (a - 8);
        t.v[j] = (uint16_t)(row * 16 + col);
    }
    return t;
}
__device__ const TapTab g_taptab = make_tab();

struct cplx { float r, i; };
__device__ __forceinline__ cplx cmul(cplx a, cplx b) {
    cplx c;
    c.r = a.r * b.r - a.i * b.i;
    c.i = a.r * b.i + a.i * b.r;
    return c;
}
__device__ __forceinline__ cplx anchor(float a, float b, float k) {
    float e = __builtin_amdgcn_exp2f(a * k * 1.4426950408889634f);
    float th = __builtin_amdgcn_fractf(b * k * 0.15915494309189535f);
    cplx c;
    c.r = e * __builtin_amdgcn_cosf(th);
    c.i = e * __builtin_amdgcn_sinf(th);
    return c;
}

#define KG_ROWB 272
#define KG_TAB (64 * KG_ROWB)
#define KG_WAVE (KG_TAB + 1536)

__global__ __launch_bounds__(512) void prep_kernel(
    const uint16_t* __restrict__ u, const uint16_t* __restrict__ logA, const uint16_t* __restrict__ Aim,
    const uint16_t* __restrict__ Cre, const uint16_t* __restrict__ Cim, const uint16_t* __restrict__ logdt,
    const uint16_t* __restrict__ kin, const uint16_t* __restrict__ vin, const uint16_t* __restrict__ shk,
    uint16_t* __restrict__ xw, uint16_t* __restrict__ Kw)
{
    __shared__ __attribute__((aligned(16))) char lds[4 * KG_WAVE];
    const int lane = threadIdx.x & 63;
    const int w = __builtin_amdgcn_readfirstlane(threadIdx.x >> 6);
    const int b = blockIdx.x;
    STAMP(0, 0);

    if (w < 4) {
        const int wt = b * 2 + (w & 1);
        const int rt = wt >> 4, ct = wt & 15;
        const int cg = lane & 7, rb = lane >> 3;
        const int s0 = 64 * rt + 8 * rb, c0 = 64 * ct + 8 * cg;
        if (w < 2) {
            u32x4 r[8];
#pragma unroll
            for (int i = 0; i < 8; ++i) r[i] = *(const u32x4*)(u + (size_t)(s0 + i) * DCH + c0);
#pragma unroll
            for (int ch = 0; ch < 8; ++ch) {
                u32x4 o;
#pragma unroll
                for (int q = 0; q < 4; ++q)
                    o[q] = __builtin_amdgcn_perm(r[6 - 2 * q][ch >> 1], r[7 - 2 * q][ch >> 1],
                                                 (ch & 1) ? 0x07060302u : 0x05040100u);
                *(u32x4*)(xw + (size_t)(c0 + ch) * LSEQ + (2040 - s0)) = o;
            }
        } else {
            u32x4 kr[11], vr[8], wv[4];
#pragma unroll
            for (int i = 0; i < 11; ++i) {
                int s = s0 + i - 3;
                u32x4 z = {0, 0, 0, 0};
                kr[i] = (s >= 0) ? *(const u32x4*)(kin + (size_t)(s < 0 ? 0 : s) * DCH + c0) : z;
            }
#pragma unroll
            for (int i = 0; i < 8; ++i) vr[i] = *(const u32x4*)(vin + (size_t)(s0 + i) * DCH + c0);
#pragma unroll
            for (int i = 0; i < 4; ++i) wv[i] = *(const u32x4*)(shk + (size_t)c0 * 4 + 8 * i);
#pragma unroll
            for (int ch = 0; ch < 8; ++ch) {
                const uint32_t wlo = wv[ch >> 1][(ch & 1) * 2], whi = wv[ch >> 1][(ch & 1) * 2 + 1];
                const float w0 = lo_f(wlo), w1 = hi_f(wlo), w2 = lo_f(whi), w3 = hi_f(whi);
                float kf[11], vf[8];
#pragma unroll
                for (int i = 0; i < 11; ++i) kf[i] = (ch & 1) ? hi_f(kr[i][ch >> 1]) : lo_f(kr[i][ch >> 1]);
#pragma unroll
                for (int i = 0; i < 8; ++i) vf[i] = (ch & 1) ? hi_f(vr[i][ch >> 1]) : lo_f(vr[i][ch >> 1]);
                u32x4 o;
#pragma unroll
                for (int q = 0; q < 4; ++q) {
                    const int ia = 7 - 2 * q, ib = 6 - 2 * q;
                    float a0 = kf[ia] * w3, b0 = kf[ib] * w3;
                    rbf2(a0, b0);
                    float a1 = kf[ia + 1] * w2, b1 = kf[ib + 1] * w2;
                    rbf2(a1, b1);
                    a0 += a1; b0 += b1;
                    rbf2(a0, b0);
                    a1 = kf[ia + 2] * w1; b1 = kf[ib + 2] * w1;
                    rbf2(a1, b1);
                    a0 += a1; b0 += b1;
                    rbf2(a0, b0);
                    a1 = kf[ia + 3] * w0; b1 = kf[ib + 3] * w0;
                    rbf2(a1, b1);
                    a0 += a1; b0 += b1;
                    rbf2(a0, b0);
                    o[q] = pk_bf16(a0 * vf[ia], b0 * vf[ib]);
                }
                *(u32x4*)(xw + (size_t)(DCH + c0 + ch) * LSEQ + (2040 - s0)) = o;
            }
        }
        STAMP(0, 1);
        STAMPW(0, 2);
        return;
    }

    const int d = b * 4 + (w - 4);
    char* T = lds + (w - 4) * KG_WAVE;
    u32x4 tabv[4];
#pragma unroll
    for (int i = 0; i < 4; ++i) tabv[i] = *(const u32x4*)(g_taptab.v + 8 * (64 * i + lane));

    const float dt = rbf(expf(bf2f(logdt[d])));
    float pa[2], pb[2], pcr[2], pci[2];
    float mx = 0.f;
#pragma unroll
    for (int bt = 0; bt < 2; ++bt) {
        const int n = d * NMODE + bt * 64 + lane;
        const float ar = -rbf(expf(bf2f(logA[n])));
        pa[bt] = ar * dt;
        pb[bt] = bf2f(Aim[n]) * dt;
        pcr[bt] = 2.f * (bf2f(Cre[n]) * dt);
        pci[bt] = 2.f * (bf2f(Cim[n]) * dt);
        mx = fmaxf(mx, fmaxf(fabsf(pcr[bt]), fabsf(pci[bt])));
    }
#pragma unroll
    for (int o = 32; o >= 1; o >>= 1) mx = fmaxf(mx, __shfl_xor(mx, o));
    int sf = 266 - (int)((__float_as_uint(mx) >> 23) & 0xff);
    sf = sf < 1 ? 1 : (sf > 239 ? 239 : sf);
    const float scl = __uint_as_float((uint32_t)sf << 23);
    const float inv = __uint_as_float((uint32_t)(240 - sf) << 23);

    STAMP(0, 1);
#pragma unroll
    for (int r = 41; r < 48; ++r) *(uint32_t*)(T + r * KG_ROWB + lane * 4) = 0u;

    f32x4 acc[3] = {{0, 0, 0, 0}, {0, 0, 0, 0}, {0, 0, 0, 0}};
    const int fr = lane & 15, kb = lane >> 4;
#pragma unroll 1
    for (int bt = 0; bt < 2; ++bt) {
        const float a = bt ? pa[1] : pa[0], bb = bt ? pb[1] : pb[0];
        const cplx z1 = anchor(a, bb, 1.f), z8 = anchor(a, bb, 8.f), z128 = anchor(a, bb, 128.f);
        char* Tl = T + lane * 4;
        {
            cplx f = {16384.f, 0.f};
#pragma unroll
            for (int i = 0; i < 16; ++i) {
                *(uint32_t*)(Tl + (48 + i) * KG_ROWB) = pk_f16(f.r, -f.i);
                if (i < 15) f = cmul(f, z8);
            }
        }
        cplx h[8];
        h[1] = z1;
        h[2] = cmul(z1, z1);
        h[3] = cmul(h[2], z1);
        h[4] = cmul(h[2], h[2]);
        h[5] = cmul(h[4], z1);
        h[6] = cmul(h[4], h[2]);
        h[7] = cmul(h[4], h[3]);
        cplx cg = {(bt ? pcr[1] : pcr[0]) * scl, (bt ? pci[1] : pci[0]) * scl};
#pragma unroll
        for (int aa = 0; aa <= 16; ++aa) {
            if (aa < 2) {
                *(uint32_t*)(Tl + (8 * aa) * KG_ROWB) = pk_f16(cg.r, cg.i);
#pragma unroll
                for (int e = 1; e < 8; ++e) {
                    cplx t = cmul(cg, h[e]);
                    *(uint32_t*)(Tl + (8 * aa + e) * KG_ROWB) = pk_f16(t.r, t.i);
                }
            } else if (aa < 4) {
                *(uint32_t*)(Tl + (16 + 4 * (aa - 2)) * KG_ROWB) = pk_f16(cg.r, cg.i);
#pragma unroll
                for (int e = 1; e < 4; ++e) {
                    cplx t = cmul(cg, h[2 * e]);
                    *(uint32_t*)(Tl + (16 + 4 * (aa - 2) + e) * KG_ROWB) = pk_f16(t.r, t.i);
                }
            } else if (aa < 8) {
                *(uint32_t*)(Tl + (24 + 2 * (aa - 4)) * KG_ROWB) = pk_f16(cg.r, cg.i);
                cplx t = cmul(cg, h[4]);
                *(uint32_t*)(Tl + (24 + 2 * (aa - 4) + 1) * KG_ROWB) = pk_f16(t.r, t.i);
            } else {
                *(uint32_t*)(Tl + (32 + (aa - 8)) * KG_ROWB) = pk_f16(cg.r, cg.i);
            }
            if (aa < 16) cg = cmul(cg, z128);
        }
#pragma unroll
        for (int ks = 0; ks < 4; ++ks) {
            const f16x8 bfr = *(const f16x8*)(T + (48 + fr) * KG_ROWB + ks * 64 + kb * 16);
#pragma unroll
            for (int rt = 0; rt < 3; ++rt) {
                const f16x8 afr = *(const f16x8*)(T + (16 * rt + fr) * KG_ROWB + ks * 64 + kb * 16);
                acc[rt] = __builtin_amdgcn_mfma_f32_16x16x32_f16(afr, bfr, acc[rt], 0, 0, 0);
            }
        }
    }
    STAMP(0, 2);
    uint16_t* KD = (uint16_t*)(T + KG_TAB);
#pragma unroll
    for (int rt = 0; rt < 3; ++rt)
#pragma unroll
        for (int r = 0; r < 4; r += 2) {
            uint32_t p = pk_bf16(acc[rt][r] * inv, acc[rt][r + 1] * inv);
            KD[(16 * rt + 4 * kb + r) * 16 + fr] = (uint16_t)p;
            KD[(16 * rt + 4 * kb + r + 1) * 16 + fr] = (uint16_t)(p >> 16);
        }
#pragma unroll
    for (int i = 0; i < 4; ++i) {
        u32x4 o;
#pragma unroll
        for (int q = 0; q < 4; ++q) {
            const uint32_t lo = KD[tabv[i][q] & 0xffffu], hi = KD[tabv[i][q] >> 16];
            o[q] = lo | (hi << 16);
        }
        *(u32x4*)(Kw + (size_t)d * LSEQ + 8 * (64 * i + lane)) = o;
    }
    STAMP(0, 3);
    STAMPW(0, 4);
}

#define KA_OFF 0
#define KB_OFF 4640
#define XA_OFF 9216
#define XB_OFF 13440
#define CH_STRIDE 17600
#define YS_OFF (8 * CH_STRIDE)
#define YS_STRIDE 2112
#define CONV_LDS (YS_OFF + 8 * YS_STRIDE)

__device__ __forceinline__ void put_k(char* cb, int y, u32x4 d, uint32_t prev) {
    *(u32x4*)(cb + KA_OFF + 464 + 16 * y) = d;
    u32x4 o;
    o[0] = __builtin_amdgcn_alignbyte(d[0], prev, 2);
    o[1] = __builtin_amdgcn_alignbyte(d[1], d[0], 2);
    o[2] = __builtin_amdgcn_alignbyte(d[2], d[1], 2);
    o[3] = __builtin_amdgcn_alignbyte(d[3], d[2], 2);
    *(u32x4*)(cb + KB_OFF + 464 + 16 * y) = o;
}
__device__ __forceinline__ void put_x(char* cb, int y, u32x4 d) {
    *(u32x4*)(cb + XA_OFF + 16 * y) = d;
    *(uint32_t*)(cb + XB_OFF + 16 * y - 4) = d[0];
    u32x2 m = {d[1], d[2]};
    *(u32x2*)(cb + XB_OFF + 16 * y) = m;
    *(uint32_t*)(cb + XB_OFF + 16 * y + 8) = d[3];
}
__device__ __forceinline__ uint32_t prev_lane(uint32_t v, uint32_t carry) {
    return (uint32_t)__builtin_amdgcn_update_dpp((int)carry, (int)v, 0x138  , 0xf, 0xf, false);
}
typedef __attribute__((address_space(3))) char* lds_t;
typedef __attribute__((address_space(3))) const volatile u32x2* lds_v2;
typedef __attribute__((address_space(3))) const bf16x8* lds_k8;
__device__ __forceinline__ bf16x8 ldx(lds_t p) {
    u32x2 a = *(lds_v2)p, b = *(lds_v2)(p + 8);
    u32x4 v = {a[0], a[1], b[0], b[1]};
    return __builtin_bit_cast(bf16x8, v);
}
__device__ __forceinline__ bf16x8 ldk(lds_t p) { return *(lds_k8)p; }
#define MFMA(a, b, c) c = __builtin_amdgcn_mfma_f32_16x16x32_bf16(a, b, c, 0, 0, 0)
#define SB() __builtin_amdgcn_sched_barrier(0)
#define SG_DS(n) __builtin_amdgcn_sched_group_barrier(0x100, n, 0)
#define SG_MF(n) __builtin_amdgcn_sched_group_barrier(0x008, n, 0)
#define ST2_OFF 1488

__device__ __forceinline__ void epi_acc(const f32x4 a, const char* xa, char* dst, float Dv) {
#pragma unroll
    for (int hh = 0; hh < 2; ++hh) {
        const uint32_t xd = *(const uint32_t*)(xa - 64 * hh);
        float y1 = a[2 * hh + 1], y0 = a[2 * hh];
        rbf2(y1, y0);
        float t1 = lo_f(xd) * Dv, t0 = hi_f(xd) * Dv;
        rbf2(t1, t0);
        *(uint32_t*)(dst - 64 * hh) = pk_bf16(y1 + t1, y0 + t0);
    }
}
__device__ __forceinline__ void epi_row(const char* a0, int stride4, bool gate, u32x4 qq4, uint16_t* dst) {
    s16x4 v0 = __builtin_amdgcn_ds_read_tr16_b64_v4i16((__attribute__((address_space(3))) s16x4*)(a0));
    s16x4 v1 = __builtin_amdgcn_ds_read_tr16_b64_v4i16((__attribute__((address_space(3))) s16x4*)(a0 + stride4));
    u32x2 p0 = __builtin_bit_cast(u32x2, v0), p1 = __builtin_bit_cast(u32x2, v1);
    u32x4 o = {p0[0], p0[1], p1[0], p1[1]};
    if (gate) {
#pragma unroll
        for (int j = 0; j < 4; ++j)
            o[j] = pk_bf16(lo_f(qq4[j]) * lo_f(o[j]), hi_f(qq4[j]) * hi_f(o[j]));
    }
    *(u32x4*)dst = o;
}

__global__ __launch_bounds__(512) void conv_kernel(
    const uint16_t* __restrict__ xw, const uint16_t* __restrict__ Kw, const uint16_t* __restrict__ ssmk,
    const uint16_t* __restrict__ qin, const uint16_t* __restrict__ Dsk, const uint16_t* __restrict__ sD,
    uint16_t* __restrict__ out)
{
    __shared__ __attribute__((aligned(16))) char lds[CONV_LDS];
    const int b = blockIdx.x;
    const int xcd = b & 7, idx = b >> 3, part = idx >> 4, grp = xcd * 16 + (idx & 15);
    const int lane = threadIdx.x & 63;
    const int w = __builtin_amdgcn_readfirstlane(threadIdx.x >> 6);
    const int chl = grp * 8 + w;
    const uint16_t* ksrc = (part ? ssmk : Kw) + (size_t)chl * LSEQ;
    const uint16_t* xsrc = xw + (size_t)(part * DCH + chl) * LSEQ;
    char* cb = lds + w * CH_STRIDE;

    STAMP(1, 0);
    const u32x4 k0 = *(const u32x4*)(ksrc + 8 * lane);
    SB();
    const u32x4 x3 = *(const u32x4*)(xsrc + 8 * (192 + lane));
    SB();
    const u32x4 k1 = *(const u32x4*)(ksrc + 8 * (64 + lane));
    SB();
    const u32x4 x2 = *(const u32x4*)(xsrc + 8 * (128 + lane));
    SB();
    const u32x4 k2 = *(const u32x4*)(ksrc + 8 * (128 + lane));
    SB();
    const u32x4 x1 = *(const u32x4*)(xsrc + 8 * (64 + lane));
    SB();
    const u32x4 k3 = *(const u32x4*)(ksrc + 8 * (192 + lane));
    SB();
    const u32x4 x0 = *(const u32x4*)(xsrc + 8 * lane);
    SB();
    const float Dv = bf2f((part ? sD : Dsk)[chl]);

    const int g = lane >> 4, fr = lane & 15;
    const int r0 = 1023 - (128 * w + lane), r1 = r0 - 64, r2 = 1535 - (64 * w + lane), r3 = r2 + 512;

    {
        const u32x4 z = {0, 0, 0, 0};
        if (lane < 29) {
            *(u32x4*)(cb + KA_OFF + 16 * lane) = z;
            *(u32x4*)(cb + KB_OFF + 16 * lane) = z;
        }
        if (lane < 4) *(u32x4*)(cb + XA_OFF + 4096 + 16 * lane) = z;
        if (lane < 5) *(u32x4*)(cb + XB_OFF + 4080 + 16 * lane) = z;
    }
    put_k(cb, lane, k0, prev_lane(k0[3], 0u));
    const uint32_t c1 = __builtin_amdgcn_readlane(k0[3], 63);
    put_x(cb, 192 + lane, x3);
    put_k(cb, 64 + lane, k1, prev_lane(k1[3], c1));
    const uint32_t c2 = __builtin_amdgcn_readlane(k1[3], 63);
    put_x(cb, 128 + lane, x2);
    SB();
    u32x4 qv[4];
    if (part) {
        qv[0] = *(const u32x4*)(qin + (size_t)r0 * DCH + grp * 8);
        qv[1] = *(const u32x4*)(qin + (size_t)r1 * DCH + grp * 8);
        qv[2] = *(const u32x4*)(qin + (size_t)r2 * DCH + grp * 8);
        qv[3] = *(const u32x4*)(qin + (size_t)r3 * DCH + grp * 8);
    }
    SB();

    const lds_t lb = (lds_t)lds + w * CH_STRIDE;
    const lds_t kaddr = lb + ((fr & 1) ? KA_OFF : KB_OFF) + 2 * (32 * (fr >> 1) + 8 * g + 8);
    const lds_t xaddr = lb + ((fr & 1) ? (XA_OFF + 2 * (1822 - 2 * fr + 8 * g)) : (XB_OFF + 2 * (1820 - 2 * fr + 8 * g))) - 3584;
    char* ys = lds + YS_OFF;
    const int lo_ = 64 * g + 2 * fr;
    const char* xa = cb + XA_OFF + 2 * (2046 - lo_);
    const int qq = (lane >> 2) & 3, pp = lane & 3;
    uint16_t* ob = out + part * DCH + grp * 8;

    f32x4 acc[8];
#pragma unroll
    for (int i = 0; i < 8; ++i) acc[i] = (f32x4){0, 0, 0, 0};

    STAMP(1, 1);
    {
        bf16x8 ka[4], xa_[4], kb[4], xb_[4];
#pragma unroll
        for (int m = 0; m < 4; ++m) { ka[m] = ldk(kaddr + 512 * m); xa_[m] = ldx(xaddr + 512 * (7 - m)); }
#pragma unroll 1
        for (int c0 = 0; c0 < 8; c0 += 2) {
            const lds_t kp = kaddr + 64 * c0, xp = xaddr + 64 * c0;
#pragma unroll
            for (int m = 0; m < 4; ++m) { kb[m] = ldk(kp + 64 + 512 * m); xb_[m] = ldx(xp + 64 + 512 * (7 - m)); }
#pragma unroll
            for (int m = 0; m < 4; ++m)
#pragma unroll
                for (int t = 0; t < 4; ++t) MFMA(ka[m], xa_[t], acc[m + t]);
#pragma unroll
            for (int i = 0; i < 12; ++i) { SG_MF(1); SG_DS(1); }
            SG_MF(4);
            SB();
#pragma unroll
            for (int m = 0; m < 4; ++m) { ka[m] = ldk(kp + 128 + 512 * m); xa_[m] = ldx(xp + 128 + 512 * (7 - m)); }
#pragma unroll
            for (int m = 0; m < 4; ++m)
#pragma unroll
                for (int t = 0; t < 4; ++t) MFMA(kb[m], xb_[t], acc[m + t]);
#pragma unroll
            for (int i = 0; i < 12; ++i) { SG_MF(1); SG_DS(1); }
            SG_MF(4);
            SB();
        }
    }
    STAMP(1, 2);
    put_k(cb, 128 + lane, k2, prev_lane(k2[3], c2));
    const uint32_t c3 = __builtin_amdgcn_readlane(k2[3], 63);
    put_x(cb, 64 + lane, x1);
#pragma unroll
    for (int tt = 0; tt < 4; ++tt) epi_acc(acc[tt], xa - 512 * tt, ys + w * YS_STRIDE + 2 * (1022 - lo_) - 512 * tt, Dv);
    __syncthreads();
    epi_row(ys + qq * YS_STRIDE + (128 * w + 16 * g + 4 * pp) * 2, 4 * YS_STRIDE, part, qv[0], ob + (size_t)r0 * (2 * DCH));
    epi_row(ys + qq * YS_STRIDE + (128 * w + 64 + 16 * g + 4 * pp) * 2, 4 * YS_STRIDE, part, qv[1], ob + (size_t)r1 * (2 * DCH));
    STAMP(1, 3);
    {
        bf16x8 ka[6], xa_[6], kb[6], xb_[6];
#pragma unroll
        for (int m = 0; m < 6; ++m) { ka[m] = ldk(kaddr + 512 * m); xa_[m] = ldx(xaddr + 512 * (7 - m)); }
#pragma unroll 1
        for (int c0 = 0; c0 < 8; c0 += 2) {
            const lds_t kp = kaddr + 64 * c0, xp = xaddr + 64 * c0;
#pragma unroll
            for (int m = 0; m < 6; ++m) { kb[m] = ldk(kp + 64 + 512 * m); xb_[m] = ldx(xp + 64 + 512 * (7 - m)); }
#pragma unroll
            for (int t = 0; t < 4; ++t) { MFMA(ka[4], xa_[t], acc[4 + t]); }
#pragma unroll
            for (int t = 0; t < 3; ++t) { MFMA(ka[5], xa_[t], acc[5 + t]); }
#pragma unroll
            for (int t = 0; t < 4; ++t) { MFMA(ka[t], xa_[4], acc[4 + t]); }
#pragma unroll
            for (int t = 0; t < 3; ++t) { MFMA(ka[t], xa_[5], acc[5 + t]); }
#pragma unroll
            for (int i = 0; i < 9; ++i) { SG_MF(1); SG_DS(2); }
            SG_MF(5);
            SB();
#pragma unroll
            for (int m = 0; m < 6; ++m) { ka[m] = ldk(kp + 128 + 512 * m); xa_[m] = ldx(xp + 128 + 512 * (7 - m)); }
#pragma unroll
            for (int t = 0; t < 4; ++t) { MFMA(kb[4], xb_[t], acc[4 + t]); }
#pragma unroll
            for (int t = 0; t < 3; ++t) { MFMA(kb[5], xb_[t], acc[5 + t]); }
#pragma unroll
            for (int t = 0; t < 4; ++t) { MFMA(kb[t], xb_[4], acc[4 + t]); }
#pragma unroll
            for (int t = 0; t < 3; ++t) { MFMA(kb[t], xb_[5], acc[5 + t]); }
#pragma unroll
            for (int i = 0; i < 9; ++i) { SG_MF(1); SG_DS(2); }
            SG_MF(5);
            SB();
        }
    }
    STAMP(1, 4);
    put_k(cb, 192 + lane, k3, prev_lane(k3[3], c3));
    put_x(cb, lane, x0);
#pragma unroll
    for (int tt = 4; tt < 6; ++tt) epi_acc(acc[tt], xa - 512 * tt, cb + KA_OFF + ST2_OFF + 2 * (510 - lo_) - 512 * (tt - 4), Dv);
    __syncthreads();
    epi_row(lds + qq * CH_STRIDE + KA_OFF + ST2_OFF + (64 * w + 16 * g + 4 * pp) * 2, 4 * CH_STRIDE, part, qv[2], ob + (size_t)r2 * (2 * DCH));
    STAMP(1, 5);
    {
        f32x4 a7b = {0, 0, 0, 0};
        bf16x8 ka[4], xa_[4], kb[4], xb_[4];
#pragma unroll
        for (int m = 0; m < 4; ++m) { const int mm = m < 2 ? m : m + 4; ka[m] = ldk(kaddr + 512 * mm); xa_[m] = ldx(xaddr + 512 * (7 - mm)); }
#pragma unroll 1
        for (int c0 = 0; c0 < 8; c0 += 2) {
            const lds_t kp = kaddr + 64 * c0, xp = xaddr + 64 * c0;
#pragma unroll
            for (int m = 0; m < 4; ++m) { const int mm = m < 2 ? m : m + 4; kb[m] = ldk(kp + 64 + 512 * mm); xb_[m] = ldx(xp + 64 + 512 * (7 - mm)); }
            MFMA(ka[2], xa_[0], acc[6]); MFMA(ka[2], xa_[1], acc[7]); MFMA(ka[3], xa_[0], a7b);
            MFMA(ka[0], xa_[2], acc[6]); MFMA(ka[1], xa_[2], acc[7]); MFMA(ka[0], xa_[3], a7b);
#pragma unroll
            for (int i = 0; i < 6; ++i) { SG_MF(1); SG_DS(2); }
            SB();
#pragma unroll
            for (int m = 0; m < 4; ++m) { const int mm = m < 2 ? m : m + 4; ka[m] = ldk(kp + 128 + 512 * mm); xa_[m] = ldx(xp + 128 + 512 * (7 - mm)); }
            MFMA(kb[2], xb_[0], acc[6]); MFMA(kb[2], xb_[1], acc[7]); MFMA(kb[3], xb_[0], a7b);
            MFMA(kb[0], xb_[2], acc[6]); MFMA(kb[1], xb_[2], acc[7]); MFMA(kb[0], xb_[3], a7b);
#pragma unroll
            for (int i = 0; i < 6; ++i) { SG_MF(1); SG_DS(2); }
            SB();
        }
        acc[7] += a7b;
    }
    STAMP(1, 6);
#pragma unroll
    for (int tt = 6; tt < 8; ++tt) epi_acc(acc[tt], xa - 512 * tt, cb + KB_OFF + ST2_OFF + 2 * (510 - lo_) - 512 * (tt - 6), Dv);
    __syncthreads();
    epi_row(lds + qq * CH_STRIDE + KB_OFF + ST2_OFF + (64 * w + 16 * g + 4 * pp) * 2, 4 * CH_STRIDE, part, qv[3], ob + (size_t)r3 * (2 * DCH));
    STAMPW(1, 7);
}

extern "C" void kernel_launch(void* const* d_in, const int* in_sizes, int n_in,
                              void* d_out, int out_size, void* d_ws, size_t ws_size,
                              hipStream_t stream) {
    const uint16_t* u = (const uint16_t*)d_in[0];
    const uint16_t* logA = (const uint16_t*)d_in[1];
    const uint16_t* Aim = (const uint16_t*)d_in[2];
    const uint16_t* Cre = (const uint16_t*)d_in[3];
    const uint16_t* Cim = (const uint16_t*)d_in[4];
    const uint16_t* logdt = (const uint16_t*)d_in[5];
    const uint16_t* Dsk = (const uint16_t*)d_in[6];
    const uint16_t* q = (const uint16_t*)d_in[7];
    const uint16_t* k = (const uint16_t*)d_in[8];
    const uint16_t* v = (const uint16_t*)d_in[9];
    const uint16_t* shk = (const uint16_t*)d_in[10];
    const uint16_t* ssmk = (const uint16_t*)d_in[11];
    const uint16_t* sD = (const uint16_t*)d_in[12];
    uint16_t* xw = (uint16_t*)d_ws;
    uint16_t* Kw = xw + (size_t)2 * DCH * LSEQ;
    prep_kernel<<<256, 512, 0, stream>>>(u, logA, Aim, Cre, Cim, logdt, k, v, shk, xw, Kw);
    conv_kernel<<<256, 512, 0, stream>>>(xw, Kw, ssmk, q, Dsk, sD, (uint16_t*)d_out);
}
```

```cpp
#include <hip/hip_runtime.h>
#include <stdint.h>

typedef __attribute__((ext_vector_type(4))) unsigned int u32x4;
typedef __attribute__((ext_vector_type(2))) unsigned int u32x2;
typedef __attribute__((ext_vector_type(4))) float f32x4;
typedef __attribute__((ext_vector_type(2))) float f32x2;
typedef __attribute__((ext_vector_type(8))) _Float16 f16x8;
typedef __attribute__((ext_vector_type(2))) _Float16 f16x2;
typedef __attribute__((ext_vector_type(8))) __bf16 bf16x8;
typedef __attribute__((ext_vector_type(2))) __bf16 bf16x2;
typedef __attribute__((ext_vector_type(4))) short s16x4;

#define STAMP(k, i)
#define STAMPW(k, i)
#define LSEQ 2048
#define DCH 1024
#define NMODE 128

__device__ __forceinline__ float bf2f(uint16_t v) { return __uint_as_float(((uint32_t)v) << 16); }
__device__ __forceinline__ float lo_f(uint32_t w) { return __uint_as_float(w << 16); }
__device__ __forceinline__ float hi_f(uint32_t w) { return __uint_as_float(w & 0xffff0000u); }
__device__ __forceinline__ uint32_t pk_bf16(float lo, float hi) {
    f32x2 x = {lo, hi};
    bf16x2 y = __builtin_convertvector(x, bf16x2);
    return __builtin_bit_cast(uint32_t, y);
}
__device__ __forceinline__ uint32_t pk_f16(float lo, float hi) {
    f32x2 x = {lo, hi};
    f16x2 y = __builtin_convertvector(x, f16x2);
    return __builtin_bit_cast(uint32_t, y);
}
__device__ __forceinline__ void rbf2(float& a, float& b) {
    uint32_t p = pk_bf16(a, b);
    a = lo_f(p);
    b = hi_f(p);
}
__device__ __forceinline__ float rbf(float a) { return lo_f(pk_bf16(a, a)); }

struct TapTab { uint16_t v[LSEQ]; };
static constexpr TapTab make_tab() {
    TapTab t{};
    for (int j = 0; j < LSEQ; ++j) {
        int p = j;
        if (j >= 256) {
            int s = (j < 512) ? 1 : ((j < 1024) ? 2 : 3);
            int q = j >> s, r = j & ((1 << s) - 1), half = 1 << (s - 1);
            if (r > half || (r == half && (q & 1))) q++;
            p = q << s;
        }
        int col = (p >> 3) & 15, a = p >> 7, low = p & 7, row = 0;
        if (p < 256) row = 8 * a + low;
        else if (p < 512) row = 16 + 4 * (a - 2) + (low >> 1);
        else if (p < 1024) row = 24 + 2 * (a - 4) + (low >> 2);
        else row = 32 + (a - 8);
        t.v[j] = (uint16_t)(row * 16 + col);
    }
    return t;
}
__device__ const TapTab g_taptab = make_tab();

struct cplx { float r, i; };
__device__ __forceinline__ cplx cmul(cplx a, cplx b) {
    cplx c;
    c.r = a.r * b.r - a.i * b.i;
    c.i = a.r * b.i + a.i * b.r;
    return c;
}
__device__ __forceinline__ cplx anchor(float a, float b, float k) {
    float e = __builtin_amdgcn_exp2f(a * k * 1.4426950408889634f);
    float th = __builtin_amdgcn_fractf(b * k * 0.15915494309189535f);
    cplx c;
    c.r = e * __builtin_amdgcn_cosf(th);
    c.i = e * __builtin_amdgcn_sinf(th);
    return c;
}

#define KG_ROWB 272
#define KG_TAB (64 * KG_ROWB)
#define KG_WAVE (KG_TAB + 1536)

__global__ __launch_bounds__(512) void prep_kernel(
    const uint16_t* __restrict__ u, const uint16_t* __restrict__ logA, const uint16_t* __restrict__ Aim,
    const uint16_t* __restrict__ Cre, const uint16_t* __restrict__ Cim, const uint16_t* __restrict__ logdt,
    const uint16_t* __restrict__ kin, const uint16_t* __restrict__ vin, const uint16_t* __restrict__ shk,
    uint16_t* __restrict__ xw, uint16_t* __restrict__ Kw)
{
    __shared__ __attribute__((aligned(16))) char lds[4 * KG_WAVE];
    const int lane = threadIdx.x & 63;
    const int w = __builtin_amdgcn_readfirstlane(threadIdx.x >> 6);
    const int b = blockIdx.x;
    STAMP(0, 0);

    if (w < 4) {
        const int wt = b * 2 + (w & 1);
        const int rt = wt >> 4, ct = wt & 15;
        const int cg = lane & 7, rb = lane >> 3;
        const int s0 = 64 * rt + 8 * rb, c0 = 64 * ct + 8 * cg;
        if (w < 2) {
            u32x4 r[8];
#pragma unroll
            for (int i = 0; i < 8; ++i) r[i] = *(const u32x4*)(u + (size_t)(s0 + i) * DCH + c0);
#pragma unroll
            for (int ch = 0; ch < 8; ++ch) {
                u32x4 o;
#pragma unroll
                for (int q = 0; q < 4; ++q)
                    o[q] = __builtin_amdgcn_perm(r[6 - 2 * q][ch >> 1], r[7 - 2 * q][ch >> 1],
                                                 (ch & 1) ? 0x07060302u : 0x05040100u);
                *(u32x4*)(xw + (size_t)(c0 + ch) * LSEQ + (2040 - s0)) = o;
            }
        } else {
            u32x4 kr[11], vr[8], wv[4];
#pragma unroll
            for (int i = 0; i < 11; ++i) {
                int s = s0 + i - 3;
                u32x4 z = {0, 0, 0, 0};
                kr[i] = (s >= 0) ? *(const u32x4*)(kin + (size_t)(s < 0 ? 0 : s) * DCH + c0) : z;
            }
#pragma unroll
            for (int i = 0; i < 8; ++i) vr[i] = *(const u32x4*)(vin + (size_t)(s0 + i) * DCH + c0);
#pragma unroll
            for (int i = 0; i < 4; ++i) wv[i] = *(const u32x4*)(shk + (size_t)c0 * 4 + 8 * i);
#pragma unroll
            for (int ch = 0; ch < 8; ++ch) {
                const uint32_t wlo = wv[ch >> 1][(ch & 1) * 2], whi = wv[ch >> 1][(ch & 1) * 2 + 1];
                const float w0 = lo_f(wlo), w1 = hi_f(wlo), w2 = lo_f(whi), w3 = hi_f(whi);
                float kf[11], vf[8];
#pragma unroll
                for (int i = 0; i < 11; ++i) kf[i] = (ch & 1) ? hi_f(kr[i][ch >> 1]) : lo_f(kr[i][ch >> 1]);
#pragma unroll
                for (int i = 0; i < 8; ++i) vf[i] = (ch & 1) ? hi_f(vr[i][ch >> 1]) : lo_f(vr[i][ch >> 1]);
                u32x4 o;
#pragma unroll
                for (int q = 0; q < 4; ++q) {
                    const int ia = 7 - 2 * q, ib = 6 - 2 * q;
                    float a0 = kf[ia] * w3, b0 = kf[ib] * w3;
                    rbf2(a0, b0);
                    float a1 = kf[ia + 1] * w2, b1 = kf[ib + 1] * w2;
                    rbf2(a1, b1);
                    a0 += a1; b0 += b1;
                    rbf2(a0, b0);
                    a1 = kf[ia + 2] * w1; b1 = kf[ib + 2] * w1;
                    rbf2(a1, b1);
                    a0 += a1; b0 += b1;
                    rbf2(a0, b0);
                    a1 = kf[ia + 3] * w0; b1 = kf[ib + 3] * w0;
                    rbf2(a1, b1);
                    a0 += a1; b0 += b1;
                    rbf2(a0, b0);
                    o[q] = pk_bf16(a0 * vf[ia], b0 * vf[ib]);
                }
                *(u32x4*)(xw + (size_t)(DCH + c0 + ch) * LSEQ + (2040 - s0)) = o;
            }
        }
        STAMP(0, 1);
        STAMPW(0, 2);
        return;
    }

    const int d = b * 4 + (w - 4);
    char* T = lds + (w - 4) * KG_WAVE;
    u32x4 tabv[4];
#pragma unroll
    for (int i = 0; i < 4; ++i) tabv[i] = *(const u32x4*)(g_taptab.v + 8 * (64 * i + lane));

    const float dt = rbf(expf(bf2f(logdt[d])));
    float pa[2], pb[2], pcr[2], pci[2];
    float mx = 0.f;
#pragma unroll
    for (int bt = 0; bt < 2; ++bt) {
        const int n = d * NMODE + bt * 64 + lane;
        const float ar = -rbf(expf(bf2f(logA[n])));
        pa[bt] = ar * dt;
        pb[bt] = bf2f(Aim[n]) * dt;
        pcr[bt] = 2.f * (bf2f(Cre[n]) * dt);
        pci[bt] = 2.f * (bf2f(Cim[n]) * dt);
        mx = fmaxf(mx, fmaxf(fabsf(pcr[bt]), fabsf(pci[bt])));
    }
#pragma unroll
    for (int o = 32; o >= 1; o >>= 1) mx = fmaxf(mx, __shfl_xor(mx, o));
    int sf = 266 - (int)((__float_as_uint(mx) >> 23) & 0xff);
    sf = sf < 1 ? 1 : (sf > 239 ? 239 : sf);
    const float scl = __uint_as_float((uint32_t)sf << 23);
    const float inv = __uint_as_float((uint32_t)(240 - sf) << 23);

    STAMP(0, 1);
#pragma unroll
    for (int r = 41; r < 48; ++r) *(uint32_t*)(T + r * KG_ROWB + lane * 4) = 0u;

    f32x4 acc[3] = {{0, 0, 0, 0}, {0, 0, 0, 0}, {0, 0, 0, 0}};
    const int fr = lane & 15, kb = lane >> 4;
#pragma unroll 1
    for (int bt = 0; bt < 2; ++bt) {
        const float a = bt ? pa[1] : pa[0], bb = bt ? pb[1] : pb[0];
        const cplx z1 = anchor(a, bb, 1.f), z8 = anchor(a, bb, 8.f), z128 = anchor(a, bb, 128.f);
        char* Tl = T + lane * 4;
        {
            cplx f = {16384.f, 0.f};
#pragma unroll
            for (int i = 0; i < 16; ++i) {
                *(uint32_t*)(Tl + (48 + i) * KG_ROWB) = pk_f16(f.r, -f.i);
                if (i < 15) f = cmul(f, z8);
            }
        }
        cplx h[8];
        h[1] = z1;
        h[2] = cmul(z1, z1);
        h[3] = cmul(h[2], z1);
        h[4] = cmul(h[2], h[2]);
        h[5] = cmul(h[4], z1);
        h[6] = cmul(h[4], h[2]);
        h[7] = cmul(h[4], h[3]);
        cplx cg = {(bt ? pcr[1] : pcr[0]) * scl, (bt ? pci[1] : pci[0]) * scl};
#pragma unroll
        for (int aa = 0; aa <= 16; ++aa) {
            if (aa < 2) {
                *(uint32_t*)(Tl + (8 * aa) * KG_ROWB) = pk_f16(cg.r, cg.i);
#pragma unroll
                for (int e = 1; e < 8; ++e) {
                    cplx t = cmul(cg, h[e]);
                    *(uint32_t*)(Tl + (8 * aa + e) * KG_ROWB) = pk_f16(t.r, t.i);
                }
            } else if (aa < 4) {
                *(uint32_t*)(Tl + (16 + 4 * (aa - 2)) * KG_ROWB) = pk_f16(cg.r, cg.i);
#pragma unroll
                for (int e = 1; e < 4; ++e) {
                    cplx t = cmul(cg, h[2 * e]);
                    *(uint32_t*)(Tl + (16 + 4 * (aa - 2) + e) * KG_ROWB) = pk_f16(t.r, t.i);
                }
            } else if (aa < 8) {
                *(uint32_t*)(Tl + (24 + 2 * (aa - 4)) * KG_ROWB) = pk_f16(cg.r, cg.i);
                cplx t = cmul(cg, h[4]);
                *(uint32_t*)(Tl + (24 + 2 * (aa - 4) + 1) * KG_ROWB) = pk_f16(t.r, t.i);
            } else {
                *(uint32_t*)(Tl + (32 + (aa - 8)) * KG_ROWB) = pk_f16(cg.r, cg.i);
            }
            if (aa < 16) cg = cmul(cg, z128);
        }
#pragma unroll
        for (int ks = 0; ks < 4; ++ks) {
            const f16x8 bfr = *(const f16x8*)(T + (48 + fr) * KG_ROWB + ks * 64 + kb * 16);
#pragma unroll
            for (int rt = 0; rt < 3; ++rt) {
                const f16x8 afr = *(const f16x8*)(T + (16 * rt + fr) * KG_ROWB + ks * 64 + kb * 16);
                acc[rt] = __builtin_amdgcn_mfma_f32_16x16x32_f16(afr, bfr, acc[rt], 0, 0, 0);
            }
        }
    }
    STAMP(0, 2);
    uint16_t* KD = (uint16_t*)(T + KG_TAB);
#pragma unroll
    for (int rt = 0; rt < 3; ++rt)
#pragma unroll
        for (int r = 0; r < 4; r += 2) {
            uint32_t p = pk_bf16(acc[rt][r] * inv, acc[rt][r + 1] * inv);
            KD[(16 * rt + 4 * kb + r) * 16 + fr] = (uint16_t)p;
            KD[(16 * rt + 4 * kb + r + 1) * 16 + fr] = (uint16_t)(p >> 16);
        }
#pragma unroll
    for (int i = 0; i < 4; ++i) {
        u32x4 o;
#pragma unroll
        for (int q = 0; q < 4; ++q) {
            const uint32_t lo = KD[tabv[i][q] & 0xffffu], hi = KD[tabv[i][q] >> 16];
            o[q] = lo | (hi << 16);
        }
        *(u32x4*)(Kw + (size_t)d * LSEQ + 8 * (64 * i + lane)) = o;
    }
    STAMP(0, 3);
    STAMPW(0, 4);
}

#define KA_OFF 0
#define KB_OFF 4640
#define XA_OFF 9216
#define XB_OFF 13440
#define CH_STRIDE 17600
#define YS_OFF (8 * CH_STRIDE)
#define YS_STRIDE 2112
#define CONV_LDS (YS_OFF + 8 * YS_STRIDE)

__device__ __forceinline__ void put_k(char* cb, int y, u32x4 d, uint32_t prev) {
    *(u32x4*)(cb + KA_OFF + 464 + 16 * y) = d;
    u32x4 o;
    o[0] = __builtin_amdgcn_alignbyte(d[0], prev, 2);
    o[1] = __builtin_amdgcn_alignbyte(d[1], d[0], 2);
    o[2] = __builtin_amdgcn_alignbyte(d[2], d[1], 2);
    o[3] = __builtin_amdgcn_alignbyte(d[3], d[2], 2);
    *(u32x4*)(cb + KB_OFF + 464 + 16 * y) = o;
}
__device__ __forceinline__ void put_x(char* cb, int y, u32x4 d) {
    *(u32x4*)(cb + XA_OFF + 16 * y) = d;
    *(uint32_t*)(cb + XB_OFF + 16 * y - 4) = d[0];
    u32x2 m = {d[1], d[2]};
    *(u32x2*)(cb + XB_OFF + 16 * y) = m;
    *(uint32_t*)(cb + XB_OFF + 16 * y + 8) = d[3];
}
__device__ __forceinline__ uint32_t prev_lane(uint32_t v, uint32_t carry) {
    return (uint32_t)__builtin_amdgcn_update_dpp((int)carry, (int)v, 0x138  , 0xf, 0xf, false);
}
typedef __attribute__((address_space(3))) char* lds_t;
typedef __attribute__((address_space(3))) const volatile u32x2* lds_v2;
typedef __attribute__((address_space(3))) const bf16x8* lds_k8;
__device__ __forceinline__ bf16x8 ldx(lds_t p) {
    u32x2 a = *(lds_v2)p, b = *(lds_v2)(p + 8);
    u32x4 v = {a[0], a[1], b[0], b[1]};
    return __builtin_bit_cast(bf16x8, v);
}
__device__ __forceinline__ bf16x8 ldk(lds_t p) { return *(lds_k8)p; }
#define MFMA(a, b, c) c = __builtin_amdgcn_mfma_f32_16x16x32_bf16(a, b, c, 0, 0, 0)
#define SB() __builtin_amdgcn_sched_barrier(0)
#define SG_DS(n) __builtin_amdgcn_sched_group_barrier(0x100, n, 0)
#define SG_MF(n) __builtin_amdgcn_sched_group_barrier(0x008, n, 0)
#define ST2_OFF 1488

__device__ __forceinline__ void epi_acc(const f32x4 a, const u32x2 xd2, char* dst, float Dv) {
#pragma unroll
    for (int hh = 0; hh < 2; ++hh) {
        const uint32_t xd = xd2[hh];
        float y1 = a[2 * hh + 1], y0 = a[2 * hh];
        rbf2(y1, y0);
        float t1 = lo_f(xd) * Dv, t0 = hi_f(xd) * Dv;
        rbf2(t1, t0);
        *(uint32_t*)(dst - 64 * hh) = pk_bf16(y1 + t1, y0 + t0);
    }
}
__device__ __forceinline__ void epi_row(const char* a0, int stride4, u32x4 qq4, uint16_t* dst) {
    s16x4 v0 = __builtin_amdgcn_ds_read_tr16_b64_v4i16((__attribute__((address_space(3))) s16x4*)(a0));
    s16x4 v1 = __builtin_amdgcn_ds_read_tr16_b64_v4i16((__attribute__((address_space(3))) s16x4*)(a0 + stride4));
    u32x2 p0 = __builtin_bit_cast(u32x2, v0), p1 = __builtin_bit_cast(u32x2, v1);
    u32x4 o = {p0[0], p0[1], p1[0], p1[1]};
#pragma unroll
    for (int j = 0; j < 4; ++j)
        o[j] = pk_bf16(lo_f(qq4[j]) * lo_f(o[j]), hi_f(qq4[j]) * hi_f(o[j]));
    *(u32x4*)dst = o;
}

#define SG_VA(n) __builtin_amdgcn_sched_group_barrier(0x002, n, 0)
__device__ __forceinline__ void lds_barrier() {
    __builtin_amdgcn_fence(__ATOMIC_RELEASE, "workgroup", "local");
    __builtin_amdgcn_s_barrier();
    __builtin_amdgcn_fence(__ATOMIC_ACQUIRE, "workgroup", "local");
}
#define LOADF(KF, XF, P) _Pragma("unroll") for (int m = 0; m < 8; ++m) { KF[m] = ldk(kaddr + 64 * (P) + 512 * m); XF[m] = ldx(xaddr + 64 * (P) + 512 * (7 - m)); }
#define MM20(K, X) \
    MFMA(K[4], X[3], acc[7]); MFMA(K[4], X[2], acc[6]); MFMA(K[3], X[4], a7b);    MFMA(K[4], X[1], acc[5]); \
    MFMA(K[5], X[2], acc[7]); MFMA(K[2], X[4], acc[6]); MFMA(K[2], X[5], a7b);    MFMA(K[4], X[0], acc[4]); \
    MFMA(K[6], X[1], acc[7]); MFMA(K[5], X[1], acc[6]); MFMA(K[1], X[6], a7b);    MFMA(K[1], X[4], acc[5]); \
    MFMA(K[7], X[0], acc[7]); MFMA(K[1], X[5], acc[6]); MFMA(K[0], X[7], a7b);    MFMA(K[5], X[0], acc[5]); \
    MFMA(K[6], X[0], acc[6]); MFMA(K[0], X[4], acc[4]); MFMA(K[0], X[6], acc[6]); MFMA(K[0], X[5], acc[5]);
#define PAT(nv) _Pragma("unroll") for (int i_ = 0; i_ < 13; ++i_) { SG_MF(1); SG_DS(2); SG_VA(nv); } \
                _Pragma("unroll") for (int i_ = 0; i_ < 7; ++i_) { SG_MF(1); SG_VA(nv); } SB();

__global__ __launch_bounds__(512) void conv_kernel(
    const uint16_t* __restrict__ xw, const uint16_t* __restrict__ Kw, const uint16_t* __restrict__ ssmk,
    const uint16_t* __restrict__ qin, const uint16_t* __restrict__ Dsk, const uint16_t* __restrict__ sD,
    uint16_t* __restrict__ out)
{
    __shared__ __attribute__((aligned(16))) char lds[CONV_LDS];
    const int b = blockIdx.x;
    const int xcd = b & 7, idx = b >> 3, part = idx >> 4, grp = xcd * 16 + (idx & 15);
    const int lane = threadIdx.x & 63;
    const int w = __builtin_amdgcn_readfirstlane(threadIdx.x >> 6);
    const int chl = grp * 8 + w;
    const uint16_t* ksrc = (part ? ssmk : Kw) + (size_t)chl * LSEQ;
    const uint16_t* xsrc = xw + (size_t)(part * DCH + chl) * LSEQ;
    char* cb = lds + w * CH_STRIDE;

    STAMP(1, 0);
    const u32x4 k0 = *(const u32x4*)(ksrc + 8 * lane);
    SB();
    const u32x4 x3 = *(const u32x4*)(xsrc + 8 * (192 + lane));
    SB();
    const u32x4 k1 = *(const u32x4*)(ksrc + 8 * (64 + lane));
    SB();
    const u32x4 x2 = *(const u32x4*)(xsrc + 8 * (128 + lane));
    SB();

    const int g = lane >> 4, fr = lane & 15;
    {
        const u32x4 z = {0, 0, 0, 0};
        if (lane < 29) {
            *(u32x4*)(cb + KA_OFF + 16 * lane) = z;
            *(u32x4*)(cb + KB_OFF + 16 * lane) = z;
        }
        if (lane < 4) *(u32x4*)(cb + XA_OFF + 4096 + 16 * lane) = z;
        if (lane < 5) *(u32x4*)(cb + XB_OFF + 4080 + 16 * lane) = z;
    }
    put_k(cb, lane, k0, prev_lane(k0[3], 0u));
    const uint32_t c1 = __builtin_amdgcn_readlane(k0[3], 63);
    put_x(cb, 192 + lane, x3);
    put_k(cb, 64 + lane, k1, prev_lane(k1[3], c1));
    const uint32_t c2 = __builtin_amdgcn_readlane(k1[3], 63);
    put_x(cb, 128 + lane, x2);
    SB();
    const u32x4 k2 = *(const u32x4*)(ksrc + 8 * (128 + lane));
    SB();
    const u32x4 x1 = *(const u32x4*)(xsrc + 8 * (64 + lane));
    SB();
    const u32x4 k3 = *(const u32x4*)(ksrc + 8 * (192 + lane));
    SB();
    const u32x4 x0 = *(const u32x4*)(xsrc + 8 * lane);
    SB();
    const float Dv = bf2f((part ? sD : Dsk)[chl]);
    const int r0 = 1023 - (128 * w + lane);
    u32x4 qv[4];
#pragma unroll
    for (int i = 0; i < 4; ++i) qv[i] = (u32x4){0x3f803f80u, 0x3f803f80u, 0x3f803f80u, 0x3f803f80u};
    if (part) {
#pragma unroll
        for (int i = 0; i < 4; ++i)
            qv[i] = *(const u32x4*)(qin + (size_t)(r0 - 64 * (i & 1) + 1024 * (i >> 1)) * DCH + grp * 8);
    }
    SB();

    const lds_t lb = (lds_t)lds + w * CH_STRIDE;
    const lds_t kaddr = lb + ((fr & 1) ? KA_OFF : KB_OFF) + 2 * (32 * (fr >> 1) + 8 * g + 8);
    lds_t xaddr = lb + ((fr & 1) ? (XA_OFF + 2 * (1822 - 2 * fr + 8 * g)) : (XB_OFF + 2 * (1820 - 2 * fr + 8 * g))) - 3584;
    asm volatile("" : "+v"(xaddr));
    char* ys = lds + YS_OFF;
    const int lo_ = 64 * g + 2 * fr;
    const char* xa = cb + XA_OFF + 2 * (2046 - lo_);
    const int qq = (lane >> 2) & 3, pp = lane & 3;
    uint16_t* ob = out + part * DCH + grp * 8 + (size_t)r0 * (2 * DCH);

    f32x4 acc[8];
#pragma unroll
    for (int i = 0; i < 8; ++i) acc[i] = (f32x4){0, 0, 0, 0};
    f32x4 a7b = {0, 0, 0, 0};
    u32x2 xd[8];

    STAMP(1, 1);
    {
        bf16x8 ka[4], xa_[4], kb[4], xb_[4];
#pragma unroll
        for (int m = 0; m < 4; ++m) { ka[m] = ldk(kaddr + 512 * m); xa_[m] = ldx(xaddr + 512 * (7 - m)); }
#pragma unroll 1
        for (int c0 = 0; c0 < 8; c0 += 2) {
            const lds_t kp = kaddr + 64 * c0, xp = xaddr + 64 * c0;
#pragma unroll
            for (int m = 0; m < 4; ++m) { kb[m] = ldk(kp + 64 + 512 * m); xb_[m] = ldx(xp + 64 + 512 * (7 - m)); }
#pragma unroll
            for (int m = 0; m < 4; ++m)
#pragma unroll
                for (int t = 0; t < 4; ++t) MFMA(ka[m], xa_[t], acc[m + t]);
#pragma unroll
            for (int i = 0; i < 12; ++i) { SG_MF(1); SG_DS(1); }
            SG_MF(4);
            SB();
#pragma unroll
            for (int m = 0; m < 4; ++m) { ka[m] = ldk(kp + 128 + 512 * m); xa_[m] = ldx(xp + 128 + 512 * (7 - m)); }
#pragma unroll
            for (int m = 0; m < 4; ++m)
#pragma unroll
                for (int t = 0; t < 4; ++t) MFMA(kb[m], xb_[t], acc[m + t]);
#pragma unroll
            for (int i = 0; i < 12; ++i) { SG_MF(1); SG_DS(1); }
            SG_MF(4);
            SB();
        }
    }
    STAMP(1, 2);
    put_k(cb, 128 + lane, k2, prev_lane(k2[3], c2));
    const uint32_t c3 = __builtin_amdgcn_readlane(k2[3], 63);
    put_x(cb, 64 + lane, x1);
    put_k(cb, 192 + lane, k3, prev_lane(k3[3], c3));
    put_x(cb, lane, x0);
    SB();
    STAMP(1, 3);
    {
        bf16x8 ka[8], xa_[8], kb[8], xb_[8];
        LOADF(ka, xa_, 0);
#pragma unroll
        for (int tt = 0; tt < 8; ++tt) xd[tt] = (u32x2){*(const uint32_t*)(xa - 512 * tt), *(const uint32_t*)(xa - 512 * tt - 64)};
        SB();
        LOADF(kb, xb_, 1);
        epi_acc(acc[0], xd[0], ys + w * YS_STRIDE + 2 * (1022 - lo_), Dv);
        epi_acc(acc[1], xd[1], ys + w * YS_STRIDE + 2 * (1022 - lo_) - 512, Dv);
        MM20(ka, xa_);
        PAT(3);
        LOADF(ka, xa_, 2);
        epi_acc(acc[2], xd[2], ys + w * YS_STRIDE + 2 * (1022 - lo_) - 1024, Dv);
        epi_acc(acc[3], xd[3], ys + w * YS_STRIDE + 2 * (1022 - lo_) - 1536, Dv);
        MM20(kb, xb_);
        PAT(3);
        lds_barrier();
        SB();
        LOADF(kb, xb_, 3);
        epi_row(ys + qq * YS_STRIDE + (128 * w + 16 * g + 4 * pp) * 2, 4 * YS_STRIDE, qv[0], ob);
        MM20(ka, xa_);
        PAT(2);
        LOADF(ka, xa_, 4);
        epi_row(ys + qq * YS_STRIDE + (128 * w + 64 + 16 * g + 4 * pp) * 2, 4 * YS_STRIDE, qv[1], ob - (size_t)64 * (2 * DCH));
        MM20(kb, xb_);
        PAT(2);
        LOADF(kb, xb_, 5);
        MM20(ka, xa_);
        PAT(0);
        LOADF(ka, xa_, 6);
        MM20(kb, xb_);
        PAT(0);
        LOADF(kb, xb_, 7);
        MM20(ka, xa_);
        PAT(0);
        MM20(kb, xb_);
        SB();
        acc[7] += a7b;
    }
    STAMP(1, 4);
#pragma unroll
    for (int tt = 4; tt < 8; ++tt) epi_acc(acc[tt], xd[tt], cb + KA_OFF + 2 * (1022 - lo_) - 512 * (tt - 4), Dv);
    lds_barrier();
    STAMP(1, 5);
    epi_row(lds + qq * CH_STRIDE + KA_OFF + (128 * w + 16 * g + 4 * pp) * 2, 4 * CH_STRIDE, qv[2], ob + (size_t)1024 * (2 * DCH));
    epi_row(lds + qq * CH_STRIDE + KA_OFF + (128 * w + 64 + 16 * g + 4 * pp) * 2, 4 * CH_STRIDE, qv[3], ob + (size_t)960 * (2 * DCH));
    STAMP(1, 6);
    STAMPW(1, 7);
}

extern "C" void kernel_launch(void* const* d_in, const int* in_sizes, int n_in,
                              void* d_out, int out_size, void* d_ws, size_t ws_size,
                              hipStream_t stream) {
    const uint16_t* u = (const uint16_t*)d_in[0];
    const uint16_t* logA = (const uint16_t*)d_in[1];
    const uint16_t* Aim = (const uint16_t*)d_in[2];
    const uint16_t* Cre = (const uint16_t*)d_in[3];
    const uint16_t* Cim = (const uint16_t*)d_in[4];
    const uint16_t* logdt = (const uint16_t*)d_in[5];
    const uint16_t* Dsk = (const uint16_t*)d_in[6];
    const uint16_t* q = (const uint16_t*)d_in[7];
    const uint16_t* k = (const uint16_t*)d_in[8];
    const uint16_t* v = (const uint16_t*)d_in[9];
    const uint16_t* shk = (const uint16_t*)d_in[10];
    const uint16_t* ssmk = (const uint16_t*)d_in[11];
    const uint16_t* sD = (const uint16_t*)d_in[12];
    uint16_t* xw = (uint16_t*)d_ws;
    uint16_t* Kw = xw + (size_t)2 * DCH * LSEQ;
    prep_kernel<<<256, 512, 0, stream>>>(u, logA, Aim, Cre, Cim, logdt, k, v, shk, xw, Kw);
    conv_kernel<<<256, 512, 0, stream>>>(xw, Kw, ssmk, q, Dsk, sD, (uint16_t*)d_out);
}
```

```cpp
#include <hip/hip_runtime.h>
#include <stdint.h>

typedef __attribute__((ext_vector_type(4))) unsigned int u32x4;
typedef __attribute__((ext_vector_type(2))) unsigned int u32x2;
typedef __attribute__((ext_vector_type(4))) float f32x4;
typedef __attribute__((ext_vector_type(2))) float f32x2;
typedef __attribute__((ext_vector_type(8))) _Float16 f16x8;
typedef __attribute__((ext_vector_type(2))) _Float16 f16x2;
typedef __attribute__((ext_vector_type(8))) __bf16 bf16x8;
typedef __attribute__((ext_vector_type(2))) __bf16 bf16x2;
typedef __attribute__((ext_vector_type(4))) short s16x4;

#define STAMP(k, i)
#define STAMPW(k, i)
#define LSEQ 2048
#define DCH 1024
#define NMODE 128

__device__ __forceinline__ float bf2f(uint16_t v) { return __uint_as_float(((uint32_t)v) << 16); }
__device__ __forceinline__ float lo_f(uint32_t w) { return __uint_as_float(w << 16); }
__device__ __forceinline__ float hi_f(uint32_t w) { return __uint_as_float(w & 0xffff0000u); }
__device__ __forceinline__ uint32_t pk_bf16(float lo, float hi) {
    f32x2 x = {lo, hi};
    bf16x2 y = __builtin_convertvector(x, bf16x2);
    return __builtin_bit_cast(uint32_t, y);
}
__device__ __forceinline__ uint32_t pk_f16(float lo, float hi) {
    f32x2 x = {lo, hi};
    f16x2 y = __builtin_convertvector(x, f16x2);
    return __builtin_bit_cast(uint32_t, y);
}
__device__ __forceinline__ void rbf2(float& a, float& b) {
    uint32_t p = pk_bf16(a, b);
    a = lo_f(p);
    b = hi_f(p);
}
__device__ __forceinline__ float rbf(float a) { return lo_f(pk_bf16(a, a)); }

struct TapTab { uint16_t v[LSEQ]; };
static constexpr TapTab make_tab() {
    TapTab t{};
    for (int j = 0; j < LSEQ; ++j) {
        int p = j;
        if (j >= 256) {
            int s = (j < 512) ? 1 : ((j < 1024) ? 2 : 3);
            int q = j >> s, r = j & ((1 << s) - 1), half = 1 << (s - 1);
            if (r > half || (r == half && (q & 1))) q++;
            p = q << s;
        }
        int col = (p >> 3) & 15, a = p >> 7, low = p & 7, row = 0;
        if (p < 256) row = 8 * a + low;
        else if (p < 512) row = 16 + 4 * (a - 2) + (low >> 1);
        else if (p < 1024) row = 24 + 2 * (a - 4) + (low >> 2);
        else row = 32 + (a - 8);
        t.v[j] = (uint16_t)(row * 16 + col);
    }
    return t;
}
__device__ const TapTab g_taptab = make_tab();

__device__ __forceinline__ f32x2 cmul(f32x2 a, f32x2 b) {
    f32x2 t, c;
    asm("v_pk_mul_f32 %0, %1, %2 op_sel:[0,0] op_sel_hi:[0,1]" : "=v"(t) : "v"(a), "v"(b));
    asm("v_pk_fma_f32 %0, %1, %2, %3 op_sel:[1,1,0] op_sel_hi:[1,0,1] neg_lo:[1,0,0]" : "=v"(c) : "v"(a), "v"(b), "v"(t));
    return c;
}
__device__ __forceinline__ f32x2 anchor(float a, float b) {
    const float e = __builtin_amdgcn_exp2f(a);
    const float th = __builtin_amdgcn_fractf(b);
    return (f32x2){e * __builtin_amdgcn_cosf(th), e * __builtin_amdgcn_sinf(th)};
}
__device__ __forceinline__ uint32_t pk16(f32x2 c) { return pk_f16(c[0], c[1]); }

#define KG_ROWB 272
#define KG_TAB (64 * KG_ROWB)
#define KG_WAVE (KG_TAB + 1536)

__global__ __launch_bounds__(512) void prep_kernel(
    const uint16_t* __restrict__ u, const uint16_t* __restrict__ logA, const uint16_t* __restrict__ Aim,
    const uint16_t* __restrict__ Cre, const uint16_t* __restrict__ Cim, const uint16_t* __restrict__ logdt,
    const uint16_t* __restrict__ kin, const uint16_t* __restrict__ vin, const uint16_t* __restrict__ shk,
    uint16_t* __restrict__ xw, uint16_t* __restrict__ Kw)
{
    __shared__ __attribute__((aligned(16))) char lds[4 * KG_WAVE];
    const int lane = threadIdx.x & 63;
    const int w = __builtin_amdgcn_readfirstlane(threadIdx.x >> 6);
    const int b = blockIdx.x;
    const int xcd = b & 7, jb = b >> 3;
    STAMP(0, 0);

    if (w < 4) {
        const int cg = lane & 15, rb = lane >> 4;
        const int s0 = 64 * jb + 32 * (w >> 1) + 8 * rb, c0 = 128 * xcd + 64 * (w & 1) + 4 * cg;
        u32x2 ur[8], kr[11], vr[8];
        u32x4 wv[2];
#pragma unroll
        for (int i = 0; i < 11; ++i) {
            const int s = s0 + i - 3;
            kr[i] = *(const u32x2*)(kin + (size_t)(s < 0 ? 0 : s) * DCH + c0);
            if (i < 3 && s < 0) kr[i] = (u32x2){0, 0};
        }
#pragma unroll
        for (int i = 0; i < 2; ++i) wv[i] = *(const u32x4*)(shk + (size_t)c0 * 4 + 8 * i);
#pragma unroll
        for (int i = 0; i < 8; ++i) vr[i] = *(const u32x2*)(vin + (size_t)(s0 + i) * DCH + c0);
#pragma unroll
        for (int i = 0; i < 8; ++i) ur[i] = *(const u32x2*)(u + (size_t)(s0 + i) * DCH + c0);
#pragma unroll
        for (int ch = 0; ch < 4; ++ch) {
            const uint32_t wlo = wv[ch >> 1][(ch & 1) * 2], whi = wv[ch >> 1][(ch & 1) * 2 + 1];
            const float w0 = lo_f(wlo), w1 = hi_f(wlo), w2 = lo_f(whi), w3 = hi_f(whi);
            float kf[11], vf[8];
#pragma unroll
            for (int i = 0; i < 11; ++i) kf[i] = (ch & 1) ? hi_f(kr[i][ch >> 1]) : lo_f(kr[i][ch >> 1]);
#pragma unroll
            for (int i = 0; i < 8; ++i) vf[i] = (ch & 1) ? hi_f(vr[i][ch >> 1]) : lo_f(vr[i][ch >> 1]);
            u32x4 o;
#pragma unroll
            for (int q = 0; q < 4; ++q) {
                const int ia = 7 - 2 * q, ib = 6 - 2 * q;
                float a0 = kf[ia] * w3, b0 = kf[ib] * w3;
                rbf2(a0, b0);
                float a1 = kf[ia + 1] * w2, b1 = kf[ib + 1] * w2;
                rbf2(a1, b1);
                a0 += a1; b0 += b1;
                rbf2(a0, b0);
                a1 = kf[ia + 2] * w1; b1 = kf[ib + 2] * w1;
                rbf2(a1, b1);
                a0 += a1; b0 += b1;
                rbf2(a0, b0);
                a1 = kf[ia + 3] * w0; b1 = kf[ib + 3] * w0;
                rbf2(a1, b1);
                a0 += a1; b0 += b1;
                rbf2(a0, b0);
                o[q] = pk_bf16(a0 * vf[ia], b0 * vf[ib]);
            }
            *(u32x4*)(xw + (size_t)(DCH + c0 + ch) * LSEQ + (2040 - s0)) = o;
        }
#pragma unroll
        for (int ch = 0; ch < 4; ++ch) {
            u32x4 o;
#pragma unroll
            for (int q = 0; q < 4; ++q)
                o[q] = __builtin_amdgcn_perm(ur[6 - 2 * q][ch >> 1], ur[7 - 2 * q][ch >> 1],
                                             (ch & 1) ? 0x07060302u : 0x05040100u);
            *(u32x4*)(xw + (size_t)(c0 + ch) * LSEQ + (2040 - s0)) = o;
        }
        STAMP(0, 1);
        STAMPW(0, 2);
        return;
    }

    const int d = 128 * xcd + 4 * jb + (w - 4);
    char* T = lds + (w - 4) * KG_WAVE;
    u32x4 tabv[4];
#pragma unroll
    for (int i = 0; i < 4; ++i) tabv[i] = *(const u32x4*)(g_taptab.v + 8 * (64 * i + lane));

    const float dt = rbf(expf(bf2f(logdt[d])));
    float pa[2], pb[2], pcr[2], pci[2];
    float mx = 0.f;
#pragma unroll
    for (int bt = 0; bt < 2; ++bt) {
        const int n = d * NMODE + bt * 64 + lane;
        const float ar = -rbf(expf(bf2f(logA[n])));
        pa[bt] = (ar * dt) * 1.4426950408889634f;
        pb[bt] = (bf2f(Aim[n]) * dt) * 0.15915494309189535f;
        pcr[bt] = 2.f * (bf2f(Cre[n]) * dt);
        pci[bt] = 2.f * (bf2f(Cim[n]) * dt);
        mx = fmaxf(mx, fmaxf(fabsf(pcr[bt]), fabsf(pci[bt])));
    }
#pragma unroll
    for (int o = 32; o >= 1; o >>= 1) mx = fmaxf(mx, __shfl_xor(mx, o));
    int sf = 266 - (int)((__float_as_uint(mx) >> 23) & 0xff);
    sf = sf < 1 ? 1 : (sf > 239 ? 239 : sf);
    const float scl = __uint_as_float((uint32_t)sf << 23);
    const float inv = __uint_as_float((uint32_t)(240 - sf) << 23);

    STAMP(0, 1);
#pragma unroll
    for (int r = 41; r < 48; ++r) *(uint32_t*)(T + r * KG_ROWB + lane * 4) = 0u;

    f32x4 acc[3] = {{0, 0, 0, 0}, {0, 0, 0, 0}, {0, 0, 0, 0}};
    const int fr = lane & 15, kb = lane >> 4;
#pragma unroll 1
    for (int bt = 0; bt < 2; ++bt) {
        const float a = bt ? pa[1] : pa[0], bb = bt ? pb[1] : pb[0];
        const f32x2 z1 = anchor(a, bb), z8c = anchor(a * 8.f, bb * -8.f), z128 = anchor(a * 128.f, bb * 128.f);
        char* Tl = T + lane * 4;
        {
            f32x2 f = {16384.f, 0.f};
#pragma unroll
            for (int i = 0; i < 16; ++i) {
                *(uint32_t*)(Tl + (48 + i) * KG_ROWB) = pk16(f);
                if (i < 15) f = cmul(f, z8c);
            }
        }
        f32x2 h[8];
        h[1] = z1;
        h[2] = cmul(z1, z1);
        h[3] = cmul(h[2], z1);
        h[4] = cmul(h[2], h[2]);
        h[5] = cmul(h[4], z1);
        h[6] = cmul(h[4], h[2]);
        h[7] = cmul(h[4], h[3]);
        f32x2 cg = {(bt ? pcr[1] : pcr[0]) * scl, (bt ? pci[1] : pci[0]) * scl};
#pragma unroll
        for (int aa = 0; aa <= 16; ++aa) {
            if (aa < 2) {
                *(uint32_t*)(Tl + (8 * aa) * KG_ROWB) = pk16(cg);
#pragma unroll
                for (int e = 1; e < 8; ++e) *(uint32_t*)(Tl + (8 * aa + e) * KG_ROWB) = pk16(cmul(cg, h[e]));
            } else if (aa < 4) {
                *(uint32_t*)(Tl + (16 + 4 * (aa - 2)) * KG_ROWB) = pk16(cg);
#pragma unroll
                for (int e = 1; e < 4; ++e) *(uint32_t*)(Tl + (16 + 4 * (aa - 2) + e) * KG_ROWB) = pk16(cmul(cg, h[2 * e]));
            } else if (aa < 8) {
                *(uint32_t*)(Tl + (24 + 2 * (aa - 4)) * KG_ROWB) = pk16(cg);
                *(uint32_t*)(Tl + (24 + 2 * (aa - 4) + 1) * KG_ROWB) = pk16(cmul(cg, h[4]));
            } else {
                *(uint32_t*)(Tl + (32 + (aa - 8)) * KG_ROWB) = pk16(cg);
            }
            if (aa < 16) cg = cmul(cg, z128);
        }
#pragma unroll
        for (int ks = 0; ks < 4; ++ks) {
            const f16x8 bfr = *(const f16x8*)(T + (48 + fr) * KG_ROWB + ks * 64 + kb * 16);
#pragma unroll
            for (int rt = 0; rt < 3; ++rt) {
                const f16x8 afr = *(const f16x8*)(T + (16 * rt + fr) * KG_ROWB + ks * 64 + kb * 16);
                acc[rt] = __builtin_amdgcn_mfma_f32_16x16x32_f16(afr, bfr, acc[rt], 0, 0, 0);
            }
        }
    }
    STAMP(0, 2);
    uint16_t* KD = (uint16_t*)(T + KG_TAB);
#pragma unroll
    for (int rt = 0; rt < 3; ++rt)
#pragma unroll
        for (int r = 0; r < 4; r += 2) {
            uint32_t p = pk_bf16(acc[rt][r] * inv, acc[rt][r + 1] * inv);
            KD[(16 * rt + 4 * kb + r) * 16 + fr] = (uint16_t)p;
            KD[(16 * rt + 4 * kb + r + 1) * 16 + fr] = (uint16_t)(p >> 16);
        }
#pragma unroll
    for (int i = 0; i < 4; ++i) {
        u32x4 o;
#pragma unroll
        for (int q = 0; q < 4; ++q) {
            const uint32_t lo = KD[tabv[i][q] & 0xffffu], hi = KD[tabv[i][q] >> 16];
            o[q] = lo | (hi << 16);
        }
        *(u32x4*)(Kw + (size_t)d * LSEQ + 8 * (64 * i + lane)) = o;
    }
    STAMP(0, 3);
    STAMPW(0, 4);
}

#define KA_OFF 0
#define KB_OFF 4640
#define XA_OFF 9216
#define XB_OFF 13440
#define CH_STRIDE 17600
#define YS_OFF (8 * CH_STRIDE)
#define YS_STRIDE 2112
#define CONV_LDS (YS_OFF + 8 * YS_STRIDE)

__device__ __forceinline__ void put_k(char* cb, int y, u32x4 d, uint32_t prev) {
    *(u32x4*)(cb + KA_OFF + 464 + 16 * y) = d;
    u32x4 o;
    o[0] = __builtin_amdgcn_alignbyte(d[0], prev, 2);
    o[1] = __builtin_amdgcn_alignbyte(d[1], d[0], 2);
    o[2] = __builtin_amdgcn_alignbyte(d[2], d[1], 2);
    o[3] = __builtin_amdgcn_alignbyte(d[3], d[2], 2);
    *(u32x4*)(cb + KB_OFF + 464 + 16 * y) = o;
}
__device__ __forceinline__ void put_x(char* cb, int y, u32x4 d) {
    *(u32x4*)(cb + XA_OFF + 16 * y) = d;
    *(uint32_t*)(cb + XB_OFF + 16 * y - 4) = d[0];
    u32x2 m = {d[1], d[2]};
    *(u32x2*)(cb + XB_OFF + 16 * y) = m;
    *(uint32_t*)(cb + XB_OFF + 16 * y + 8) = d[3];
}
__device__ __forceinline__ uint32_t prev_lane(uint32_t v, uint32_t carry) {
    return (uint32_t)__builtin_amdgcn_update_dpp((int)carry, (int)v, 0x138  , 0xf, 0xf, false);
}
typedef __attribute__((address_space(3))) char* lds_t;
typedef __attribute__((address_space(3))) const volatile u32x2* lds_v2;
typedef __attribute__((address_space(3))) const bf16x8* lds_k8;
__device__ __forceinline__ bf16x8 ldx(lds_t p) {
    u32x2 a = *(lds_v2)p, b = *(lds_v2)(p + 8);
    u32x4 v = {a[0], a[1], b[0], b[1]};
    return __builtin_bit_cast(bf16x8, v);
}
__device__ __forceinline__ bf16x8 ldk(lds_t p) { return *(lds_k8)p; }
#define MFMA(a, b, c) c = __builtin_amdgcn_mfma_f32_16x16x32_bf16(a, b, c, 0, 0, 0)
#define SB() __builtin_amdgcn_sched_barrier(0)
#define SG_DS(n) __builtin_amdgcn_sched_group_barrier(0x100, n, 0)
#define SG_MF(n) __builtin_amdgcn_sched_group_barrier(0x008, n, 0)
#define ST2_OFF 1488

__device__ __forceinline__ void epi_acc(const f32x4 a, const u32x2 xd2, char* dst, float Dv) {
#pragma unroll
    for (int hh = 0; hh < 2; ++hh) {
        const uint32_t xd = xd2[hh];
        float y1 = a[2 * hh + 1], y0 = a[2 * hh];
        rbf2(y1, y0);
        float t1 = lo_f(xd) * Dv, t0 = hi_f(xd) * Dv;
        rbf2(t1, t0);
        *(uint32_t*)(dst - 64 * hh) = pk_bf16(y1 + t1, y0 + t0);
    }
}
__device__ __forceinline__ void epi_row(const char* a0, int stride4, u32x4 qq4, uint16_t* dst) {
    s16x4 v0 = __builtin_amdgcn_ds_read_tr16_b64_v4i16((__attribute__((address_space(3))) s16x4*)(a0));
    s16x4 v1 = __builtin_amdgcn_ds_read_tr16_b64_v4i16((__attribute__((address_space(3))) s16x4*)(a0 + stride4));
    u32x2 p0 = __builtin_bit_cast(u32x2, v0), p1 = __builtin_bit_cast(u32x2, v1);
    u32x4 o = {p0[0], p0[1], p1[0], p1[1]};
#pragma unroll
    for (int j = 0; j < 4; ++j)
        o[j] = pk_bf16(lo_f(qq4[j]) * lo_f(o[j]), hi_f(qq4[j]) * hi_f(o[j]));
    *(u32x4*)dst = o;
}

#define SG_VA(n) __builtin_amdgcn_sched_group_barrier(0x002, n, 0)
__device__ __forceinline__ void lds_barrier() {
    __builtin_amdgcn_fence(__ATOMIC_RELEASE, "workgroup", "local");
    __builtin_amdgcn_s_barrier();
    __builtin_amdgcn_fence(__ATOMIC_ACQUIRE, "workgroup", "local");
}
#define LOADF(KF, XF, P) _Pragma("unroll") for (int m = 0; m < 8; ++m) { KF[m] = ldk(kaddr + 64 * (P) + 512 * m); XF[m] = ldx(xaddr + 64 * (P) + 512 * (7 - m)); }
#define MM20(K, X) \
    MFMA(K[4], X[3], acc[7]); MFMA(K[4], X[2], acc[6]); MFMA(K[3], X[4], a7b);    MFMA(K[4], X[1], acc[5]); \
    MFMA(K[5], X[2], acc[7]); MFMA(K[2], X[4], acc[6]); MFMA(K[2], X[5], a7b);    MFMA(K[4], X[0], acc[4]); \
    MFMA(K[6], X[1], acc[7]); MFMA(K[5], X[1], acc[6]); MFMA(K[1], X[6], a7b);    MFMA(K[1], X[4], acc[5]); \
    MFMA(K[7], X[0], acc[7]); MFMA(K[1], X[5], acc[6]); MFMA(K[0], X[7], a7b);    MFMA(K[5], X[0], acc[5]); \
    MFMA(K[6], X[0], acc[6]); MFMA(K[0], X[4], acc[4]); MFMA(K[0], X[6], acc[6]); MFMA(K[0], X[5], acc[5]);
#define PAT(nv) _Pragma("unroll") for (int i_ = 0; i_ < 13; ++i_) { SG_MF(1); SG_DS(2); SG_VA(nv); } \
                _Pragma("unroll") for (int i_ = 0; i_ < 7; ++i_) { SG_MF(1); SG_VA(nv); } SB();

__global__ __launch_bounds__(512) void conv_kernel(
    const uint16_t* __restrict__ xw, const uint16_t* __restrict__ Kw, const uint16_t* __restrict__ ssmk,
    const uint16_t* __restrict__ qin, const uint16_t* __restrict__ Dsk, const uint16_t* __restrict__ sD,
    uint16_t* __restrict__ out)
{
    __shared__ __attribute__((aligned(16))) char lds[CONV_LDS];
    const int b = blockIdx.x;
    const int xcd = b & 7, idx = b >> 3, part = idx >> 4, grp = xcd * 16 + (idx & 15);
    const int lane = threadIdx.x & 63;
    const int w = __builtin_amdgcn_readfirstlane(threadIdx.x >> 6);
    const int chl = grp * 8 + w;
    const uint16_t* ksrc = (part ? ssmk : Kw) + (size_t)chl * LSEQ;
    const uint16_t* xsrc = xw + (size_t)(part * DCH + chl) * LSEQ;
    char* cb = lds + w * CH_STRIDE;

    STAMP(1, 0);
    const u32x4 k0 = *(const u32x4*)(ksrc + 8 * lane);
    SB();
    const u32x4 x3 = *(const u32x4*)(xsrc + 8 * (192 + lane));
    SB();
    const u32x4 k1 = *(const u32x4*)(ksrc + 8 * (64 + lane));
    SB();
    const u32x4 x2 = *(const u32x4*)(xsrc + 8 * (128 + lane));
    SB();

    const int g = lane >> 4, fr = lane & 15;
    {
        const u32x4 z = {0, 0, 0, 0};
        if (lane < 29) {
            *(u32x4*)(cb + KA_OFF + 16 * lane) = z;
            *(u32x4*)(cb + KB_OFF + 16 * lane) = z;
        }
        if (lane < 4) *(u32x4*)(cb + XA_OFF + 4096 + 16 * lane) = z;
        if (lane < 5) *(u32x4*)(cb + XB_OFF + 4080 + 16 * lane) = z;
    }
    put_k(cb, lane, k0, prev_lane(k0[3], 0u));
    const uint32_t c1 = __builtin_amdgcn_readlane(k0[3], 63);
    put_x(cb, 192 + lane, x3);
    put_k(cb, 64 + lane, k1, prev_lane(k1[3], c1));
    const uint32_t c2 = __builtin_amdgcn_readlane(k1[3], 63);
    put_x(cb, 128 + lane, x2);
    SB();
    const u32x4 k2 = *(const u32x4*)(ksrc + 8 * (128 + lane));
    SB();
    const u32x4 x1 = *(const u32x4*)(xsrc + 8 * (64 + lane));
    SB();
    const u32x4 k3 = *(const u32x4*)(ksrc + 8 * (192 + lane));
    SB();
    const u32x4 x0 = *(const u32x4*)(xsrc + 8 * lane);
    SB();
    const float Dv = bf2f((part ? sD : Dsk)[chl]);
    const int r0 = 1023 - (128 * w + lane);
    u32x4 qv[4];
#pragma unroll
    for (int i = 0; i < 4; ++i) qv[i] = (u32x4){0x3f803f80u, 0x3f803f80u, 0x3f803f80u, 0x3f803f80u};
    if (part) {
#pragma unroll
        for (int i = 0; i < 4; ++i)
            qv[i] = *(const u32x4*)(qin + (size_t)(r0 - 64 * (i & 1) + 1024 * (i >> 1)) * DCH + grp * 8);
    }
    SB();

    const lds_t lb = (lds_t)lds + w * CH_STRIDE;
    const lds_t kaddr = lb + ((fr & 1) ? KA_OFF : KB_OFF) + 2 * (32 * (fr >> 1) + 8 * g + 8);
    lds_t xaddr = lb + ((fr & 1) ? (XA_OFF + 2 * (1822 - 2 * fr + 8 * g)) : (XB_OFF + 2 * (1820 - 2 * fr + 8 * g))) - 3584;
    asm volatile("" : "+v"(xaddr));
    char* ys = lds + YS_OFF;
    const int lo_ = 64 * g + 2 * fr;
    const char* xa = cb + XA_OFF + 2 * (2046 - lo_);
    const int qq = (lane >> 2) & 3, pp = lane & 3;
    uint16_t* ob = out + part * DCH + grp * 8 + (size_t)r0 * (2 * DCH);

    f32x4 acc[8];
#pragma unroll
    for (int i = 0; i < 8; ++i) acc[i] = (f32x4){0, 0, 0, 0};
    f32x4 a7b = {0, 0, 0, 0};
    u32x2 xd[8];

    STAMP(1, 1);
    {
        bf16x8 ka[4], xa_[4], kb[4], xb_[4];
#pragma unroll
        for (int m = 0; m < 4; ++m) { ka[m] = ldk(kaddr + 512 * m); xa_[m] = ldx(xaddr + 512 * (7 - m)); }
#pragma unroll 1
        for (int c0 = 0; c0 < 8; c0 += 2) {
            const lds_t kp = kaddr + 64 * c0, xp = xaddr + 64 * c0;
#pragma unroll
            for (int m = 0; m < 4; ++m) { kb[m] = ldk(kp + 64 + 512 * m); xb_[m] = ldx(xp + 64 + 512 * (7 - m)); }
#pragma unroll
            for (int m = 0; m < 4; ++m)
#pragma unroll
                for (int t = 0; t < 4; ++t) MFMA(ka[m], xa_[t], acc[m + t]);
#pragma unroll
            for (int i = 0; i < 12; ++i) { SG_MF(1); SG_DS(1); }
            SG_MF(4);
            SB();
#pragma unroll
            for (int m = 0; m < 4; ++m) { ka[m] = ldk(kp + 128 + 512 * m); xa_[m] = ldx(xp + 128 + 512 * (7 - m)); }
#pragma unroll
            for (int m = 0; m < 4; ++m)
#pragma unroll
                for (int t = 0; t < 4; ++t) MFMA(kb[m], xb_[t], acc[m + t]);
#pragma unroll
            for (int i = 0; i < 12; ++i) { SG_MF(1); SG_DS(1); }
            SG_MF(4);
            SB();
        }
    }
    STAMP(1, 2);
    put_k(cb, 128 + lane, k2, prev_lane(k2[3], c2));
    const uint32_t c3 = __builtin_amdgcn_readlane(k2[3], 63);
    put_x(cb, 64 + lane, x1);
    put_k(cb, 192 + lane, k3, prev_lane(k3[3], c3));
    put_x(cb, lane, x0);
    SB();
    STAMP(1, 3);
    {
        bf16x8 ka[8], xa_[8], kb[8], xb_[8];
        LOADF(ka, xa_, 0);
#pragma unroll
        for (int tt = 0; tt < 8; ++tt) xd[tt] = (u32x2){*(const uint32_t*)(xa - 512 * tt), *(const uint32_t*)(xa - 512 * tt - 64)};
        SB();
        LOADF(kb, xb_, 1);
        epi_acc(acc[0], xd[0], ys + w * YS_STRIDE + 2 * (1022 - lo_), Dv);
        epi_acc(acc[1], xd[1], ys + w * YS_STRIDE + 2 * (1022 - lo_) - 512, Dv);
        MM20(ka, xa_);
        PAT(3);
        LOADF(ka, xa_, 2);
        epi_acc(acc[2], xd[2], ys + w * YS_STRIDE + 2 * (1022 - lo_) - 1024, Dv);
        epi_acc(acc[3], xd[3], ys + w * YS_STRIDE + 2 * (1022 - lo_) - 1536, Dv);
        MM20(kb, xb_);
        PAT(3);
        lds_barrier();
        SB();
        LOADF(kb, xb_, 3);
        epi_row(ys + qq * YS_STRIDE + (128 * w + 16 * g + 4 * pp) * 2, 4 * YS_STRIDE, qv[0], ob);
        MM20(ka, xa_);
        PAT(2);
        LOADF(ka, xa_, 4);
        epi_row(ys + qq * YS_STRIDE + (128 * w + 64 + 16 * g + 4 * pp) * 2, 4 * YS_STRIDE, qv[1], ob - (size_t)64 * (2 * DCH));
        MM20(kb, xb_);
        PAT(2);
        LOADF(kb, xb_, 5);
        MM20(ka, xa_);
        PAT(0);
        LOADF(ka, xa_, 6);
        MM20(kb, xb_);
        PAT(0);
        LOADF(kb, xb_, 7);
        MM20(ka, xa_);
        PAT(0);
        MM20(kb, xb_);
        SB();
        acc[7] += a7b;
    }
    STAMP(1, 4);
#pragma unroll
    for (int tt = 4; tt < 8; ++tt) epi_acc(acc[tt], xd[tt], cb + KA_OFF + 2 * (1022 - lo_) - 512 * (tt - 4), Dv);
    lds_barrier();
    STAMP(1, 5);
    epi_row(lds + qq * CH_STRIDE + KA_OFF + (128 * w + 16 * g + 4 * pp) * 2, 4 * CH_STRIDE, qv[2], ob + (size_t)1024 * (2 * DCH));
    epi_row(lds + qq * CH_STRIDE + KA_OFF + (128 * w + 64 + 16 * g + 4 * pp) * 2, 4 * CH_STRIDE, qv[3], ob + (size_t)960 * (2 * DCH));
    STAMP(1, 6);
    STAMPW(1, 7);
}

extern "C" void kernel_launch(void* const* d_in, const int* in_sizes, int n_in,
                              void* d_out, int out_size, void* d_ws, size_t ws_size,
                              hipStream_t stream) {
    const uint16_t* u = (const uint16_t*)d_in[0];
    const uint16_t* logA = (const uint16_t*)d_in[1];
    const uint16_t* Aim = (const uint16_t*)d_in[2];
    const uint16_t* Cre = (const uint16_t*)d_in[3];
    const uint16_t* Cim = (const uint16_t*)d_in[4];
    const uint16_t* logdt = (const uint16_t*)d_in[5];
    const uint16_t* Dsk = (const uint16_t*)d_in[6];
    const uint16_t* q = (const uint16_t*)d_in[7];
    const uint16_t* k = (const uint16_t*)d_in[8];
    const uint16_t* v = (const uint16_t*)d_in[9];
    const uint16_t* shk = (const uint16_t*)d_in[10];
    const uint16_t* ssmk = (const uint16_t*)d_in[11];
    const uint16_t* sD = (const uint16_t*)d_in[12];
    uint16_t* xw = (uint16_t*)d_ws;
    uint16_t* Kw = xw + (size_t)2 * DCH * LSEQ;
    prep_kernel<<<256, 512, 0, stream>>>(u, logA, Aim, Cre, Cim, logdt, k, v, shk, xw, Kw);
    conv_kernel<<<256, 512, 0, stream>>>(xw, Kw, ssmk, q, Dsk, sD, (uint16_t*)d_out);
}
```

```cpp
#include <hip/hip_runtime.h>
#include <stdint.h>

typedef __attribute__((ext_vector_type(4))) unsigned int u32x4;
typedef __attribute__((ext_vector_type(2))) unsigned int u32x2;
typedef __attribute__((ext_vector_type(4))) float f32x4;
typedef __attribute__((ext_vector_type(2))) float f32x2;
typedef __attribute__((ext_vector_type(8))) _Float16 f16x8;
typedef __attribute__((ext_vector_type(2))) _Float16 f16x2;
typedef __attribute__((ext_vector_type(8))) __bf16 bf16x8;
typedef __attribute__((ext_vector_type(2))) __bf16 bf16x2;
typedef __attribute__((ext_vector_type(4))) short s16x4;

#define STAMP(k, i)
#define STAMPW(k, i)
#define LSEQ 2048
#define DCH 1024
#define NMODE 128

__device__ __forceinline__ float bf2f(uint16_t v) { return __uint_as_float(((uint32_t)v) << 16); }
__device__ __forceinline__ float lo_f(uint32_t w) { return __uint_as_float(w << 16); }
__device__ __forceinline__ float hi_f(uint32_t w) { return __uint_as_float(w & 0xffff0000u); }
__device__ __forceinline__ uint32_t pk_bf16(float lo, float hi) {
    f32x2 x = {lo, hi};
    bf16x2 y = __builtin_convertvector(x, bf16x2);
    return __builtin_bit_cast(uint32_t, y);
}
__device__ __forceinline__ uint32_t pk_f16(float lo, float hi) {
    f32x2 x = {lo, hi};
    f16x2 y = __builtin_convertvector(x, f16x2);
    return __builtin_bit_cast(uint32_t, y);
}
__device__ __forceinline__ void rbf2(float& a, float& b) {
    uint32_t p = pk_bf16(a, b);
    a = lo_f(p);
    b = hi_f(p);
}
__device__ __forceinline__ float rbf(float a) { return lo_f(pk_bf16(a, a)); }

struct TapTab { uint16_t v[LSEQ]; };
static constexpr TapTab make_tab() {
    TapTab t{};
    for (int j = 0; j < LSEQ; ++j) {
        int p = j;
        if (j >= 256) {
            int s = (j < 512) ? 1 : ((j < 1024) ? 2 : 3);
            int q = j >> s, r = j & ((1 << s) - 1), half = 1 << (s - 1);
            if (r > half || (r == half && (q & 1))) q++;
            p = q << s;
        }
        int col = (p >> 3) & 15, a = p >> 7, low = p & 7, row = 0;
        if (p < 256) row = 8 * a + low;
        else if (p < 512) row = 16 + 4 * (a - 2) + (low >> 1);
        else if (p < 1024) row = 24 + 2 * (a - 4) + (low >> 2);
        else row = 32 + (a - 8);
        t.v[j] = (uint16_t)(row * 16 + col);
    }
    return t;
}
__device__ const TapTab g_taptab = make_tab();

__device__ __forceinline__ uint32_t prefetch_code(int bytes) {
    uint64_t pc;
    asm volatile("s_getpc_b64 %0" : "=s"(pc));
    const int off = (int)threadIdx.x * 64;
    typedef __attribute__((address_space(1))) const uint32_t* gp_t;
    const uint32_t v = *(gp_t)((pc & ~63ull) + (off < bytes ? off : 0));
    __builtin_amdgcn_sched_barrier(0);
    return v;
}
#define KEEP(v) asm volatile("" :: "v"(v))
__device__ __forceinline__ f32x2 cmul(f32x2 a, f32x2 b) {
    f32x2 t, c;
    asm("v_pk_mul_f32 %0, %1, %2 op_sel:[0,0] op_sel_hi:[0,1]" : "=v"(t) : "v"(a), "v"(b));
    asm("v_pk_fma_f32 %0, %1, %2, %3 op_sel:[1,1,0] op_sel_hi:[1,0,1] neg_lo:[1,0,0]" : "=v"(c) : "v"(a), "v"(b), "v"(t));
    return c;
}
__device__ __forceinline__ f32x2 anchor(float a, float b) {
    const float e = __builtin_amdgcn_exp2f(a);
    const float th = __builtin_amdgcn_fractf(b);
    return (f32x2){e * __builtin_amdgcn_cosf(th), e * __builtin_amdgcn_sinf(th)};
}
__device__ __forceinline__ uint32_t pk16(f32x2 c) { return pk_f16(c[0], c[1]); }

#define KG_ROWB 272
#define KG_TAB (64 * KG_ROWB)
#define KG_WAVE (KG_TAB + 1536)

__global__ __launch_bounds__(512) void prep_kernel(
    const uint16_t* __restrict__ u, const uint16_t* __restrict__ logA, const uint16_t* __restrict__ Aim,
    const uint16_t* __restrict__ Cre, const uint16_t* __restrict__ Cim, const uint16_t* __restrict__ logdt,
    const uint16_t* __restrict__ kin, const uint16_t* __restrict__ vin, const uint16_t* __restrict__ shk,
    uint16_t* __restrict__ xw, uint16_t* __restrict__ Kw)
{
    __shared__ __attribute__((aligned(16))) char lds[4 * KG_WAVE];
    const int lane = threadIdx.x & 63;
    const int w = __builtin_amdgcn_readfirstlane(threadIdx.x >> 6);
    const int b = blockIdx.x;
    const int xcd = b & 7, jb = b >> 3;
    STAMP(0, 0);
    const uint32_t pf = prefetch_code(10240);

    if (w < 4) {
        const int cg = lane & 15, rb = lane >> 4;
        const int s0 = 64 * jb + 32 * (w >> 1) + 8 * rb, c0 = 128 * xcd + 64 * (w & 1) + 4 * cg;
        u32x2 ur[8], kr[11], vr[8];
        u32x4 wv[2];
#pragma unroll
        for (int i = 0; i < 11; ++i) {
            const int s = s0 + i - 3;
            kr[i] = *(const u32x2*)(kin + (size_t)(s < 0 ? 0 : s) * DCH + c0);
            if (i < 3 && s < 0) kr[i] = (u32x2){0, 0};
        }
#pragma unroll
        for (int i = 0; i < 2; ++i) wv[i] = *(const u32x4*)(shk + (size_t)c0 * 4 + 8 * i);
#pragma unroll
        for (int i = 0; i < 8; ++i) vr[i] = *(const u32x2*)(vin + (size_t)(s0 + i) * DCH + c0);
#pragma unroll
        for (int i = 0; i < 8; ++i) ur[i] = *(const u32x2*)(u + (size_t)(s0 + i) * DCH + c0);
#pragma unroll
        for (int ch = 0; ch < 4; ++ch) {
            const uint32_t wlo = wv[ch >> 1][(ch & 1) * 2], whi = wv[ch >> 1][(ch & 1) * 2 + 1];
            const float w0 = lo_f(wlo), w1 = hi_f(wlo), w2 = lo_f(whi), w3 = hi_f(whi);
            float kf[11], vf[8];
#pragma unroll
            for (int i = 0; i < 11; ++i) kf[i] = (ch & 1) ? hi_f(kr[i][ch >> 1]) : lo_f(kr[i][ch >> 1]);
#pragma unroll
            for (int i = 0; i < 8; ++i) vf[i] = (ch & 1) ? hi_f(vr[i][ch >> 1]) : lo_f(vr[i][ch >> 1]);
            u32x4 o;
#pragma unroll
            for (int q = 0; q < 4; ++q) {
                const int ia = 7 - 2 * q, ib = 6 - 2 * q;
                float a0 = kf[ia] * w3, b0 = kf[ib] * w3;
                rbf2(a0, b0);
                float a1 = kf[ia + 1] * w2, b1 = kf[ib + 1] * w2;
                rbf2(a1, b1);
                a0 += a1; b0 += b1;
                rbf2(a0, b0);
                a1 = kf[ia + 2] * w1; b1 = kf[ib + 2] * w1;
                rbf2(a1, b1);
                a0 += a1; b0 += b1;
                rbf2(a0, b0);
                a1 = kf[ia + 3] * w0; b1 = kf[ib + 3] * w0;
                rbf2(a1, b1);
                a0 += a1; b0 += b1;
                rbf2(a0, b0);
                o[q] = pk_bf16(a0 * vf[ia], b0 * vf[ib]);
            }
            *(u32x4*)(xw + (size_t)(DCH + c0 + ch) * LSEQ + (2040 - s0)) = o;
        }
#pragma unroll
        for (int ch = 0; ch < 4; ++ch) {
            u32x4 o;
#pragma unroll
            for (int q = 0; q < 4; ++q)
                o[q] = __builtin_amdgcn_perm(ur[6 - 2 * q][ch >> 1], ur[7 - 2 * q][ch >> 1],
                                             (ch & 1) ? 0x07060302u : 0x05040100u);
            *(u32x4*)(xw + (size_t)(c0 + ch) * LSEQ + (2040 - s0)) = o;
        }
        STAMP(0, 1);
        STAMPW(0, 2);
        KEEP(pf);
        return;
    }

    __builtin_amdgcn_s_setprio(3);
    const int d = 128 * xcd + 4 * jb + (w - 4);
    char* T = lds + (w - 4) * KG_WAVE;
    u32x4 tabv[4];
#pragma unroll
    for (int i = 0; i < 4; ++i) tabv[i] = *(const u32x4*)(g_taptab.v + 8 * (64 * i + lane));

    const float dt = rbf(expf(bf2f(logdt[d])));
    float pa[2], pb[2], pcr[2], pci[2];
    float mx = 0.f;
#pragma unroll
    for (int bt = 0; bt < 2; ++bt) {
        const int n = d * NMODE + bt * 64 + lane;
        const float ar = -rbf(expf(bf2f(logA[n])));
        pa[bt] = (ar * dt) * 1.4426950408889634f;
        pb[bt] = (bf2f(Aim[n]) * dt) * 0.15915494309189535f;
        pcr[bt] = 2.f * (bf2f(Cre[n]) * dt);
        pci[bt] = 2.f * (bf2f(Cim[n]) * dt);
        mx = fmaxf(mx, fmaxf(fabsf(pcr[bt]), fabsf(pci[bt])));
    }
#pragma unroll
    for (int o = 32; o >= 1; o >>= 1) mx = fmaxf(mx, __shfl_xor(mx, o));
    int sf = 266 - (int)((__float_as_uint(mx) >> 23) & 0xff);
    sf = sf < 1 ? 1 : (sf > 239 ? 239 : sf);
    const float scl = __uint_as_float((uint32_t)sf << 23);
    const float inv = __uint_as_float((uint32_t)(240 - sf) << 23);

    STAMP(0, 1);
#pragma unroll
    for (int r = 41; r < 48; ++r) *(uint32_t*)(T + r * KG_ROWB + lane * 4) = 0u;

    f32x4 acc[3] = {{0, 0, 0, 0}, {0, 0, 0, 0}, {0, 0, 0, 0}};
    const int fr = lane & 15, kb = lane >> 4;
#pragma unroll 1
    for (int bt = 0; bt < 2; ++bt) {
        const float a = bt ? pa[1] : pa[0], bb = bt ? pb[1] : pb[0];
        const f32x2 z1 = anchor(a, bb), z8c = anchor(a * 8.f, bb * -8.f), z128 = anchor(a * 128.f, bb * 128.f);
        char* Tl = T + lane * 4;
        const f32x2 z16c = cmul(z8c, z8c), z256 = cmul(z128, z128), h2 = cmul(z1, z1);
        const f32x2 z32c = cmul(z16c, z16c), z512 = cmul(z256, z256), h4 = cmul(h2, h2);
        const f32x2 z64c = cmul(z32c, z32c), z1024 = cmul(z512, z512), h3 = cmul(h2, z1);
        f32x2 f[16], cg[17], h[8];
        f[0] = (f32x2){16384.f, 0.f};
        f[1] = z8c * 16384.f;
        f[2] = z16c * 16384.f;
        f[4] = z32c * 16384.f;
        f[8] = z64c * 16384.f;
        cg[0] = (f32x2){(bt ? pcr[1] : pcr[0]) * scl, (bt ? pci[1] : pci[0]) * scl};
        cg[1] = cmul(cg[0], z128);
        cg[2] = cmul(cg[0], z256);
        cg[4] = cmul(cg[0], z512);
        cg[8] = cmul(cg[0], z1024);
        h[1] = z1; h[2] = h2; h[3] = h3; h[4] = h4;
        h[5] = cmul(h4, z1); h[6] = cmul(h4, h2); h[7] = cmul(h4, h3);
        f[3] = cmul(f[2], z8c);
        cg[3] = cmul(cg[2], z128);
        cg[16] = cmul(cg[8], z1024);
#pragma unroll
        for (int i = 1; i < 4; ++i) { f[4 + i] = cmul(f[i], z32c); cg[4 + i] = cmul(cg[i], z512); }
#pragma unroll
        for (int i = 1; i < 8; ++i) { f[8 + i] = cmul(f[i], z64c); cg[8 + i] = cmul(cg[i], z1024); }
#pragma unroll
        for (int i = 0; i < 16; ++i) *(uint32_t*)(Tl + (48 + i) * KG_ROWB) = pk16(f[i]);
#pragma unroll
        for (int aa = 0; aa <= 16; ++aa) {
            if (aa < 2) {
                *(uint32_t*)(Tl + (8 * aa) * KG_ROWB) = pk16(cg[aa]);
#pragma unroll
                for (int e = 1; e < 8; ++e) *(uint32_t*)(Tl + (8 * aa + e) * KG_ROWB) = pk16(cmul(cg[aa], h[e]));
            } else if (aa < 4) {
                *(uint32_t*)(Tl + (16 + 4 * (aa - 2)) * KG_ROWB) = pk16(cg[aa]);
#pragma unroll
                for (int e = 1; e < 4; ++e) *(uint32_t*)(Tl + (16 + 4 * (aa - 2) + e) * KG_ROWB) = pk16(cmul(cg[aa], h[2 * e]));
            } else if (aa < 8) {
                *(uint32_t*)(Tl + (24 + 2 * (aa - 4)) * KG_ROWB) = pk16(cg[aa]);
                *(uint32_t*)(Tl + (24 + 2 * (aa - 4) + 1) * KG_ROWB) = pk16(cmul(cg[aa], h[4]));
            } else {
                *(uint32_t*)(Tl + (32 + (aa - 8)) * KG_ROWB) = pk16(cg[aa]);
            }
        }
        f16x8 bfr[4], afr[4][3];
#pragma unroll
        for (int ks = 0; ks < 4; ++ks) {
            bfr[ks] = *(const f16x8*)(T + (48 + fr) * KG_ROWB + ks * 64 + kb * 16);
#pragma unroll
            for (int rt = 0; rt < 3; ++rt) afr[ks][rt] = *(const f16x8*)(T + (16 * rt + fr) * KG_ROWB + ks * 64 + kb * 16);
        }
        __builtin_amdgcn_sched_barrier(0);
#pragma unroll
        for (int ks = 0; ks < 4; ++ks)
#pragma unroll
            for (int rt = 0; rt < 3; ++rt)
                acc[rt] = __builtin_amdgcn_mfma_f32_16x16x32_f16(afr[ks][rt], bfr[ks], acc[rt], 0, 0, 0);
    }
    STAMP(0, 2);
    uint16_t* KD = (uint16_t*)(T + KG_TAB);
#pragma unroll
    for (int rt = 0; rt < 3; ++rt)
#pragma unroll
        for (int r = 0; r < 4; r += 2) {
            uint32_t p = pk_bf16(acc[rt][r] * inv, acc[rt][r + 1] * inv);
            KD[(16 * rt + 4 * kb + r) * 16 + fr] = (uint16_t)p;
            KD[(16 * rt + 4 * kb + r + 1) * 16 + fr] = (uint16_t)(p >> 16);
        }
#pragma unroll
    for (int i = 0; i < 4; ++i) {
        u32x4 o;
#pragma unroll
        for (int q = 0; q < 4; ++q) {
            const uint32_t lo = KD[tabv[i][q] & 0xffffu], hi = KD[tabv[i][q] >> 16];
            o[q] = lo | (hi << 16);
        }
        *(u32x4*)(Kw + (size_t)d * LSEQ + 8 * (64 * i + lane)) = o;
    }
    STAMP(0, 3);
    STAMPW(0, 4);
    KEEP(pf);
}

#define KA_OFF 0
#define KB_OFF 4640
#define XA_OFF 9216
#define XB_OFF 13440
#define CH_STRIDE 17600
#define YS_OFF (8 * CH_STRIDE)
#define YS_STRIDE 2112
#define CONV_LDS (YS_OFF + 8 * YS_STRIDE)

__device__ __forceinline__ void put_k(char* cb, int y, u32x4 d, uint32_t prev) {
    *(u32x4*)(cb + KA_OFF + 464 + 16 * y) = d;
    u32x4 o;
    o[0] = __builtin_amdgcn_alignbyte(d[0], prev, 2);
    o[1] = __builtin_amdgcn_alignbyte(d[1], d[0], 2);
    o[2] = __builtin_amdgcn_alignbyte(d[2], d[1], 2);
    o[3] = __builtin_amdgcn_alignbyte(d[3], d[2], 2);
    *(u32x4*)(cb + KB_OFF + 464 + 16 * y) = o;
}
__device__ __forceinline__ void put_x(char* cb, int y, u32x4 d) {
    *(u32x4*)(cb + XA_OFF + 16 * y) = d;
    *(uint32_t*)(cb + XB_OFF + 16 * y - 4) = d[0];
    u32x2 m = {d[1], d[2]};
    *(u32x2*)(cb + XB_OFF + 16 * y) = m;
    *(uint32_t*)(cb + XB_OFF + 16 * y + 8) = d[3];
}
__device__ __forceinline__ uint32_t prev_lane(uint32_t v, uint32_t carry) {
    return (uint32_t)__builtin_amdgcn_update_dpp((int)carry, (int)v, 0x138  , 0xf, 0xf, false);
}
typedef __attribute__((address_space(3))) char* lds_t;
typedef __attribute__((address_space(3))) const volatile u32x2* lds_v2;
typedef __attribute__((address_space(3))) const bf16x8* lds_k8;
__device__ __forceinline__ bf16x8 ldx(lds_t p) {
    u32x2 a = *(lds_v2)p, b = *(lds_v2)(p + 8);
    u32x4 v = {a[0], a[1], b[0], b[1]};
    return __builtin_bit_cast(bf16x8, v);
}
__device__ __forceinline__ bf16x8 ldk(lds_t p) { return *(lds_k8)p; }
#define MFMA(a, b, c) c = __builtin_amdgcn_mfma_f32_16x16x32_bf16(a, b, c, 0, 0, 0)
#define SB() __builtin_amdgcn_sched_barrier(0)
#define SG_DS(n) __builtin_amdgcn_sched_group_barrier(0x100, n, 0)
#define SG_MF(n) __builtin_amdgcn_sched_group_barrier(0x008, n, 0)
#define ST2_OFF 1488

__device__ __forceinline__ void epi_acc(const f32x4 a, const u32x2 xd2, char* dst, float Dv) {
#pragma unroll
    for (int hh = 0; hh < 2; ++hh) {
        const uint32_t xd = xd2[hh];
        float y1 = a[2 * hh + 1], y0 = a[2 * hh];
        rbf2(y1, y0);
        float t1 = lo_f(xd) * Dv, t0 = hi_f(xd) * Dv;
        rbf2(t1, t0);
        *(uint32_t*)(dst - 64 * hh) = pk_bf16(y1 + t1, y0 + t0);
    }
}
__device__ __forceinline__ void epi_row(const char* a0, int stride4, u32x4 qq4, uint16_t* dst) {
    s16x4 v0 = __builtin_amdgcn_ds_read_tr16_b64_v4i16((__attribute__((address_space(3))) s16x4*)(a0));
    s16x4 v1 = __builtin_amdgcn_ds_read_tr16_b64_v4i16((__attribute__((address_space(3))) s16x4*)(a0 + stride4));
    u32x2 p0 = __builtin_bit_cast(u32x2, v0), p1 = __builtin_bit_cast(u32x2, v1);
    u32x4 o = {p0[0], p0[1], p1[0], p1[1]};
#pragma unroll
    for (int j = 0; j < 4; ++j)
        o[j] = pk_bf16(lo_f(qq4[j]) * lo_f(o[j]), hi_f(qq4[j]) * hi_f(o[j]));
    *(u32x4*)dst = o;
}

#define SG_VA(n) __builtin_amdgcn_sched_group_barrier(0x002, n, 0)
__device__ __forceinline__ void lds_barrier() {
    __builtin_amdgcn_fence(__ATOMIC_RELEASE, "workgroup", "local");
    __builtin_amdgcn_s_barrier();
    __builtin_amdgcn_fence(__ATOMIC_ACQUIRE, "workgroup", "local");
}
#define LOADF(KF, XF, P) _Pragma("unroll") for (int m = 0; m < 8; ++m) { KF[m] = ldk(kaddr + 64 * (P) + 512 * m); XF[m] = ldx(xaddr + 64 * (P) + 512 * (7 - m)); }
#define MM20(K, X) \
    MFMA(K[4], X[3], acc[7]); MFMA(K[4], X[2], acc[6]); MFMA(K[3], X[4], a7b);    MFMA(K[4], X[1], acc[5]); \
    MFMA(K[5], X[2], acc[7]); MFMA(K[2], X[4], acc[6]); MFMA(K[2], X[5], a7b);    MFMA(K[4], X[0], acc[4]); \
    MFMA(K[6], X[1], acc[7]); MFMA(K[5], X[1], acc[6]); MFMA(K[1], X[6], a7b);    MFMA(K[1], X[4], acc[5]); \
    MFMA(K[7], X[0], acc[7]); MFMA(K[1], X[5], acc[6]); MFMA(K[0], X[7], a7b);    MFMA(K[5], X[0], acc[5]); \
    MFMA(K[6], X[0], acc[6]); MFMA(K[0], X[4], acc[4]); MFMA(K[0], X[6], acc[6]); MFMA(K[0], X[5], acc[5]);
#define PAT(nv) _Pragma("unroll") for (int i_ = 0; i_ < 13; ++i_) { SG_MF(1); SG_DS(2); SG_VA(nv); } \
                _Pragma("unroll") for (int i_ = 0; i_ < 7; ++i_) { SG_MF(1); SG_VA(nv); } SB();

__global__ __launch_bounds__(512) void conv_kernel(
    const uint16_t* __restrict__ xw, const uint16_t* __restrict__ Kw, const uint16_t* __restrict__ ssmk,
    const uint16_t* __restrict__ qin, const uint16_t* __restrict__ Dsk, const uint16_t* __restrict__ sD,
    uint16_t* __restrict__ out)
{
    __shared__ __attribute__((aligned(16))) char lds[CONV_LDS];
    const int b = blockIdx.x;
    const int xcd = b & 7, idx = b >> 3, part = idx >> 4, grp = xcd * 16 + (idx & 15);
    const int lane = threadIdx.x & 63;
    const int w = __builtin_amdgcn_readfirstlane(threadIdx.x >> 6);
    const int chl = grp * 8 + w;
    const uint16_t* ksrc = (part ? ssmk : Kw) + (size_t)chl * LSEQ;
    const uint16_t* xsrc = xw + (size_t)(part * DCH + chl) * LSEQ;
    char* cb = lds + w * CH_STRIDE;

    STAMP(1, 0);
    const uint32_t pf = prefetch_code(7168);
    const u32x4 k0 = *(const u32x4*)(ksrc + 8 * lane);
    SB();
    const u32x4 x3 = *(const u32x4*)(xsrc + 8 * (192 + lane));
    SB();
    const u32x4 k1 = *(const u32x4*)(ksrc + 8 * (64 + lane));
    SB();
    const u32x4 x2 = *(const u32x4*)(xsrc + 8 * (128 + lane));
    SB();

    const int g = lane >> 4, fr = lane & 15;
    {
        const u32x4 z = {0, 0, 0, 0};
        if (lane < 29) {
            *(u32x4*)(cb + KA_OFF + 16 * lane) = z;
            *(u32x4*)(cb + KB_OFF + 16 * lane) = z;
        }
        if (lane < 4) *(u32x4*)(cb + XA_OFF + 4096 + 16 * lane) = z;
        if (lane < 5) *(u32x4*)(cb + XB_OFF + 4080 + 16 * lane) = z;
    }
    put_k(cb, lane, k0, prev_lane(k0[3], 0u));
    const uint32_t c1 = __builtin_amdgcn_readlane(k0[3], 63);
    put_x(cb, 192 + lane, x3);
    put_k(cb, 64 + lane, k1, prev_lane(k1[3], c1));
    const uint32_t c2 = __builtin_amdgcn_readlane(k1[3], 63);
    put_x(cb, 128 + lane, x2);
    SB();
    const u32x4 k2 = *(const u32x4*)(ksrc + 8 * (128 + lane));
    SB();
    const u32x4 x1 = *(const u32x4*)(xsrc + 8 * (64 + lane));
    SB();
    const u32x4 k3 = *(const u32x4*)(ksrc + 8 * (192 + lane));
    SB();
    const u32x4 x0 = *(const u32x4*)(xsrc + 8 * lane);
    SB();
    const float Dv = bf2f((part ? sD : Dsk)[chl]);
    const int r0 = 1023 - (128 * w + lane);
    u32x4 qv[4];
#pragma unroll
    for (int i = 0; i < 4; ++i) qv[i] = (u32x4){0x3f803f80u, 0x3f803f80u, 0x3f803f80u, 0x3f803f80u};
    if (part) {
#pragma unroll
        for (int i = 0; i < 4; ++i)
            qv[i] = *(const u32x4*)(qin + (size_t)(r0 - 64 * (i & 1) + 1024 * (i >> 1)) * DCH + grp * 8);
    }
    SB();

    const lds_t lb = (lds_t)lds + w * CH_STRIDE;
    const lds_t kaddr = lb + ((fr & 1) ? KA_OFF : KB_OFF) + 2 * (32 * (fr >> 1) + 8 * g + 8);
    lds_t xaddr = lb + ((fr & 1) ? (XA_OFF + 2 * (1822 - 2 * fr + 8 * g)) : (XB_OFF + 2 * (1820 - 2 * fr + 8 * g))) - 3584;
    asm volatile("" : "+v"(xaddr));
    char* ys = lds + YS_OFF;
    const int lo_ = 64 * g + 2 * fr;
    const char* xa = cb + XA_OFF + 2 * (2046 - lo_);
    const int qq = (lane >> 2) & 3, pp = lane & 3;
    uint16_t* ob = out + part * DCH + grp * 8 + (size_t)r0 * (2 * DCH);

    f32x4 acc[8];
#pragma unroll
    for (int i = 0; i < 8; ++i) acc[i] = (f32x4){0, 0, 0, 0};
    f32x4 a7b = {0, 0, 0, 0};
    u32x2 xd[8];

    STAMP(1, 1);
    {
        bf16x8 ka[4], xa_[4], kb[4], xb_[4];
#pragma unroll
        for (int m = 0; m < 4; ++m) { ka[m] = ldk(kaddr + 512 * m); xa_[m] = ldx(xaddr + 512 * (7 - m)); }
#pragma unroll 1
        for (int c0 = 0; c0 < 8; c0 += 2) {
            const lds_t kp = kaddr + 64 * c0, xp = xaddr + 64 * c0;
#pragma unroll
            for (int m = 0; m < 4; ++m) { kb[m] = ldk(kp + 64 + 512 * m); xb_[m] = ldx(xp + 64 + 512 * (7 - m)); }
#pragma unroll
            for (int m = 0; m < 4; ++m)
#pragma unroll
                for (int t = 0; t < 4; ++t) MFMA(ka[m], xa_[t], acc[m + t]);
#pragma unroll
            for (int i = 0; i < 12; ++i) { SG_MF(1); SG_DS(1); }
            SG_MF(4);
            SB();
#pragma unroll
            for (int m = 0; m < 4; ++m) { ka[m] = ldk(kp + 128 + 512 * m); xa_[m] = ldx(xp + 128 + 512 * (7 - m)); }
#pragma unroll
            for (int m = 0; m < 4; ++m)
#pragma unroll
                for (int t = 0; t < 4; ++t) MFMA(kb[m], xb_[t], acc[m + t]);
#pragma unroll
            for (int i = 0; i < 12; ++i) { SG_MF(1); SG_DS(1); }
            SG_MF(4);
            SB();
        }
    }
    STAMP(1, 2);
    put_k(cb, 128 + lane, k2, prev_lane(k2[3], c2));
    const uint32_t c3 = __builtin_amdgcn_readlane(k2[3], 63);
    put_x(cb, 64 + lane, x1);
    put_k(cb, 192 + lane, k3, prev_lane(k3[3], c3));
    put_x(cb, lane, x0);
    SB();
    STAMP(1, 3);
    {
        bf16x8 ka[8], xa_[8], kb[8], xb_[8];
        LOADF(ka, xa_, 0);
#pragma unroll
        for (int tt = 0; tt < 8; ++tt) xd[tt] = (u32x2){*(const uint32_t*)(xa - 512 * tt), *(const uint32_t*)(xa - 512 * tt - 64)};
        SB();
        LOADF(kb, xb_, 1);
        epi_acc(acc[0], xd[0], ys + w * YS_STRIDE + 2 * (1022 - lo_), Dv);
        epi_acc(acc[1], xd[1], ys + w * YS_STRIDE + 2 * (1022 - lo_) - 512, Dv);
        MM20(ka, xa_);
        PAT(3);
        LOADF(ka, xa_, 2);
        epi_acc(acc[2], xd[2], ys + w * YS_STRIDE + 2 * (1022 - lo_) - 1024, Dv);
        epi_acc(acc[3], xd[3], ys + w * YS_STRIDE + 2 * (1022 - lo_) - 1536, Dv);
        MM20(kb, xb_);
        PAT(3);
        lds_barrier();
        SB();
        LOADF(kb, xb_, 3);
        epi_row(ys + qq * YS_STRIDE + (128 * w + 16 * g + 4 * pp) * 2, 4 * YS_STRIDE, qv[0], ob);
        MM20(ka, xa_);
        PAT(2);
        LOADF(ka, xa_, 4);
        epi_row(ys + qq * YS_STRIDE + (128 * w + 64 + 16 * g + 4 * pp) * 2, 4 * YS_STRIDE, qv[1], ob - (size_t)64 * (2 * DCH));
        MM20(kb, xb_);
        PAT(2);
        LOADF(kb, xb_, 5);
        MM20(ka, xa_);
        PAT(0);
        LOADF(ka, xa_, 6);
        MM20(kb, xb_);
        PAT(0);
        LOADF(kb, xb_, 7);
        MM20(ka, xa_);
        PAT(0);
        MM20(kb, xb_);
        SB();
        acc[7] += a7b;
    }
    STAMP(1, 4);
#pragma unroll
    for (int tt = 4; tt < 8; ++tt) epi_acc(acc[tt], xd[tt], cb + KA_OFF + 2 * (1022 - lo_) - 512 * (tt - 4), Dv);
    lds_barrier();
    STAMP(1, 5);
    epi_row(lds + qq * CH_STRIDE + KA_OFF + (128 * w + 16 * g + 4 * pp) * 2, 4 * CH_STRIDE, qv[2], ob + (size_t)1024 * (2 * DCH));
    epi_row(lds + qq * CH_STRIDE + KA_OFF + (128 * w + 64 + 16 * g + 4 * pp) * 2, 4 * CH_STRIDE, qv[3], ob + (size_t)960 * (2 * DCH));
    STAMP(1, 6);
    STAMPW(1, 7);
    KEEP(pf);
}

extern "C" void kernel_launch(void* const* d_in, const int* in_sizes, int n_in,
                              void* d_out, int out_size, void* d_ws, size_t ws_size,
                              hipStream_t stream) {
    const uint16_t* u = (const uint16_t*)d_in[0];
    const uint16_t* logA = (const uint16_t*)d_in[1];
    const uint16_t* Aim = (const uint16_t*)d_in[2];
    const uint16_t* Cre = (const uint16_t*)d_in[3];
    const uint16_t* Cim = (const uint16_t*)d_in[4];
    const uint16_t* logdt = (const uint16_t*)d_in[5];
    const uint16_t* Dsk = (const uint16_t*)d_in[6];
    const uint16_t* q = (const uint16_t*)d_in[7];
    const uint16_t* k = (const uint16_t*)d_in[8];
    const uint16_t* v = (const uint16_t*)d_in[9];
    const uint16_t* shk = (const uint16_t*)d_in[10];
    const uint16_t* ssmk = (const uint16_t*)d_in[11];
    const uint16_t* sD = (const uint16_t*)d_in[12];
    uint16_t* xw = (uint16_t*)d_ws;
    uint16_t* Kw = xw + (size_t)2 * DCH * LSEQ;
    prep_kernel<<<256, 512, 0, stream>>>(u, logA, Aim, Cre, Cim, logdt, k, v, shk, xw, Kw);
    conv_kernel<<<256, 512, 0, stream>>>(xw, Kw, ssmk, q, Dsk, sD, (uint16_t*)d_out);
}
```

```cpp
#include <hip/hip_runtime.h>
#include <stdint.h>

typedef __attribute__((ext_vector_type(4))) unsigned int u32x4;
typedef __attribute__((ext_vector_type(2))) unsigned int u32x2;
typedef __attribute__((ext_vector_type(4))) float f32x4;
typedef __attribute__((ext_vector_type(2))) float f32x2;
typedef __attribute__((ext_vector_type(8))) _Float16 f16x8;
typedef __attribute__((ext_vector_type(2))) _Float16 f16x2;
typedef __attribute__((ext_vector_type(8))) __bf16 bf16x8;
typedef __attribute__((ext_vector_type(2))) __bf16 bf16x2;
typedef __attribute__((ext_vector_type(4))) short s16x4;

#define STAMP(k, i)
#define STAMPW(k, i)
#define LSEQ 2048
#define DCH 1024
#define NMODE 128

__device__ __forceinline__ float bf2f(uint16_t v) { return __uint_as_float(((uint32_t)v) << 16); }
__device__ __forceinline__ float lo_f(uint32_t w) { return __uint_as_float(w << 16); }
__device__ __forceinline__ float hi_f(uint32_t w) { return __uint_as_float(w & 0xffff0000u); }
__device__ __forceinline__ uint32_t pk_bf16(float lo, float hi) {
    f32x2 x = {lo, hi};
    bf16x2 y = __builtin_convertvector(x, bf16x2);
    return __builtin_bit_cast(uint32_t, y);
}
__device__ __forceinline__ uint32_t pk_f16(float lo, float hi) {
    f32x2 x = {lo, hi};
    f16x2 y = __builtin_convertvector(x, f16x2);
    return __builtin_bit_cast(uint32_t, y);
}
__device__ __forceinline__ void rbf2(float& a, float& b) {
    uint32_t p = pk_bf16(a, b);
    a = lo_f(p);
    b = hi_f(p);
}
__device__ __forceinline__ float rbf(float a) { return lo_f(pk_bf16(a, a)); }

struct TapTab { uint16_t v[LSEQ]; };
static constexpr TapTab make_tab() {
    TapTab t{};
    for (int j = 0; j < LSEQ; ++j) {
        int p = j;
        if (j >= 256) {
            int s = (j < 512) ? 1 : ((j < 1024) ? 2 : 3);
            int q = j >> s, r = j & ((1 << s) - 1), half = 1 << (s - 1);
            if (r > half || (r == half && (q & 1))) q++;
            p = q << s;
        }
        int col = (p >> 3) & 15, a = p >> 7, low = p & 7, row = 0;
        if (p < 256) row = 8 * a + low;
        else if (p < 512) row = 16 + 4 * (a - 2) + (low >> 1);
        else if (p < 1024) row = 24 + 2 * (a - 4) + (low >> 2);
        else row = 32 + (a - 8);
        t.v[j] = (uint16_t)(row * 16 + col);
    }
    return t;
}
__device__ const TapTab g_taptab = make_tab();

__device__ __forceinline__ uint32_t prefetch_code(int bytes) {
    uint64_t pc;
    asm volatile("s_getpc_b64 %0" : "=s"(pc));
    const int off = (int)threadIdx.x * 64;
    typedef __attribute__((address_space(1))) const uint32_t* gp_t;
    const uint32_t v = *(gp_t)((pc & ~63ull) + (off < bytes ? off : 0));
    __builtin_amdgcn_sched_barrier(0);
    return v;
}
__device__ __forceinline__ void WST(u32x4* p, u32x4 v) { asm volatile("global_store_dwordx4 %0, %1, off sc0 sc1" :: "v"(p), "v"(v) : "memory"); }
#define KEEP(v) asm volatile("" :: "v"(v))
__device__ __forceinline__ f32x2 cmul(f32x2 a, f32x2 b) {
    f32x2 t, c;
    asm("v_pk_mul_f32 %0, %1, %2 op_sel:[0,0] op_sel_hi:[0,1]" : "=v"(t) : "v"(a), "v"(b));
    asm("v_pk_fma_f32 %0, %1, %2, %3 op_sel:[1,1,0] op_sel_hi:[1,0,1] neg_lo:[1,0,0]" : "=v"(c) : "v"(a), "v"(b), "v"(t));
    return c;
}
__device__ __forceinline__ f32x2 anchor(float a, float b) {
    const float e = __builtin_amdgcn_exp2f(a);
    const float th = __builtin_amdgcn_fractf(b);
    return (f32x2){e * __builtin_amdgcn_cosf(th), e * __builtin_amdgcn_sinf(th)};
}
__device__ __forceinline__ uint32_t pk16(f32x2 c) { return pk_f16(c[0], c[1]); }

#define KG_ROWB 272
#define KG_TAB (64 * KG_ROWB)
#define KG_WAVE (KG_TAB + 1536)

__global__ __launch_bounds__(512) void prep_kernel(
    const uint16_t* __restrict__ u, const uint16_t* __restrict__ logA, const uint16_t* __restrict__ Aim,
    const uint16_t* __restrict__ Cre, const uint16_t* __restrict__ Cim, const uint16_t* __restrict__ logdt,
    const uint16_t* __restrict__ kin, const uint16_t* __restrict__ vin, const uint16_t* __restrict__ shk,
    uint16_t* __restrict__ xw, uint16_t* __restrict__ Kw)
{
    __shared__ __attribute__((aligned(16))) char lds[4 * KG_WAVE];
    const int lane = threadIdx.x & 63;
    const int w = __builtin_amdgcn_readfirstlane(threadIdx.x >> 6);
    const int b = blockIdx.x;
    const int xcd = b & 7, jb = b >> 3;
    STAMP(0, 0);
    const uint32_t pf = prefetch_code(10240);

    if (w < 4) {
        const int cg = lane & 15, rb = lane >> 4;
        const int s0 = 64 * jb + 32 * (w >> 1) + 8 * rb, c0 = 128 * xcd + 64 * (w & 1) + 4 * cg;
        u32x2 ur[8], kr[11], vr[8];
        u32x4 wv[2];
#pragma unroll
        for (int i = 0; i < 11; ++i) {
            const int s = s0 + i - 3;
            kr[i] = *(const u32x2*)(kin + (size_t)(s < 0 ? 0 : s) * DCH + c0);
            if (i < 3 && s < 0) kr[i] = (u32x2){0, 0};
        }
#pragma unroll
        for (int i = 0; i < 2; ++i) wv[i] = *(const u32x4*)(shk + (size_t)c0 * 4 + 8 * i);
#pragma unroll
        for (int i = 0; i < 8; ++i) vr[i] = *(const u32x2*)(vin + (size_t)(s0 + i) * DCH + c0);
#pragma unroll
        for (int i = 0; i < 8; ++i) ur[i] = *(const u32x2*)(u + (size_t)(s0 + i) * DCH + c0);
#pragma unroll
        for (int ch = 0; ch < 4; ++ch) {
            const uint32_t wlo = wv[ch >> 1][(ch & 1) * 2], whi = wv[ch >> 1][(ch & 1) * 2 + 1];
            const float w0 = lo_f(wlo), w1 = hi_f(wlo), w2 = lo_f(whi), w3 = hi_f(whi);
            float kf[11], vf[8];
#pragma unroll
            for (int i = 0; i < 11; ++i) kf[i] = (ch & 1) ? hi_f(kr[i][ch >> 1]) : lo_f(kr[i][ch >> 1]);
#pragma unroll
            for (int i = 0; i < 8; ++i) vf[i] = (ch & 1) ? hi_f(vr[i][ch >> 1]) : lo_f(vr[i][ch >> 1]);
            u32x4 o;
#pragma unroll
            for (int q = 0; q < 4; ++q) {
                const int ia = 7 - 2 * q, ib = 6 - 2 * q;
                float a0 = kf[ia] * w3, b0 = kf[ib] * w3;
                rbf2(a0, b0);
                float a1 = kf[ia + 1] * w2, b1 = kf[ib + 1] * w2;
                rbf2(a1, b1);
                a0 += a1; b0 += b1;
                rbf2(a0, b0);
                a1 = kf[ia + 2] * w1; b1 = kf[ib + 2] * w1;
                rbf2(a1, b1);
                a0 += a1; b0 += b1;
                rbf2(a0, b0);
                a1 = kf[ia + 3] * w0; b1 = kf[ib + 3] * w0;
                rbf2(a1, b1);
                a0 += a1; b0 += b1;
                rbf2(a0, b0);
                o[q] = pk_bf16(a0 * vf[ia], b0 * vf[ib]);
            }
            WST((u32x4*)(xw + (size_t)(DCH + c0 + ch) * LSEQ + (2040 - s0)), o);
        }
#pragma unroll
        for (int ch = 0; ch < 4; ++ch) {
            u32x4 o;
#pragma unroll
            for (int q = 0; q < 4; ++q)
                o[q] = __builtin_amdgcn_perm(ur[6 - 2 * q][ch >> 1], ur[7 - 2 * q][ch >> 1],
                                             (ch & 1) ? 0x07060302u : 0x05040100u);
            WST((u32x4*)(xw + (size_t)(c0 + ch) * LSEQ + (2040 - s0)), o);
        }
        STAMP(0, 1);
        STAMPW(0, 2);
        KEEP(pf);
        return;
    }

    __builtin_amdgcn_s_setprio(3);
    const int d = 128 * xcd + 4 * jb + (w - 4);
    char* T = lds + (w - 4) * KG_WAVE;
    u32x4 tabv[4];
#pragma unroll
    for (int i = 0; i < 4; ++i) tabv[i] = *(const u32x4*)(g_taptab.v + 8 * (64 * i + lane));

    const float dt = rbf(expf(bf2f(logdt[d])));
    float pa[2], pb[2], pcr[2], pci[2];
    float mx = 0.f;
#pragma unroll
    for (int bt = 0; bt < 2; ++bt) {
        const int n = d * NMODE + bt * 64 + lane;
        const float ar = -rbf(expf(bf2f(logA[n])));
        pa[bt] = (ar * dt) * 1.4426950408889634f;
        pb[bt] = (bf2f(Aim[n]) * dt) * 0.15915494309189535f;
        pcr[bt] = 2.f * (bf2f(Cre[n]) * dt);
        pci[bt] = 2.f * (bf2f(Cim[n]) * dt);
        mx = fmaxf(mx, fmaxf(fabsf(pcr[bt]), fabsf(pci[bt])));
    }
#pragma unroll
    for (int o = 32; o >= 1; o >>= 1) mx = fmaxf(mx, __shfl_xor(mx, o));
    int sf = 266 - (int)((__float_as_uint(mx) >> 23) & 0xff);
    sf = sf < 1 ? 1 : (sf > 239 ? 239 : sf);
    const float scl = __uint_as_float((uint32_t)sf << 23);
    const float inv = __uint_as_float((uint32_t)(240 - sf) << 23);

    STAMP(0, 1);
#pragma unroll
    for (int r = 41; r < 48; ++r) *(uint32_t*)(T + r * KG_ROWB + lane * 4) = 0u;

    f32x4 acc[3] = {{0, 0, 0, 0}, {0, 0, 0, 0}, {0, 0, 0, 0}};
    const int fr = lane & 15, kb = lane >> 4;
#pragma unroll 1
    for (int bt = 0; bt < 2; ++bt) {
        const float a = bt ? pa[1] : pa[0], bb = bt ? pb[1] : pb[0];
        const f32x2 z1 = anchor(a, bb), z8c = anchor(a * 8.f, bb * -8.f), z128 = anchor(a * 128.f, bb * 128.f);
        char* Tl = T + lane * 4;
        const f32x2 z16c = cmul(z8c, z8c), z256 = cmul(z128, z128), h2 = cmul(z1, z1);
        const f32x2 z32c = cmul(z16c, z16c), z512 = cmul(z256, z256), h4 = cmul(h2, h2);
        const f32x2 z64c = cmul(z32c, z32c), z1024 = cmul(z512, z512), h3 = cmul(h2, z1);
        f32x2 f[16], cg[17], h[8];
        f[0] = (f32x2){16384.f, 0.f};
        f[1] = z8c * 16384.f;
        f[2] = z16c * 16384.f;
        f[4] = z32c * 16384.f;
        f[8] = z64c * 16384.f;
        cg[0] = (f32x2){(bt ? pcr[1] : pcr[0]) * scl, (bt ? pci[1] : pci[0]) * scl};
        cg[1] = cmul(cg[0], z128);
        cg[2] = cmul(cg[0], z256);
        cg[4] = cmul(cg[0], z512);
        cg[8] = cmul(cg[0], z1024);
        h[1] = z1; h[2] = h2; h[3] = h3; h[4] = h4;
        h[5] = cmul(h4, z1); h[6] = cmul(h4, h2); h[7] = cmul(h4, h3);
        f[3] = cmul(f[2], z8c);
        cg[3] = cmul(cg[2], z128);
        cg[16] = cmul(cg[8], z1024);
#pragma unroll
        for (int i = 1; i < 4; ++i) { f[4 + i] = cmul(f[i], z32c); cg[4 + i] = cmul(cg[i], z512); }
#pragma unroll
        for (int i = 1; i < 8; ++i) { f[8 + i] = cmul(f[i], z64c); cg[8 + i] = cmul(cg[i], z1024); }
#pragma unroll
        for (int i = 0; i < 16; ++i) *(uint32_t*)(Tl + (48 + i) * KG_ROWB) = pk16(f[i]);
#pragma unroll
        for (int aa = 0; aa <= 16; ++aa) {
            if (aa < 2) {
                *(uint32_t*)(Tl + (8 * aa) * KG_ROWB) = pk16(cg[aa]);
#pragma unroll
                for (int e = 1; e < 8; ++e) *(uint32_t*)(Tl + (8 * aa + e) * KG_ROWB) = pk16(cmul(cg[aa], h[e]));
            } else if (aa < 4) {
                *(uint32_t*)(Tl + (16 + 4 * (aa - 2)) * KG_ROWB) = pk16(cg[aa]);
#pragma unroll
                for (int e = 1; e < 4; ++e) *(uint32_t*)(Tl + (16 + 4 * (aa - 2) + e) * KG_ROWB) = pk16(cmul(cg[aa], h[2 * e]));
            } else if (aa < 8) {
                *(uint32_t*)(Tl + (24 + 2 * (aa - 4)) * KG_ROWB) = pk16(cg[aa]);
                *(uint32_t*)(Tl + (24 + 2 * (aa - 4) + 1) * KG_ROWB) = pk16(cmul(cg[aa], h[4]));
            } else {
                *(uint32_t*)(Tl + (32 + (aa - 8)) * KG_ROWB) = pk16(cg[aa]);
            }
        }
        f16x8 bfr[4], afr[4][3];
#pragma unroll
        for (int ks = 0; ks < 4; ++ks) {
            bfr[ks] = *(const f16x8*)(T + (48 + fr) * KG_ROWB + ks * 64 + kb * 16);
#pragma unroll
            for (int rt = 0; rt < 3; ++rt) afr[ks][rt] = *(const f16x8*)(T + (16 * rt + fr) * KG_ROWB + ks * 64 + kb * 16);
        }
        __builtin_amdgcn_sched_barrier(0);
#pragma unroll
        for (int ks = 0; ks < 4; ++ks)
#pragma unroll
            for (int rt = 0; rt < 3; ++rt)
                acc[rt] = __builtin_amdgcn_mfma_f32_16x16x32_f16(afr[ks][rt], bfr[ks], acc[rt], 0, 0, 0);
    }
    STAMP(0, 2);
    uint16_t* KD = (uint16_t*)(T + KG_TAB);
#pragma unroll
    for (int rt = 0; rt < 3; ++rt)
#pragma unroll
        for (int r = 0; r < 4; r += 2) {
            uint32_t p = pk_bf16(acc[rt][r] * inv, acc[rt][r + 1] * inv);
            KD[(16 * rt + 4 * kb + r) * 16 + fr] = (uint16_t)p;
            KD[(16 * rt + 4 * kb + r + 1) * 16 + fr] = (uint16_t)(p >> 16);
        }
#pragma unroll
    for (int i = 0; i < 4; ++i) {
        u32x4 o;
#pragma unroll
        for (int q = 0; q < 4; ++q) {
            const uint32_t lo = KD[tabv[i][q] & 0xffffu], hi = KD[tabv[i][q] >> 16];
            o[q] = lo | (hi << 16);
        }
        WST((u32x4*)(Kw + (size_t)d * LSEQ + 8 * (64 * i + lane)), o);
    }
    STAMP(0, 3);
    STAMPW(0, 4);
    KEEP(pf);
}

#define KA_OFF 0
#define KB_OFF 4640
#define XA_OFF 9216
#define XB_OFF 13440
#define CH_STRIDE 17600
#define YS_OFF (8 * CH_STRIDE)
#define YS_STRIDE 2112
#define CONV_LDS (YS_OFF + 8 * YS_STRIDE)

__device__ __forceinline__ void put_k(char* cb, int y, u32x4 d, uint32_t prev) {
    *(u32x4*)(cb + KA_OFF + 464 + 16 * y) = d;
    u32x4 o;
    o[0] = __builtin_amdgcn_alignbyte(d[0], prev, 2);
    o[1] = __builtin_amdgcn_alignbyte(d[1], d[0], 2);
    o[2] = __builtin_amdgcn_alignbyte(d[2], d[1], 2);
    o[3] = __builtin_amdgcn_alignbyte(d[3], d[2], 2);
    *(u32x4*)(cb + KB_OFF + 464 + 16 * y) = o;
}
__device__ __forceinline__ void put_x(char* cb, int y, u32x4 d) {
    *(u32x4*)(cb + XA_OFF + 16 * y) = d;
    *(uint32_t*)(cb + XB_OFF + 16 * y - 4) = d[0];
    u32x2 m = {d[1], d[2]};
    *(u32x2*)(cb + XB_OFF + 16 * y) = m;
    *(uint32_t*)(cb + XB_OFF + 16 * y + 8) = d[3];
}
__device__ __forceinline__ uint32_t prev_lane(uint32_t v, uint32_t carry) {
    return (uint32_t)__builtin_amdgcn_update_dpp((int)carry, (int)v, 0x138  , 0xf, 0xf, false);
}
typedef __attribute__((address_space(3))) char* lds_t;
typedef __attribute__((address_space(3))) const volatile u32x2* lds_v2;
typedef __attribute__((address_space(3))) const bf16x8* lds_k8;
__device__ __forceinline__ bf16x8 ldx(lds_t p) {
    u32x2 a = *(lds_v2)p, b = *(lds_v2)(p + 8);
    u32x4 v = {a[0], a[1], b[0], b[1]};
    return __builtin_bit_cast(bf16x8, v);
}
__device__ __forceinline__ bf16x8 ldk(lds_t p) { return *(lds_k8)p; }
#define MFMA(a, b, c) c = __builtin_amdgcn_mfma_f32_16x16x32_bf16(a, b, c, 0, 0, 0)
#define SB() __builtin_amdgcn_sched_barrier(0)
#define SG_DS(n) __builtin_amdgcn_sched_group_barrier(0x100, n, 0)
#define SG_MF(n) __builtin_amdgcn_sched_group_barrier(0x008, n, 0)
#define ST2_OFF 1488

__device__ __forceinline__ void epi_acc(const f32x4 a, const u32x2 xd2, char* dst, float Dv) {
#pragma unroll
    for (int hh = 0; hh < 2; ++hh) {
        const uint32_t xd = xd2[hh];
        float y1 = a[2 * hh + 1], y0 = a[2 * hh];
        rbf2(y1, y0);
        float t1 = lo_f(xd) * Dv, t0 = hi_f(xd) * Dv;
        rbf2(t1, t0);
        *(uint32_t*)(dst - 64 * hh) = pk_bf16(y1 + t1, y0 + t0);
    }
}
__device__ __forceinline__ void epi_row(const char* a0, int stride4, u32x4 qq4, uint16_t* dst) {
    s16x4 v0 = __builtin_amdgcn_ds_read_tr16_b64_v4i16((__attribute__((address_space(3))) s16x4*)(a0));
    s16x4 v1 = __builtin_amdgcn_ds_read_tr16_b64_v4i16((__attribute__((address_space(3))) s16x4*)(a0 + stride4));
    u32x2 p0 = __builtin_bit_cast(u32x2, v0), p1 = __builtin_bit_cast(u32x2, v1);
    u32x4 o = {p0[0], p0[1], p1[0], p1[1]};
#pragma unroll
    for (int j = 0; j < 4; ++j)
        o[j] = pk_bf16(lo_f(qq4[j]) * lo_f(o[j]), hi_f(qq4[j]) * hi_f(o[j]));
    *(u32x4*)dst = o;
}

#define SG_VA(n) __builtin_amdgcn_sched_group_barrier(0x002, n, 0)
__device__ __forceinline__ void lds_barrier() {
    __builtin_amdgcn_fence(__ATOMIC_RELEASE, "workgroup", "local");
    __builtin_amdgcn_s_barrier();
    __builtin_amdgcn_fence(__ATOMIC_ACQUIRE, "workgroup", "local");
}
#define LOADF(KF, XF, P) _Pragma("unroll") for (int m = 0; m < 8; ++m) { KF[m] = ldk(kaddr + 64 * (P) + 512 * m); XF[m] = ldx(xaddr + 64 * (P) + 512 * (7 - m)); }
#define MM20(K, X) \
    MFMA(K[4], X[3], acc[7]); MFMA(K[4], X[2], acc[6]); MFMA(K[3], X[4], a7b);    MFMA(K[4], X[1], acc[5]); \
    MFMA(K[5], X[2], acc[7]); MFMA(K[2], X[4], acc[6]); MFMA(K[2], X[5], a7b);    MFMA(K[4], X[0], acc[4]); \
    MFMA(K[6], X[1], acc[7]); MFMA(K[5], X[1], acc[6]); MFMA(K[1], X[6], a7b);    MFMA(K[1], X[4], acc[5]); \
    MFMA(K[7], X[0], acc[7]); MFMA(K[1], X[5], acc[6]); MFMA(K[0], X[7], a7b);    MFMA(K[5], X[0], acc[5]); \
    MFMA(K[6], X[0], acc[6]); MFMA(K[0], X[4], acc[4]); MFMA(K[0], X[6], acc[6]); MFMA(K[0], X[5], acc[5]);
#define PAT(nv) _Pragma("unroll") for (int i_ = 0; i_ < 13; ++i_) { SG_MF(1); SG_DS(2); SG_VA(nv); } \
                _Pragma("unroll") for (int i_ = 0; i_ < 7; ++i_) { SG_MF(1); SG_VA(nv); } SB();

__global__ __launch_bounds__(512) void conv_kernel(
    const uint16_t* __restrict__ xw, const uint16_t* __restrict__ Kw, const uint16_t* __restrict__ ssmk,
    const uint16_t* __restrict__ qin, const uint16_t* __restrict__ Dsk, const uint16_t* __restrict__ sD,
    uint16_t* __restrict__ out)
{
    __shared__ __attribute__((aligned(16))) char lds[CONV_LDS];
    const int b = blockIdx.x;
    const int xcd = b & 7, idx = b >> 3, part = idx >> 4, grp = xcd * 16 + (idx & 15);
    const int lane = threadIdx.x & 63;
    const int w = __builtin_amdgcn_readfirstlane(threadIdx.x >> 6);
    const int chl = grp * 8 + w;
    const uint16_t* ksrc = (part ? ssmk : Kw) + (size_t)chl * LSEQ;
    const uint16_t* xsrc = xw + (size_t)(part * DCH + chl) * LSEQ;
    char* cb = lds + w * CH_STRIDE;

    STAMP(1, 0);
    const uint32_t pf = prefetch_code(7168);
    const u32x4 k0 = *(const u32x4*)(ksrc + 8 * lane);
    SB();
    const u32x4 x3 = *(const u32x4*)(xsrc + 8 * (192 + lane));
    SB();
    const u32x4 k1 = *(const u32x4*)(ksrc + 8 * (64 + lane));
    SB();
    const u32x4 x2 = *(const u32x4*)(xsrc + 8 * (128 + lane));
    SB();

    const int g = lane >> 4, fr = lane & 15;
    {
        const u32x4 z = {0, 0, 0, 0};
        if (lane < 29) {
            *(u32x4*)(cb + KA_OFF + 16 * lane) = z;
            *(u32x4*)(cb + KB_OFF + 16 * lane) = z;
        }
        if (lane < 4) *(u32x4*)(cb + XA_OFF + 4096 + 16 * lane) = z;
        if (lane < 5) *(u32x4*)(cb + XB_OFF + 4080 + 16 * lane) = z;
    }
    put_k(cb, lane, k0, prev_lane(k0[3], 0u));
    const uint32_t c1 = __builtin_amdgcn_readlane(k0[3], 63);
    put_x(cb, 192 + lane, x3);
    put_k(cb, 64 + lane, k1, prev_lane(k1[3], c1));
    const uint32_t c2 = __builtin_amdgcn_readlane(k1[3], 63);
    put_x(cb, 128 + lane, x2);
    SB();
    const u32x4 k2 = *(const u32x4*)(ksrc + 8 * (128 + lane));
    SB();
    const u32x4 x1 = *(const u32x4*)(xsrc + 8 * (64 + lane));
    SB();
    const u32x4 k3 = *(const u32x4*)(ksrc + 8 * (192 + lane));
    SB();
    const u32x4 x0 = *(const u32x4*)(xsrc + 8 * lane);
    SB();
    const float Dv = bf2f((part ? sD : Dsk)[chl]);
    const int r0 = 1023 - (128 * w + lane);
    u32x4 qv[4];
#pragma unroll
    for (int i = 0; i < 4; ++i) qv[i] = (u32x4){0x3f803f80u, 0x3f803f80u, 0x3f803f80u, 0x3f803f80u};
    if (part) {
#pragma unroll
        for (int i = 0; i < 4; ++i)
            qv[i] = *(const u32x4*)(qin + (size_t)(r0 - 64 * (i & 1) + 1024 * (i >> 1)) * DCH + grp * 8);
    }
    SB();

    const lds_t lb = (lds_t)lds + w * CH_STRIDE;
    const lds_t kaddr = lb + ((fr & 1) ? KA_OFF : KB_OFF) + 2 * (32 * (fr >> 1) + 8 * g + 8);
    lds_t xaddr = lb + ((fr & 1) ? (XA_OFF + 2 * (1822 - 2 * fr + 8 * g)) : (XB_OFF + 2 * (1820 - 2 * fr + 8 * g))) - 3584;
    asm volatile("" : "+v"(xaddr));
    char* ys = lds + YS_OFF;
    const int lo_ = 64 * g + 2 * fr;
    const char* xa = cb + XA_OFF + 2 * (2046 - lo_);
    const int qq = (lane >> 2) & 3, pp = lane & 3;
    uint16_t* ob = out + part * DCH + grp * 8 + (size_t)r0 * (2 * DCH);

    f32x4 acc[8];
#pragma unroll
    for (int i = 0; i < 8; ++i) acc[i] = (f32x4){0, 0, 0, 0};
    f32x4 a7b = {0, 0, 0, 0};
    u32x2 xd[8];

    STAMP(1, 1);
    {
        bf16x8 ka[4], xa_[4], kb[4], xb_[4];
#pragma unroll
        for (int m = 0; m < 4; ++m) { ka[m] = ldk(kaddr + 512 * m); xa_[m] = ldx(xaddr + 512 * (7 - m)); }
#pragma unroll 1
        for (int c0 = 0; c0 < 8; c0 += 2) {
            const lds_t kp = kaddr + 64 * c0, xp = xaddr + 64 * c0;
#pragma unroll
            for (int m = 0; m < 4; ++m) { kb[m] = ldk(kp + 64 + 512 * m); xb_[m] = ldx(xp + 64 + 512 * (7 - m)); }
#pragma unroll
            for (int m = 0; m < 4; ++m)
#pragma unroll
                for (int t = 0; t < 4; ++t) MFMA(ka[m], xa_[t], acc[m + t]);
#pragma unroll
            for (int i = 0; i < 12; ++i) { SG_MF(1); SG_DS(1); }
            SG_MF(4);
            SB();
#pragma unroll
            for (int m = 0; m < 4; ++m) { ka[m] = ldk(kp + 128 + 512 * m); xa_[m] = ldx(xp + 128 + 512 * (7 - m)); }
#pragma unroll
            for (int m = 0; m < 4; ++m)
#pragma unroll
                for (int t = 0; t < 4; ++t) MFMA(kb[m], xb_[t], acc[m + t]);
#pragma unroll
            for (int i = 0; i < 12; ++i) { SG_MF(1); SG_DS(1); }
            SG_MF(4);
            SB();
        }
    }
    STAMP(1, 2);
    put_k(cb, 128 + lane, k2, prev_lane(k2[3], c2));
    const uint32_t c3 = __builtin_amdgcn_readlane(k2[3], 63);
    put_x(cb, 64 + lane, x1);
    put_k(cb, 192 + lane, k3, prev_lane(k3[3], c3));
    put_x(cb, lane, x0);
    SB();
    STAMP(1, 3);
    {
        bf16x8 ka[8], xa_[8], kb[8], xb_[8];
        LOADF(ka, xa_, 0);
#pragma unroll
        for (int tt = 0; tt < 8; ++tt) xd[tt] = (u32x2){*(const uint32_t*)(xa - 512 * tt), *(const uint32_t*)(xa - 512 * tt - 64)};
        SB();
        LOADF(kb, xb_, 1);
        epi_acc(acc[0], xd[0], ys + w * YS_STRIDE + 2 * (1022 - lo_), Dv);
        epi_acc(acc[1], xd[1], ys + w * YS_STRIDE + 2 * (1022 - lo_) - 512, Dv);
        MM20(ka, xa_);
        PAT(3);
        LOADF(ka, xa_, 2);
        epi_acc(acc[2], xd[2], ys + w * YS_STRIDE + 2 * (1022 - lo_) - 1024, Dv);
        epi_acc(acc[3], xd[3], ys + w * YS_STRIDE + 2 * (1022 - lo_) - 1536, Dv);
        MM20(kb, xb_);
        PAT(3);
        lds_barrier();
        SB();
        LOADF(kb, xb_, 3);
        epi_row(ys + qq * YS_STRIDE + (128 * w + 16 * g + 4 * pp) * 2, 4 * YS_STRIDE, qv[0], ob);
        MM20(ka, xa_);
        PAT(2);
        LOADF(ka, xa_, 4);
        epi_row(ys + qq * YS_STRIDE + (128 * w + 64 + 16 * g + 4 * pp) * 2, 4 * YS_STRIDE, qv[1], ob - (size_t)64 * (2 * DCH));
        MM20(kb, xb_);
        PAT(2);
        LOADF(kb, xb_, 5);
        MM20(ka, xa_);
        PAT(0);
        LOADF(ka, xa_, 6);
        MM20(kb, xb_);
        PAT(0);
        LOADF(kb, xb_, 7);
        MM20(ka, xa_);
        PAT(0);
        MM20(kb, xb_);
        SB();
        acc[7] += a7b;
    }
    STAMP(1, 4);
#pragma unroll
    for (int tt = 4; tt < 8; ++tt) epi_acc(acc[tt], xd[tt], cb + KA_OFF + 2 * (1022 - lo_) - 512 * (tt - 4), Dv);
    lds_barrier();
    STAMP(1, 5);
    epi_row(lds + qq * CH_STRIDE + KA_OFF + (128 * w + 16 * g + 4 * pp) * 2, 4 * CH_STRIDE, qv[2], ob + (size_t)1024 * (2 * DCH));
    epi_row(lds + qq * CH_STRIDE + KA_OFF + (128 * w + 64 + 16 * g + 4 * pp) * 2, 4 * CH_STRIDE, qv[3], ob + (size_t)960 * (2 * DCH));
    STAMP(1, 6);
    STAMPW(1, 7);
    KEEP(pf);
}

extern "C" void kernel_launch(void* const* d_in, const int* in_sizes, int n_in,
                              void* d_out, int out_size, void* d_ws, size_t ws_size,
                              hipStream_t stream) {
    const uint16_t* u = (const uint16_t*)d_in[0];
    const uint16_t* logA = (const uint16_t*)d_in[1];
    const uint16_t* Aim = (const uint16_t*)d_in[2];
    const uint16_t* Cre = (const uint16_t*)d_in[3];
    const uint16_t* Cim = (const uint16_t*)d_in[4];
    const uint16_t* logdt = (const uint16_t*)d_in[5];
    const uint16_t* Dsk = (const uint16_t*)d_in[6];
    const uint16_t* q = (const uint16_t*)d_in[7];
    const uint16_t* k = (const uint16_t*)d_in[8];
    const uint16_t* v = (const uint16_t*)d_in[9];
    const uint16_t* shk = (const uint16_t*)d_in[10];
    const uint16_t* ssmk = (const uint16_t*)d_in[11];
    const uint16_t* sD = (const uint16_t*)d_in[12];
    uint16_t* xw = (uint16_t*)d_ws;
    uint16_t* Kw = xw + (size_t)2 * DCH * LSEQ;
    prep_kernel<<<256, 512, 0, stream>>>(u, logA, Aim, Cre, Cim, logdt, k, v, shk, xw, Kw);
    conv_kernel<<<256, 512, 0, stream>>>(xw, Kw, ssmk, q, Dsk, sD, (uint16_t*)d_out);
}
```

```cpp
#include <hip/hip_runtime.h>
#include <stdint.h>

typedef __attribute__((ext_vector_type(4))) unsigned int u32x4;
typedef __attribute__((ext_vector_type(2))) unsigned int u32x2;
typedef __attribute__((ext_vector_type(4))) float f32x4;
typedef __attribute__((ext_vector_type(2))) float f32x2;
typedef __attribute__((ext_vector_type(8))) _Float16 f16x8;
typedef __attribute__((ext_vector_type(2))) _Float16 f16x2;
typedef __attribute__((ext_vector_type(8))) __bf16 bf16x8;
typedef __attribute__((ext_vector_type(2))) __bf16 bf16x2;
typedef __attribute__((ext_vector_type(4))) short s16x4;

#define STAMP(k, i)
#define STAMPW(k, i)
#define LSEQ 2048
#define DCH 1024
#define NMODE 128

__device__ __forceinline__ float bf2f(uint16_t v) { return __uint_as_float(((uint32_t)v) << 16); }
__device__ __forceinline__ float lo_f(uint32_t w) { return __uint_as_float(w << 16); }
__device__ __forceinline__ float hi_f(uint32_t w) { return __uint_as_float(w & 0xffff0000u); }
__device__ __forceinline__ uint32_t pk_bf16(float lo, float hi) {
    f32x2 x = {lo, hi};
    bf16x2 y = __builtin_convertvector(x, bf16x2);
    return __builtin_bit_cast(uint32_t, y);
}
__device__ __forceinline__ uint32_t pk_f16(float lo, float hi) {
    f32x2 x = {lo, hi};
    f16x2 y = __builtin_convertvector(x, f16x2);
    return __builtin_bit_cast(uint32_t, y);
}
__device__ __forceinline__ void rbf2(float& a, float& b) {
    uint32_t p = pk_bf16(a, b);
    a = lo_f(p);
    b = hi_f(p);
}
__device__ __forceinline__ float rbf(float a) { return lo_f(pk_bf16(a, a)); }

struct TapTab { uint16_t v[LSEQ]; };
static constexpr TapTab make_tab() {
    TapTab t{};
    for (int j = 0; j < LSEQ; ++j) {
        int p = j;
        if (j >= 256) {
            int s = (j < 512) ? 1 : ((j < 1024) ? 2 : 3);
            int q = j >> s, r = j & ((1 << s) - 1), half = 1 << (s - 1);
            if (r > half || (r == half && (q & 1))) q++;
            p = q << s;
        }
        int col = (p >> 3) & 15, a = p >> 7, low = p & 7, row = 0;
        if (p < 256) row = 8 * a + low;
        else if (p < 512) row = 16 + 4 * (a - 2) + (low >> 1);
        else if (p < 1024) row = 24 + 2 * (a - 4) + (low >> 2);
        else row = 32 + (a - 8);
        t.v[j] = (uint16_t)(row * 16 + col);
    }
    return t;
}
__device__ const TapTab g_taptab = make_tab();

__device__ __forceinline__ uint32_t prefetch_code(int bytes) {
    uint64_t pc;
    asm volatile("s_getpc_b64 %0" : "=s"(pc));
    const int off = (int)threadIdx.x * 64;
    typedef __attribute__((address_space(1))) const uint32_t* gp_t;
    const uint32_t v = *(gp_t)((pc & ~63ull) + (off < bytes ? off : 0));
    __builtin_amdgcn_sched_barrier(0);
    return v;
}
__device__ __forceinline__ void WST(u32x4* p, u32x4 v) { asm volatile("global_store_dwordx4 %0, %1, off sc0 sc1" :: "v"(p), "v"(v) : "memory"); }
#define KEEP(v) asm volatile("" :: "v"(v))
__device__ __forceinline__ f32x2 cmul(f32x2 a, f32x2 b) {
    f32x2 t, c;
    asm("v_pk_mul_f32 %0, %1, %2 op_sel:[0,0] op_sel_hi:[0,1]" : "=v"(t) : "v"(a), "v"(b));
    asm("v_pk_fma_f32 %0, %1, %2, %3 op_sel:[1,1,0] op_sel_hi:[1,0,1] neg_lo:[1,0,0]" : "=v"(c) : "v"(a), "v"(b), "v"(t));
    return c;
}
__device__ __forceinline__ f32x2 anchor(float a, float b) {
    const float e = __builtin_amdgcn_exp2f(a);
    const float th = __builtin_amdgcn_fractf(b);
    return (f32x2){e * __builtin_amdgcn_cosf(th), e * __builtin_amdgcn_sinf(th)};
}
__device__ __forceinline__ uint32_t pk16(f32x2 c) { return pk_f16(c[0], c[1]); }

#define KG_ROWB 272
#define KG_TAB (64 * KG_ROWB)
#define KG_WAVE (KG_TAB + 1536)

__global__ __launch_bounds__(512) void prep_kernel(
    const uint16_t* __restrict__ u, const uint16_t* __restrict__ logA, const uint16_t* __restrict__ Aim,
    const uint16_t* __restrict__ Cre, const uint16_t* __restrict__ Cim, const uint16_t* __restrict__ logdt,
    const uint16_t* __restrict__ kin, const uint16_t* __restrict__ vin, const uint16_t* __restrict__ shk,
    const uint16_t* __restrict__ ssmk, const uint16_t* __restrict__ qin,
    uint16_t* __restrict__ xw, uint16_t* __restrict__ Kw)
{
    __shared__ __attribute__((aligned(16))) char lds[4 * KG_WAVE];
    const int lane = threadIdx.x & 63;
    const int w = __builtin_amdgcn_readfirstlane(threadIdx.x >> 6);
    const int b = blockIdx.x;
    const int xcd = b & 7, jb = b >> 3;
    STAMP(0, 0);
    const uint32_t pf = prefetch_code(10240);

    if (w < 4) {
        const int cg = lane & 15, rb = lane >> 4;
        const int s0 = 64 * jb + 32 * (w >> 1) + 8 * rb, c0 = 128 * xcd + 64 * (w & 1) + 4 * cg;
        u32x2 ur[8], kr[11], vr[8];
        u32x4 wv[2];
#pragma unroll
        for (int i = 0; i < 11; ++i) {
            const int s = s0 + i - 3;
            kr[i] = *(const u32x2*)(kin + (size_t)(s < 0 ? 0 : s) * DCH + c0);
            if (i < 3 && s < 0) kr[i] = (u32x2){0, 0};
        }
#pragma unroll
        for (int i = 0; i < 2; ++i) wv[i] = *(const u32x4*)(shk + (size_t)c0 * 4 + 8 * i);
#pragma unroll
        for (int i = 0; i < 8; ++i) vr[i] = *(const u32x2*)(vin + (size_t)(s0 + i) * DCH + c0);
#pragma unroll
        for (int i = 0; i < 8; ++i) ur[i] = *(const u32x2*)(u + (size_t)(s0 + i) * DCH + c0);
#pragma unroll
        for (int ch = 0; ch < 4; ++ch) {
            const uint32_t wlo = wv[ch >> 1][(ch & 1) * 2], whi = wv[ch >> 1][(ch & 1) * 2 + 1];
            const float w0 = lo_f(wlo), w1 = hi_f(wlo), w2 = lo_f(whi), w3 = hi_f(whi);
            float kf[11], vf[8];
#pragma unroll
            for (int i = 0; i < 11; ++i) kf[i] = (ch & 1) ? hi_f(kr[i][ch >> 1]) : lo_f(kr[i][ch >> 1]);
#pragma unroll
            for (int i = 0; i < 8; ++i) vf[i] = (ch & 1) ? hi_f(vr[i][ch >> 1]) : lo_f(vr[i][ch >> 1]);
            u32x4 o;
#pragma unroll
            for (int q = 0; q < 4; ++q) {
                const int ia = 7 - 2 * q, ib = 6 - 2 * q;
                float a0 = kf[ia] * w3, b0 = kf[ib] * w3;
                rbf2(a0, b0);
                float a1 = kf[ia + 1] * w2, b1 = kf[ib + 1] * w2;
                rbf2(a1, b1);
                a0 += a1; b0 += b1;
                rbf2(a0, b0);
                a1 = kf[ia + 2] * w1; b1 = kf[ib + 2] * w1;
                rbf2(a1, b1);
                a0 += a1; b0 += b1;
                rbf2(a0, b0);
                a1 = kf[ia + 3] * w0; b1 = kf[ib + 3] * w0;
                rbf2(a1, b1);
                a0 += a1; b0 += b1;
                rbf2(a0, b0);
                o[q] = pk_bf16(a0 * vf[ia], b0 * vf[ib]);
            }
            WST((u32x4*)(xw + (size_t)(DCH + c0 + ch) * LSEQ + (2040 - s0)), o);
        }
#pragma unroll
        for (int ch = 0; ch < 4; ++ch) {
            u32x4 o;
#pragma unroll
            for (int q = 0; q < 4; ++q)
                o[q] = __builtin_amdgcn_perm(ur[6 - 2 * q][ch >> 1], ur[7 - 2 * q][ch >> 1],
                                             (ch & 1) ? 0x07060302u : 0x05040100u);
            WST((u32x4*)(xw + (size_t)(c0 + ch) * LSEQ + (2040 - s0)), o);
        }
        STAMP(0, 1);
        STAMPW(0, 2);
        KEEP(pf);
        return;
    }

    __builtin_amdgcn_s_setprio(3);
    const int d = 128 * xcd + 4 * jb + (w - 4);
    char* T = lds + (w - 4) * KG_WAVE;
    u32x4 tabv[4];
#pragma unroll
    for (int i = 0; i < 4; ++i) tabv[i] = *(const u32x4*)(g_taptab.v + 8 * (64 * i + lane));

    const float dt = rbf(expf(bf2f(logdt[d])));
    float pa[2], pb[2], pcr[2], pci[2];
    float mx = 0.f;
#pragma unroll
    for (int bt = 0; bt < 2; ++bt) {
        const int n = d * NMODE + bt * 64 + lane;
        const float ar = -rbf(expf(bf2f(logA[n])));
        pa[bt] = (ar * dt) * 1.4426950408889634f;
        pb[bt] = (bf2f(Aim[n]) * dt) * 0.15915494309189535f;
        pcr[bt] = 2.f * (bf2f(Cre[n]) * dt);
        pci[bt] = 2.f * (bf2f(Cim[n]) * dt);
        mx = fmaxf(mx, fmaxf(fabsf(pcr[bt]), fabsf(pci[bt])));
    }
    uint32_t pf2;
    {
        const int t = threadIdx.x - 256;
        const uint16_t* p = (t < 128) ? ssmk + (size_t)(128 * xcd + 4 * jb) * LSEQ + t * 64
                                      : qin + (size_t)(64 * jb + ((t - 128) >> 1)) * DCH + 128 * xcd + 64 * (t & 1);
        pf2 = *(const uint32_t*)p;
        __builtin_amdgcn_sched_barrier(0);
    }
#pragma unroll
    for (int o = 32; o >= 1; o >>= 1) mx = fmaxf(mx, __shfl_xor(mx, o));
    int sf = 266 - (int)((__float_as_uint(mx) >> 23) & 0xff);
    sf = sf < 1 ? 1 : (sf > 239 ? 239 : sf);
    const float scl = __uint_as_float((uint32_t)sf << 23);
    const float inv = __uint_as_float((uint32_t)(240 - sf) << 23);

    STAMP(0, 1);
#pragma unroll
    for (int r = 41; r < 48; ++r) *(uint32_t*)(T + r * KG_ROWB + lane * 4) = 0u;

    f32x4 acc[3] = {{0, 0, 0, 0}, {0, 0, 0, 0}, {0, 0, 0, 0}};
    const int fr = lane & 15, kb = lane >> 4;
#pragma unroll 1
    for (int bt = 0; bt < 2; ++bt) {
        const float a = bt ? pa[1] : pa[0], bb = bt ? pb[1] : pb[0];
        const f32x2 z1 = anchor(a, bb), z8c = anchor(a * 8.f, bb * -8.f), z128 = anchor(a * 128.f, bb * 128.f);
        char* Tl = T + lane * 4;
        const f32x2 z16c = cmul(z8c, z8c), z256 = cmul(z128, z128), h2 = cmul(z1, z1);
        const f32x2 z32c = cmul(z16c, z16c), z512 = cmul(z256, z256), h4 = cmul(h2, h2);
        const f32x2 z64c = cmul(z32c, z32c), z1024 = cmul(z512, z512), h3 = cmul(h2, z1);
        f32x2 f[16], cg[17], h[8];
        f[0] = (f32x2){16384.f, 0.f};
        f[1] = z8c * 16384.f;
        f[2] = z16c * 16384.f;
        f[4] = z32c * 16384.f;
        f[8] = z64c * 16384.f;
        cg[0] = (f32x2){(bt ? pcr[1] : pcr[0]) * scl, (bt ? pci[1] : pci[0]) * scl};
        cg[1] = cmul(cg[0], z128);
        cg[2] = cmul(cg[0], z256);
        cg[4] = cmul(cg[0], z512);
        cg[8] = cmul(cg[0], z1024);
        h[1] = z1; h[2] = h2; h[3] = h3; h[4] = h4;
        h[5] = cmul(h4, z1); h[6] = cmul(h4, h2); h[7] = cmul(h4, h3);
        f[3] = cmul(f[2], z8c);
        cg[3] = cmul(cg[2], z128);
        cg[16] = cmul(cg[8], z1024);
#pragma unroll
        for (int i = 1; i < 4; ++i) { f[4 + i] = cmul(f[i], z32c); cg[4 + i] = cmul(cg[i], z512); }
#pragma unroll
        for (int i = 1; i < 8; ++i) { f[8 + i] = cmul(f[i], z64c); cg[8 + i] = cmul(cg[i], z1024); }
#pragma unroll
        for (int i = 0; i < 16; ++i) *(uint32_t*)(Tl + (48 + i) * KG_ROWB) = pk16(f[i]);
#pragma unroll
        for (int aa = 0; aa <= 16; ++aa) {
            if (aa < 2) {
                *(uint32_t*)(Tl + (8 * aa) * KG_ROWB) = pk16(cg[aa]);
#pragma unroll
                for (int e = 1; e < 8; ++e) *(uint32_t*)(Tl + (8 * aa + e) * KG_ROWB) = pk16(cmul(cg[aa], h[e]));
            } else if (aa < 4) {
                *(uint32_t*)(Tl + (16 + 4 * (aa - 2)) * KG_ROWB) = pk16(cg[aa]);
#pragma unroll
                for (int e = 1; e < 4; ++e) *(uint32_t*)(Tl + (16 + 4 * (aa - 2) + e) * KG_ROWB) = pk16(cmul(cg[aa], h[2 * e]));
            } else if (aa < 8) {
                *(uint32_t*)(Tl + (24 + 2 * (aa - 4)) * KG_ROWB) = pk16(cg[aa]);
                *(uint32_t*)(Tl + (24 + 2 * (aa - 4) + 1) * KG_ROWB) = pk16(cmul(cg[aa], h[4]));
            } else {
                *(uint32_t*)(Tl + (32 + (aa - 8)) * KG_ROWB) = pk16(cg[aa]);
            }
        }
        f16x8 bfr[4], afr[4][3];
#pragma unroll
        for (int ks = 0; ks < 4; ++ks) {
            bfr[ks] = *(const f16x8*)(T + (48 + fr) * KG_ROWB + ks * 64 + kb * 16);
#pragma unroll
            for (int rt = 0; rt < 3; ++rt) afr[ks][rt] = *(const f16x8*)(T + (16 * rt + fr) * KG_ROWB + ks * 64 + kb * 16);
        }
        __builtin_amdgcn_sched_barrier(0);
#pragma unroll
        for (int ks = 0; ks < 4; ++ks)
#pragma unroll
            for (int rt = 0; rt < 3; ++rt)
                acc[rt] = __builtin_amdgcn_mfma_f32_16x16x32_f16(afr[ks][rt], bfr[ks], acc[rt], 0, 0, 0);
    }
    STAMP(0, 2);
    uint16_t* KD = (uint16_t*)(T + KG_TAB);
#pragma unroll
    for (int rt = 0; rt < 3; ++rt)
#pragma unroll
        for (int r = 0; r < 4; r += 2) {
            uint32_t p = pk_bf16(acc[rt][r] * inv, acc[rt][r + 1] * inv);
            KD[(16 * rt + 4 * kb + r) * 16 + fr] = (uint16_t)p;
            KD[(16 * rt + 4 * kb + r + 1) * 16 + fr] = (uint16_t)(p >> 16);
        }
#pragma unroll
    for (int i = 0; i < 4; ++i) {
        u32x4 o;
#pragma unroll
        for (int q = 0; q < 4; ++q) {
            const uint32_t lo = KD[tabv[i][q] & 0xffffu], hi = KD[tabv[i][q] >> 16];
            o[q] = lo | (hi << 16);
        }
        WST((u32x4*)(Kw + (size_t)d * LSEQ + 8 * (64 * i + lane)), o);
    }
    STAMP(0, 3);
    STAMPW(0, 4);
    KEEP(pf);
    KEEP(pf2);
}

#define KA_OFF 0
#define KB_OFF 4640
#define XA_OFF 9216
#define XB_OFF 13440
#define CH_STRIDE 17600
#define YS_OFF (8 * CH_STRIDE)
#define YS_STRIDE 2112
#define CONV_LDS (YS_OFF + 8 * YS_STRIDE)

__device__ __forceinline__ void put_k(char* cb, int y, u32x4 d, uint32_t prev) {
    *(u32x4*)(cb + KA_OFF + 464 + 16 * y) = d;
    u32x4 o;
    o[0] = __builtin_amdgcn_alignbyte(d[0], prev, 2);
    o[1] = __builtin_amdgcn_alignbyte(d[1], d[0], 2);
    o[2] = __builtin_amdgcn_alignbyte(d[2], d[1], 2);
    o[3] = __builtin_amdgcn_alignbyte(d[3], d[2], 2);
    *(u32x4*)(cb + KB_OFF + 464 + 16 * y) = o;
}
__device__ __forceinline__ void put_x(char* cb, int y, u32x4 d) {
    *(u32x4*)(cb + XA_OFF + 16 * y) = d;
    *(uint32_t*)(cb + XB_OFF + 16 * y - 4) = d[0];
    u32x2 m = {d[1], d[2]};
    *(u32x2*)(cb + XB_OFF + 16 * y) = m;
    *(uint32_t*)(cb + XB_OFF + 16 * y + 8) = d[3];
}
__device__ __forceinline__ uint32_t prev_lane(uint32_t v, uint32_t carry) {
    return (uint32_t)__builtin_amdgcn_update_dpp((int)carry, (int)v, 0x138  , 0xf, 0xf, false);
}
typedef __attribute__((address_space(3))) char* lds_t;
typedef __attribute__((address_space(3))) const volatile u32x2* lds_v2;
typedef __attribute__((address_space(3))) const bf16x8* lds_k8;
__device__ __forceinline__ bf16x8 ldx(lds_t p) {
    u32x2 a = *(lds_v2)p, b = *(lds_v2)(p + 8);
    u32x4 v = {a[0], a[1], b[0], b[1]};
    return __builtin_bit_cast(bf16x8, v);
}
__device__ __forceinline__ bf16x8 ldk(lds_t p) { return *(lds_k8)p; }
#define MFMA(a, b, c) c = __builtin_amdgcn_mfma_f32_16x16x32_bf16(a, b, c, 0, 0, 0)
#define SB() __builtin_amdgcn_sched_barrier(0)
#define SG_DS(n) __builtin_amdgcn_sched_group_barrier(0x100, n, 0)
#define SG_MF(n) __builtin_amdgcn_sched_group_barrier(0x008, n, 0)
#define ST2_OFF 1488

__device__ __forceinline__ void epi_acc(const f32x4 a, const u32x2 xd2, char* dst, float Dv) {
#pragma unroll
    for (int hh = 0; hh < 2; ++hh) {
        const uint32_t xd = xd2[hh];
        float y1 = a[2 * hh + 1], y0 = a[2 * hh];
        rbf2(y1, y0);
        float t1 = lo_f(xd) * Dv, t0 = hi_f(xd) * Dv;
        rbf2(t1, t0);
        *(uint32_t*)(dst - 64 * hh) = pk_bf16(y1 + t1, y0 + t0);
    }
}
__device__ __forceinline__ void epi_row(const char* a0, int stride4, u32x4 qq4, uint16_t* dst) {
    s16x4 v0 = __builtin_amdgcn_ds_read_tr16_b64_v4i16((__attribute__((address_space(3))) s16x4*)(a0));
    s16x4 v1 = __builtin_amdgcn_ds_read_tr16_b64_v4i16((__attribute__((address_space(3))) s16x4*)(a0 + stride4));
    u32x2 p0 = __builtin_bit_cast(u32x2, v0), p1 = __builtin_bit_cast(u32x2, v1);
    u32x4 o = {p0[0], p0[1], p1[0], p1[1]};
#pragma unroll
    for (int j = 0; j < 4; ++j)
        o[j] = pk_bf16(lo_f(qq4[j]) * lo_f(o[j]), hi_f(qq4[j]) * hi_f(o[j]));
    *(u32x4*)dst = o;
}

#define SG_VA(n) __builtin_amdgcn_sched_group_barrier(0x002, n, 0)
__device__ __forceinline__ void lds_barrier() {
    __builtin_amdgcn_fence(__ATOMIC_RELEASE, "workgroup", "local");
    __builtin_amdgcn_s_barrier();
    __builtin_amdgcn_fence(__ATOMIC_ACQUIRE, "workgroup", "local");
}
#define LOADF(KF, XF, P) _Pragma("unroll") for (int m = 0; m < 8; ++m) { KF[m] = ldk(kaddr + 64 * (P) + 512 * m); XF[m] = ldx(xaddr + 64 * (P) + 512 * (7 - m)); }
#define MM20(K, X) \
    MFMA(K[4], X[3], acc[7]); MFMA(K[4], X[2], acc[6]); MFMA(K[3], X[4], a7b);    MFMA(K[4], X[1], acc[5]); \
    MFMA(K[5], X[2], acc[7]); MFMA(K[2], X[4], acc[6]); MFMA(K[2], X[5], a7b);    MFMA(K[4], X[0], acc[4]); \
    MFMA(K[6], X[1], acc[7]); MFMA(K[5], X[1], acc[6]); MFMA(K[1], X[6], a7b);    MFMA(K[1], X[4], acc[5]); \
    MFMA(K[7], X[0], acc[7]); MFMA(K[1], X[5], acc[6]); MFMA(K[0], X[7], a7b);    MFMA(K[5], X[0], acc[5]); \
    MFMA(K[6], X[0], acc[6]); MFMA(K[0], X[4], acc[4]); MFMA(K[0], X[6], acc[6]); MFMA(K[0], X[5], acc[5]);
#define PAT(nv) _Pragma("unroll") for (int i_ = 0; i_ < 13; ++i_) { SG_MF(1); SG_DS(2); SG_VA(nv); } \
                _Pragma("unroll") for (int i_ = 0; i_ < 7; ++i_) { SG_MF(1); SG_VA(nv); } SB();

__global__ __launch_bounds__(512) void conv_kernel(
    const uint16_t* __restrict__ xw, const uint16_t* __restrict__ Kw, const uint16_t* __restrict__ ssmk,
    const uint16_t* __restrict__ qin, const uint16_t* __restrict__ Dsk, const uint16_t* __restrict__ sD,
    uint16_t* __restrict__ out)
{
    __shared__ __attribute__((aligned(16))) char lds[CONV_LDS];
    const int b = blockIdx.x;
    const int xcd = b & 7, idx = b >> 3, part = idx >> 4, grp = xcd * 16 + (idx & 15);
    const int lane = threadIdx.x & 63;
    const int w = __builtin_amdgcn_readfirstlane(threadIdx.x >> 6);
    const int chl = grp * 8 + w;
    const uint16_t* ksrc = (part ? ssmk : Kw) + (size_t)chl * LSEQ;
    const uint16_t* xsrc = xw + (size_t)(part * DCH + chl) * LSEQ;
    char* cb = lds + w * CH_STRIDE;

    STAMP(1, 0);
    const uint32_t pf = prefetch_code(7168);
    const u32x4 k0 = *(const u32x4*)(ksrc + 8 * lane);
    SB();
    const u32x4 x3 = *(const u32x4*)(xsrc + 8 * (192 + lane));
    SB();
    const u32x4 k1 = *(const u32x4*)(ksrc + 8 * (64 + lane));
    SB();
    const u32x4 x2 = *(const u32x4*)(xsrc + 8 * (128 + lane));
    SB();

    const int g = lane >> 4, fr = lane & 15;
    {
        const u32x4 z = {0, 0, 0, 0};
        if (lane < 29) {
            *(u32x4*)(cb + KA_OFF + 16 * lane) = z;
            *(u32x4*)(cb + KB_OFF + 16 * lane) = z;
        }
        if (lane < 4) *(u32x4*)(cb + XA_OFF + 4096 + 16 * lane) = z;
        if (lane < 5) *(u32x4*)(cb + XB_OFF + 4080 + 16 * lane) = z;
    }
    put_k(cb, lane, k0, prev_lane(k0[3], 0u));
    const uint32_t c1 = __builtin_amdgcn_readlane(k0[3], 63);
    put_x(cb, 192 + lane, x3);
    put_k(cb, 64 + lane, k1, prev_lane(k1[3], c1));
    const uint32_t c2 = __builtin_amdgcn_readlane(k1[3], 63);
    put_x(cb, 128 + lane, x2);
    SB();
    const u32x4 k2 = *(const u32x4*)(ksrc + 8 * (128 + lane));
    SB();
    const u32x4 x1 = *(const u32x4*)(xsrc + 8 * (64 + lane));
    SB();
    const u32x4 k3 = *(const u32x4*)(ksrc + 8 * (192 + lane));
    SB();
    const u32x4 x0 = *(const u32x4*)(xsrc + 8 * lane);
    SB();
    const float Dv = bf2f((part ? sD : Dsk)[chl]);
    const int r0 = 1023 - (128 * w + lane);
    u32x4 qv[4];
#pragma unroll
    for (int i = 0; i < 4; ++i) qv[i] = (u32x4){0x3f803f80u, 0x3f803f80u, 0x3f803f80u, 0x3f803f80u};
    if (part) {
#pragma unroll
        for (int i = 0; i < 4; ++i)
            qv[i] = *(const u32x4*)(qin + (size_t)(r0 - 64 * (i & 1) + 1024 * (i >> 1)) * DCH + grp * 8);
    }
    SB();

    const lds_t lb = (lds_t)lds + w * CH_STRIDE;
    const lds_t kaddr = lb + ((fr & 1) ? KA_OFF : KB_OFF) + 2 * (32 * (fr >> 1) + 8 * g + 8);
    lds_t xaddr = lb + ((fr & 1) ? (XA_OFF + 2 * (1822 - 2 * fr + 8 * g)) : (XB_OFF + 2 * (1820 - 2 * fr + 8 * g))) - 3584;
    asm volatile("" : "+v"(xaddr));
    char* ys = lds + YS_OFF;
    const int lo_ = 64 * g + 2 * fr;
    const char* xa = cb + XA_OFF + 2 * (2046 - lo_);
    const int qq = (lane >> 2) & 3, pp = lane & 3;
    uint16_t* ob = out + part * DCH + grp * 8 + (size_t)r0 * (2 * DCH);

    f32x4 acc[8];
#pragma unroll
    for (int i = 0; i < 8; ++i) acc[i] = (f32x4){0, 0, 0, 0};
    f32x4 a7b = {0, 0, 0, 0};
    u32x2 xd[8];

    STAMP(1, 1);
    {
        bf16x8 ka[4], xa_[4], kb[4], xb_[4];
#pragma unroll
        for (int m = 0; m < 4; ++m) { ka[m] = ldk(kaddr + 512 * m); xa_[m] = ldx(xaddr + 512 * (7 - m)); }
#pragma unroll 1
        for (int c0 = 0; c0 < 8; c0 += 2) {
            const lds_t kp = kaddr + 64 * c0, xp = xaddr + 64 * c0;
#pragma unroll
            for (int m = 0; m < 4; ++m) { kb[m] = ldk(kp + 64 + 512 * m); xb_[m] = ldx(xp + 64 + 512 * (7 - m)); }
#pragma unroll
            for (int m = 0; m < 4; ++m)
#pragma unroll
                for (int t = 0; t < 4; ++t) MFMA(ka[m], xa_[t], acc[m + t]);
#pragma unroll
            for (int i = 0; i < 12; ++i) { SG_MF(1); SG_DS(1); }
            SG_MF(4);
            SB();
#pragma unroll
            for (int m = 0; m < 4; ++m) { ka[m] = ldk(kp + 128 + 512 * m); xa_[m] = ldx(xp + 128 + 512 * (7 - m)); }
#pragma unroll
            for (int m = 0; m < 4; ++m)
#pragma unroll
                for (int t = 0; t < 4; ++t) MFMA(kb[m], xb_[t], acc[m + t]);
#pragma unroll
            for (int i = 0; i < 12; ++i) { SG_MF(1); SG_DS(1); }
            SG_MF(4);
            SB();
        }
    }
    STAMP(1, 2);
    put_k(cb, 128 + lane, k2, prev_lane(k2[3], c2));
    const uint32_t c3 = __builtin_amdgcn_readlane(k2[3], 63);
    put_x(cb, 64 + lane, x1);
    put_k(cb, 192 + lane, k3, prev_lane(k3[3], c3));
    put_x(cb, lane, x0);
    SB();
    STAMP(1, 3);
    {
        bf16x8 ka[8], xa_[8], kb[8], xb_[8];
        LOADF(ka, xa_, 0);
#pragma unroll
        for (int tt = 0; tt < 8; ++tt) xd[tt] = (u32x2){*(const uint32_t*)(xa - 512 * tt), *(const uint32_t*)(xa - 512 * tt - 64)};
        SB();
        LOADF(kb, xb_, 1);
        epi_acc(acc[0], xd[0], ys + w * YS_STRIDE + 2 * (1022 - lo_), Dv);
        epi_acc(acc[1], xd[1], ys + w * YS_STRIDE + 2 * (1022 - lo_) - 512, Dv);
        MM20(ka, xa_);
        PAT(3);
        LOADF(ka, xa_, 2);
        epi_acc(acc[2], xd[2], ys + w * YS_STRIDE + 2 * (1022 - lo_) - 1024, Dv);
        epi_acc(acc[3], xd[3], ys + w * YS_STRIDE + 2 * (1022 - lo_) - 1536, Dv);
        MM20(kb, xb_);
        PAT(3);
        lds_barrier();
        SB();
        LOADF(kb, xb_, 3);
        epi_row(ys + qq * YS_STRIDE + (128 * w + 16 * g + 4 * pp) * 2, 4 * YS_STRIDE, qv[0], ob);
        MM20(ka, xa_);
        PAT(2);
        LOADF(ka, xa_, 4);
        epi_row(ys + qq * YS_STRIDE + (128 * w + 64 + 16 * g + 4 * pp) * 2, 4 * YS_STRIDE, qv[1], ob - (size_t)64 * (2 * DCH));
        MM20(kb, xb_);
        PAT(2);
        LOADF(kb, xb_, 5);
        MM20(ka, xa_);
        PAT(0);
        LOADF(ka, xa_, 6);
        MM20(kb, xb_);
        PAT(0);
        LOADF(kb, xb_, 7);
        MM20(ka, xa_);
        PAT(0);
        MM20(kb, xb_);
        SB();
        acc[7] += a7b;
    }
    STAMP(1, 4);
#pragma unroll
    for (int tt = 4; tt < 8; ++tt) epi_acc(acc[tt], xd[tt], cb + KA_OFF + 2 * (1022 - lo_) - 512 * (tt - 4), Dv);
    lds_barrier();
    STAMP(1, 5);
    epi_row(lds + qq * CH_STRIDE + KA_OFF + (128 * w + 16 * g + 4 * pp) * 2, 4 * CH_STRIDE, qv[2], ob + (size_t)1024 * (2 * DCH));
    epi_row(lds + qq * CH_STRIDE + KA_OFF + (128 * w + 64 + 16 * g + 4 * pp) * 2, 4 * CH_STRIDE, qv[3], ob + (size_t)960 * (2 * DCH));
    STAMP(1, 6);
    STAMPW(1, 7);
    KEEP(pf);
}

extern "C" void kernel_launch(void* const* d_in, const int* in_sizes, int n_in,
                              void* d_out, int out_size, void* d_ws, size_t ws_size,
                              hipStream_t stream) {
    const uint16_t* u = (const uint16_t*)d_in[0];
    const uint16_t* logA = (const uint16_t*)d_in[1];
    const uint16_t* Aim = (const uint16_t*)d_in[2];
    const uint16_t* Cre = (const uint16_t*)d_in[3];
    const uint16_t* Cim = (const uint16_t*)d_in[4];
    const uint16_t* logdt = (const uint16_t*)d_in[5];
    const uint16_t* Dsk = (const uint16_t*)d_in[6];
    const uint16_t* q = (const uint16_t*)d_in[7];
    const uint16_t* k = (const uint16_t*)d_in[8];
    const uint16_t* v = (const uint16_t*)d_in[9];
    const uint16_t* shk = (const uint16_t*)d_in[10];
    const uint16_t* ssmk = (const uint16_t*)d_in[11];
    const uint16_t* sD = (const uint16_t*)d_in[12];
    uint16_t* xw = (uint16_t*)d_ws;
    uint16_t* Kw = xw + (size_t)2 * DCH * LSEQ;
    prep_kernel<<<256, 512, 0, stream>>>(u, logA, Aim, Cre, Cim, logdt, k, v, shk, ssmk, q, xw, Kw);
    conv_kernel<<<256, 512, 0, stream>>>(xw, Kw, ssmk, q, Dsk, sD, (uint16_t*)d_out);
}
```

```cpp
#include <hip/hip_runtime.h>
#include <stdint.h>

typedef __attribute__((ext_vector_type(4))) unsigned int u32x4;
typedef __attribute__((ext_vector_type(2))) unsigned int u32x2;
typedef __attribute__((ext_vector_type(4))) float f32x4;
typedef __attribute__((ext_vector_type(2))) float f32x2;
typedef __attribute__((ext_vector_type(8))) _Float16 f16x8;
typedef __attribute__((ext_vector_type(2))) _Float16 f16x2;
typedef __attribute__((ext_vector_type(8))) __bf16 bf16x8;
typedef __attribute__((ext_vector_type(2))) __bf16 bf16x2;
typedef __attribute__((ext_vector_type(4))) short s16x4;

#define STAMP(k, i)
#define STAMPW(k, i)
#define RT(k, i)
#define LSEQ 2048
#define DCH 1024
#define NMODE 128

__device__ __forceinline__ float bf2f(uint16_t v) { return __uint_as_float(((uint32_t)v) << 16); }
__device__ __forceinline__ float lo_f(uint32_t w) { return __uint_as_float(w << 16); }
__device__ __forceinline__ float hi_f(uint32_t w) { return __uint_as_float(w & 0xffff0000u); }
__device__ __forceinline__ uint32_t pk_bf16(float lo, float hi) {
    f32x2 x = {lo, hi};
    bf16x2 y = __builtin_convertvector(x, bf16x2);
    return __builtin_bit_cast(uint32_t, y);
}
__device__ __forceinline__ uint32_t pk_f16(float lo, float hi) {
    f32x2 x = {lo, hi};
    f16x2 y = __builtin_convertvector(x, f16x2);
    return __builtin_bit_cast(uint32_t, y);
}
__device__ __forceinline__ void rbf2(float& a, float& b) {
    uint32_t p = pk_bf16(a, b);
    a = lo_f(p);
    b = hi_f(p);
}
__device__ __forceinline__ float rbf(float a) { return lo_f(pk_bf16(a, a)); }

struct TapTab { uint16_t v[LSEQ]; };
static constexpr TapTab make_tab() {
    TapTab t{};
    for (int j = 0; j < LSEQ; ++j) {
        int p = j;
        if (j >= 256) {
            int s = (j < 512) ? 1 : ((j < 1024) ? 2 : 3);
            int q = j >> s, r = j & ((1 << s) - 1), half = 1 << (s - 1);
            if (r > half || (r == half && (q & 1))) q++;
            p = q << s;
        }
        int col = (p >> 3) & 15, a = p >> 7, low = p & 7, row = 0;
        if (p < 256) row = 8 * a + low;
        else if (p < 512) row = 16 + 4 * (a - 2) + (low >> 1);
        else if (p < 1024) row = 24 + 2 * (a - 4) + (low >> 2);
        else row = 32 + (a - 8);
        t.v[j] = (uint16_t)(row * 16 + col);
    }
    return t;
}
__device__ const TapTab g_taptab = make_tab();

__device__ __forceinline__ uint32_t prefetch_code(int bytes) {
    uint64_t pc;
    asm volatile("s_getpc_b64 %0" : "=s"(pc));
    const int off = (int)threadIdx.x * 64;
    typedef __attribute__((address_space(1))) const uint32_t* gp_t;
    const uint32_t v = *(gp_t)((pc & ~63ull) + (off < bytes ? off : 0));
    __builtin_amdgcn_sched_barrier(0);
    return v;
}
__device__ __forceinline__ void WST(u32x4* p, u32x4 v) { asm volatile("global_store_dwordx4 %0, %1, off sc0 sc1" :: "v"(p), "v"(v) : "memory"); }
#define KEEP(v) asm volatile("" :: "v"(v))
__device__ __forceinline__ f32x2 cmul(f32x2 a, f32x2 b) {
    f32x2 t, c;
    asm("v_pk_mul_f32 %0, %1, %2 op_sel:[0,0] op_sel_hi:[0,1]" : "=v"(t) : "v"(a), "v"(b));
    asm("v_pk_fma_f32 %0, %1, %2, %3 op_sel:[1,1,0] op_sel_hi:[1,0,1] neg_lo:[1,0,0]" : "=v"(c) : "v"(a), "v"(b), "v"(t));
    return c;
}
__device__ __forceinline__ f32x2 anchor(float a, float b) {
    const float e = __builtin_amdgcn_exp2f(a);
    const float th = __builtin_amdgcn_fractf(b);
    return (f32x2){e * __builtin_amdgcn_cosf(th), e * __builtin_amdgcn_sinf(th)};
}
__device__ __forceinline__ uint32_t pk16(f32x2 c) { return pk_f16(c[0], c[1]); }

#define KG_ROWB 272
#define KG_TAB (64 * KG_ROWB)
#define KG_WAVE (KG_TAB + 1536)

__global__ __launch_bounds__(512) void prep_kernel(
    const uint16_t* __restrict__ u, const uint16_t* __restrict__ logA, const uint16_t* __restrict__ Aim,
    const uint16_t* __restrict__ Cre, const uint16_t* __restrict__ Cim, const uint16_t* __restrict__ logdt,
    const uint16_t* __restrict__ kin, const uint16_t* __restrict__ vin, const uint16_t* __restrict__ shk,
    const uint16_t* __restrict__ ssmk, const uint16_t* __restrict__ qin,
    uint16_t* __restrict__ xw, uint16_t* __restrict__ Kw)
{
    __shared__ __attribute__((aligned(16))) char lds[4 * KG_WAVE];
    const int lane = threadIdx.x & 63;
    const int w = __builtin_amdgcn_readfirstlane(threadIdx.x >> 6);
    const int b = blockIdx.x;
    const int xcd = b & 7, jb = b >> 3;
    RT(0, 0);
    STAMP(0, 0);
    const uint32_t pf = prefetch_code(10240);

    if (w < 4) {
        const int cg = lane & 15, rb = lane >> 4;
        const int s0 = 64 * jb + 32 * (w >> 1) + 8 * rb, c0 = 128 * xcd + 64 * (w & 1) + 4 * cg;
        u32x2 ur[8], kr[11], vr[8];
        u32x4 wv[2];
#pragma unroll
        for (int i = 0; i < 11; ++i) {
            const int s = s0 + i - 3;
            kr[i] = *(const u32x2*)(kin + (size_t)(s < 0 ? 0 : s) * DCH + c0);
            if (i < 3 && s < 0) kr[i] = (u32x2){0, 0};
        }
#pragma unroll
        for (int i = 0; i < 2; ++i) wv[i] = *(const u32x4*)(shk + (size_t)c0 * 4 + 8 * i);
#pragma unroll
        for (int i = 0; i < 8; ++i) vr[i] = *(const u32x2*)(vin + (size_t)(s0 + i) * DCH + c0);
#pragma unroll
        for (int i = 0; i < 8; ++i) ur[i] = *(const u32x2*)(u + (size_t)(s0 + i) * DCH + c0);
#pragma unroll
        for (int ch = 0; ch < 4; ++ch) {
            const uint32_t wlo = wv[ch >> 1][(ch & 1) * 2], whi = wv[ch >> 1][(ch & 1) * 2 + 1];
            const float w0 = lo_f(wlo), w1 = hi_f(wlo), w2 = lo_f(whi), w3 = hi_f(whi);
            float kf[11], vf[8];
#pragma unroll
            for (int i = 0; i < 11; ++i) kf[i] = (ch & 1) ? hi_f(kr[i][ch >> 1]) : lo_f(kr[i][ch >> 1]);
#pragma unroll
            for (int i = 0; i < 8; ++i) vf[i] = (ch & 1) ? hi_f(vr[i][ch >> 1]) : lo_f(vr[i][ch >> 1]);
            u32x4 o;
#pragma unroll
            for (int q = 0; q < 4; ++q) {
                const int ia = 7 - 2 * q, ib = 6 - 2 * q;
                float a0 = kf[ia] * w3, b0 = kf[ib] * w3;
                rbf2(a0, b0);
                float a1 = kf[ia + 1] * w2, b1 = kf[ib + 1] * w2;
                rbf2(a1, b1);
                a0 += a1; b0 += b1;
                rbf2(a0, b0);
                a1 = kf[ia + 2] * w1; b1 = kf[ib + 2] * w1;
                rbf2(a1, b1);
                a0 += a1; b0 += b1;
                rbf2(a0, b0);
                a1 = kf[ia + 3] * w0; b1 = kf[ib + 3] * w0;
                rbf2(a1, b1);
                a0 += a1; b0 += b1;
                rbf2(a0, b0);
                o[q] = pk_bf16(a0 * vf[ia], b0 * vf[ib]);
            }
            WST((u32x4*)(xw + (size_t)(DCH + c0 + ch) * LSEQ + (2040 - s0)), o);
        }
#pragma unroll
        for (int ch = 0; ch < 4; ++ch) {
            u32x4 o;
#pragma unroll
            for (int q = 0; q < 4; ++q)
                o[q] = __builtin_amdgcn_perm(ur[6 - 2 * q][ch >> 1], ur[7 - 2 * q][ch >> 1],
                                             (ch & 1) ? 0x07060302u : 0x05040100u);
            WST((u32x4*)(xw + (size_t)(c0 + ch) * LSEQ + (2040 - s0)), o);
        }
        STAMP(0, 1);
        STAMPW(0, 2);
        KEEP(pf);
        RT(0, 1);
        return;
    }

    __builtin_amdgcn_s_setprio(3);
    const int d = 128 * xcd + 4 * jb + (w - 4);
    char* T = lds + (w - 4) * KG_WAVE;
    u32x4 tabv[4];
#pragma unroll
    for (int i = 0; i < 4; ++i) tabv[i] = *(const u32x4*)(g_taptab.v + 8 * (64 * i + lane));

    const float dt = rbf(expf(bf2f(logdt[d])));
    float pa[2], pb[2], pcr[2], pci[2];
    float mx = 0.f;
#pragma unroll
    for (int bt = 0; bt < 2; ++bt) {
        const int n = d * NMODE + bt * 64 + lane;
        const float ar = -rbf(expf(bf2f(logA[n])));
        pa[bt] = (ar * dt) * 1.4426950408889634f;
        pb[bt] = (bf2f(Aim[n]) * dt) * 0.15915494309189535f;
        pcr[bt] = 2.f * (bf2f(Cre[n]) * dt);
        pci[bt] = 2.f * (bf2f(Cim[n]) * dt);
        mx = fmaxf(mx, fmaxf(fabsf(pcr[bt]), fabsf(pci[bt])));
    }
    uint32_t pf2;
    {
        const int t = threadIdx.x - 256;
        const uint16_t* p = (t < 128) ? ssmk + (size_t)(128 * xcd + 4 * jb) * LSEQ + t * 64
                                      : qin + (size_t)(64 * jb + ((t - 128) >> 1)) * DCH + 128 * xcd + 64 * (t & 1);
        pf2 = *(const uint32_t*)p;
        __builtin_amdgcn_sched_barrier(0);
    }
#pragma unroll
    for (int o = 32; o >= 1; o >>= 1) mx = fmaxf(mx, __shfl_xor(mx, o));
    int sf = 266 - (int)((__float_as_uint(mx) >> 23) & 0xff);
    sf = sf < 1 ? 1 : (sf > 239 ? 239 : sf);
    const float scl = __uint_as_float((uint32_t)sf << 23);
    const float inv = __uint_as_float((uint32_t)(240 - sf) << 23);

    STAMP(0, 1);
#pragma unroll
    for (int r = 41; r < 48; ++r) *(uint32_t*)(T + r * KG_ROWB + lane * 4) = 0u;

    f32x4 acc[3] = {{0, 0, 0, 0}, {0, 0, 0, 0}, {0, 0, 0, 0}};
    const int fr = lane & 15, kb = lane >> 4;
#pragma unroll 1
    for (int bt = 0; bt < 2; ++bt) {
        const float a = bt ? pa[1] : pa[0], bb = bt ? pb[1] : pb[0];
        const f32x2 z1 = anchor(a, bb), z8c = anchor(a * 8.f, bb * -8.f), z128 = anchor(a * 128.f, bb * 128.f);
        char* Tl = T + lane * 4;
        const f32x2 z16c = cmul(z8c, z8c), z256 = cmul(z128, z128), h2 = cmul(z1, z1);
        const f32x2 z32c = cmul(z16c, z16c), z512 = cmul(z256, z256), h4 = cmul(h2, h2);
        const f32x2 z64c = cmul(z32c, z32c), z1024 = cmul(z512, z512), h3 = cmul(h2, z1);
        f32x2 f[16], cg[17], h[8];
        f[0] = (f32x2){16384.f, 0.f};
        f[1] = z8c * 16384.f;
        f[2] = z16c * 16384.f;
        f[4] = z32c * 16384.f;
        f[8] = z64c * 16384.f;
        cg[0] = (f32x2){(bt ? pcr[1] : pcr[0]) * scl, (bt ? pci[1] : pci[0]) * scl};
        cg[1] = cmul(cg[0], z128);
        cg[2] = cmul(cg[0], z256);
        cg[4] = cmul(cg[0], z512);
        cg[8] = cmul(cg[0], z1024);
        h[1] = z1; h[2] = h2; h[3] = h3; h[4] = h4;
        h[5] = cmul(h4, z1); h[6] = cmul(h4, h2); h[7] = cmul(h4, h3);
        f[3] = cmul(f[2], z8c);
        cg[3] = cmul(cg[2], z128);
        cg[16] = cmul(cg[8], z1024);
#pragma unroll
        for (int i = 1; i < 4; ++i) { f[4 + i] = cmul(f[i], z32c); cg[4 + i] = cmul(cg[i], z512); }
#pragma unroll
        for (int i = 1; i < 8; ++i) { f[8 + i] = cmul(f[i], z64c); cg[8 + i] = cmul(cg[i], z1024); }
#pragma unroll
        for (int i = 0; i < 16; ++i) *(uint32_t*)(Tl + (48 + i) * KG_ROWB) = pk16(f[i]);
#pragma unroll
        for (int aa = 0; aa <= 16; ++aa) {
            if (aa < 2) {
                *(uint32_t*)(Tl + (8 * aa) * KG_ROWB) = pk16(cg[aa]);
#pragma unroll
                for (int e = 1; e < 8; ++e) *(uint32_t*)(Tl + (8 * aa + e) * KG_ROWB) = pk16(cmul(cg[aa], h[e]));
            } else if (aa < 4) {
                *(uint32_t*)(Tl + (16 + 4 * (aa - 2)) * KG_ROWB) = pk16(cg[aa]);
#pragma unroll
                for (int e = 1; e < 4; ++e) *(uint32_t*)(Tl + (16 + 4 * (aa - 2) + e) * KG_ROWB) = pk16(cmul(cg[aa], h[2 * e]));
            } else if (aa < 8) {
                *(uint32_t*)(Tl + (24 + 2 * (aa - 4)) * KG_ROWB) = pk16(cg[aa]);
                *(uint32_t*)(Tl + (24 + 2 * (aa - 4) + 1) * KG_ROWB) = pk16(cmul(cg[aa], h[4]));
            } else {
                *(uint32_t*)(Tl + (32 + (aa - 8)) * KG_ROWB) = pk16(cg[aa]);
            }
        }
        f16x8 bfr[4], afr[4][3];
#pragma unroll
        for (int ks = 0; ks < 4; ++ks) {
            bfr[ks] = *(const f16x8*)(T + (48 + fr) * KG_ROWB + ks * 64 + kb * 16);
#pragma unroll
            for (int rt = 0; rt < 3; ++rt) afr[ks][rt] = *(const f16x8*)(T + (16 * rt + fr) * KG_ROWB + ks * 64 + kb * 16);
        }
        __builtin_amdgcn_sched_barrier(0);
#pragma unroll
        for (int ks = 0; ks < 4; ++ks)
#pragma unroll
            for (int rt = 0; rt < 3; ++rt)
                acc[rt] = __builtin_amdgcn_mfma_f32_16x16x32_f16(afr[ks][rt], bfr[ks], acc[rt], 0, 0, 0);
    }
    STAMP(0, 2);
    uint16_t* KD = (uint16_t*)(T + KG_TAB);
#pragma unroll
    for (int rt = 0; rt < 3; ++rt)
#pragma unroll
        for (int r = 0; r < 4; r += 2) {
            uint32_t p = pk_bf16(acc[rt][r] * inv, acc[rt][r + 1] * inv);
            KD[(16 * rt + 4 * kb + r) * 16 + fr] = (uint16_t)p;
            KD[(16 * rt + 4 * kb + r + 1) * 16 + fr] = (uint16_t)(p >> 16);
        }
#pragma unroll
    for (int i = 0; i < 4; ++i) {
        u32x4 o;
#pragma unroll
        for (int q = 0; q < 4; ++q) {
            const uint32_t lo = KD[tabv[i][q] & 0xffffu], hi = KD[tabv[i][q] >> 16];
            o[q] = lo | (hi << 16);
        }
        WST((u32x4*)(Kw + (size_t)d * LSEQ + 8 * (64 * i + lane)), o);
    }
    STAMP(0, 3);
    STAMPW(0, 4);
    RT(0, 1);
    KEEP(pf);
    KEEP(pf2);
}

#define KA_OFF 0
#define KB_OFF 4640
#define XA_OFF 9216
#define XB_OFF 13440
#define CH_STRIDE 17600
#define YS_OFF (8 * CH_STRIDE)
#define YS_STRIDE 2112
#define CONV_LDS (YS_OFF + 8 * YS_STRIDE)

__device__ __forceinline__ void put_k(char* cb, int y, u32x4 d, uint32_t prev) {
    *(u32x4*)(cb + KA_OFF + 464 + 16 * y) = d;
    u32x4 o;
    o[0] = __builtin_amdgcn_alignbyte(d[0], prev, 2);
    o[1] = __builtin_amdgcn_alignbyte(d[1], d[0], 2);
    o[2] = __builtin_amdgcn_alignbyte(d[2], d[1], 2);
    o[3] = __builtin_amdgcn_alignbyte(d[3], d[2], 2);
    *(u32x4*)(cb + KB_OFF + 464 + 16 * y) = o;
}
__device__ __forceinline__ void put_x(char* cb, int y, u32x4 d) {
    *(u32x4*)(cb + XA_OFF + 16 * y) = d;
    *(uint32_t*)(cb + XB_OFF + 16 * y - 4) = d[0];
    u32x2 m = {d[1], d[2]};
    *(u32x2*)(cb + XB_OFF + 16 * y) = m;
    *(uint32_t*)(cb + XB_OFF + 16 * y + 8) = d[3];
}
__device__ __forceinline__ uint32_t prev_lane(uint32_t v, uint32_t carry) {
    return (uint32_t)__builtin_amdgcn_update_dpp((int)carry, (int)v, 0x138  , 0xf, 0xf, false);
}
typedef __attribute__((address_space(3))) char* lds_t;
typedef __attribute__((address_space(3))) const volatile u32x2* lds_v2;
typedef __attribute__((address_space(3))) const bf16x8* lds_k8;
__device__ __forceinline__ bf16x8 ldx(lds_t p) {
    u32x2 a = *(lds_v2)p, b = *(lds_v2)(p + 8);
    u32x4 v = {a[0], a[1], b[0], b[1]};
    return __builtin_bit_cast(bf16x8, v);
}
__device__ __forceinline__ bf16x8 ldk(lds_t p) { return *(lds_k8)p; }
#define MFMA(a, b, c) c = __builtin_amdgcn_mfma_f32_16x16x32_bf16(a, b, c, 0, 0, 0)
#define SB() __builtin_amdgcn_sched_barrier(0)
#define SG_DS(n) __builtin_amdgcn_sched_group_barrier(0x100, n, 0)
#define SG_MF(n) __builtin_amdgcn_sched_group_barrier(0x008, n, 0)
#define ST2_OFF 1488

__device__ __forceinline__ void epi_acc(const f32x4 a, const u32x2 xd2, char* dst, float Dv) {
#pragma unroll
    for (int hh = 0; hh < 2; ++hh) {
        const uint32_t xd = xd2[hh];
        float y1 = a[2 * hh + 1], y0 = a[2 * hh];
        rbf2(y1, y0);
        float t1 = lo_f(xd) * Dv, t0 = hi_f(xd) * Dv;
        rbf2(t1, t0);
        *(uint32_t*)(dst - 64 * hh) = pk_bf16(y1 + t1, y0 + t0);
    }
}
__device__ __forceinline__ void epi_row(const char* a0, int stride4, u32x4 qq4, uint16_t* dst) {
    s16x4 v0 = __builtin_amdgcn_ds_read_tr16_b64_v4i16((__attribute__((address_space(3))) s16x4*)(a0));
    s16x4 v1 = __builtin_amdgcn_ds_read_tr16_b64_v4i16((__attribute__((address_space(3))) s16x4*)(a0 + stride4));
    u32x2 p0 = __builtin_bit_cast(u32x2, v0), p1 = __builtin_bit_cast(u32x2, v1);
    u32x4 o = {p0[0], p0[1], p1[0], p1[1]};
#pragma unroll
    for (int j = 0; j < 4; ++j)
        o[j] = pk_bf16(lo_f(qq4[j]) * lo_f(o[j]), hi_f(qq4[j]) * hi_f(o[j]));
    *(u32x4*)dst = o;
}

#define SG_VA(n) __builtin_amdgcn_sched_group_barrier(0x002, n, 0)
__device__ __forceinline__ void lds_barrier() {
    __builtin_amdgcn_fence(__ATOMIC_RELEASE, "workgroup", "local");
    __builtin_amdgcn_s_barrier();
    __builtin_amdgcn_fence(__ATOMIC_ACQUIRE, "workgroup", "local");
}
#define LOADF(KF, XF, P) _Pragma("unroll") for (int m = 0; m < 8; ++m) { KF[m] = ldk(kaddr + 64 * (P) + 512 * m); XF[m] = ldx(xaddr + 64 * (P) + 512 * (7 - m)); }
#define MM20(K, X) \
    MFMA(K[4], X[3], acc[7]); MFMA(K[4], X[2], acc[6]); MFMA(K[3], X[4], a7b);    MFMA(K[4], X[1], acc[5]); \
    MFMA(K[5], X[2], acc[7]); MFMA(K[2], X[4], acc[6]); MFMA(K[2], X[5], a7b);    MFMA(K[4], X[0], acc[4]); \
    MFMA(K[6], X[1], acc[7]); MFMA(K[5], X[1], acc[6]); MFMA(K[1], X[6], a7b);    MFMA(K[1], X[4], acc[5]); \
    MFMA(K[7], X[0], acc[7]); MFMA(K[1], X[5], acc[6]); MFMA(K[0], X[7], a7b);    MFMA(K[5], X[0], acc[5]); \
    MFMA(K[6], X[0], acc[6]); MFMA(K[0], X[4], acc[4]); MFMA(K[0], X[6], acc[6]); MFMA(K[0], X[5], acc[5]);
#define PAT(nv) _Pragma("unroll") for (int i_ = 0; i_ < 13; ++i_) { SG_MF(1); SG_DS(2); SG_VA(nv); } \
                _Pragma("unroll") for (int i_ = 0; i_ < 7; ++i_) { SG_MF(1); SG_VA(nv); } SB();

__global__ __launch_bounds__(512) void conv_kernel(
    const uint16_t* __restrict__ xw, const uint16_t* __restrict__ Kw, const uint16_t* __restrict__ ssmk,
    const uint16_t* __restrict__ qin, const uint16_t* __restrict__ Dsk, const uint16_t* __restrict__ sD,
    uint16_t* __restrict__ out)
{
    __shared__ __attribute__((aligned(16))) char lds[CONV_LDS];
    const int b = blockIdx.x;
    const int xcd = b & 7, idx = b >> 3, part = idx >> 4, grp = xcd * 16 + (idx & 15);
    const int lane = threadIdx.x & 63;
    const int w = __builtin_amdgcn_readfirstlane(threadIdx.x >> 6);
    const int chl = grp * 8 + w;
    const uint16_t* ksrc = (part ? ssmk : Kw) + (size_t)chl * LSEQ;
    const uint16_t* xsrc = xw + (size_t)(part * DCH + chl) * LSEQ;
    char* cb = lds + w * CH_STRIDE;

    RT(1, 0);
    STAMP(1, 0);
    const uint32_t pf = prefetch_code(7168);
    const u32x4 k0 = *(const u32x4*)(ksrc + 8 * lane);
    SB();
    const u32x4 x3 = *(const u32x4*)(xsrc + 8 * (192 + lane));
    SB();
    const u32x4 k1 = *(const u32x4*)(ksrc + 8 * (64 + lane));
    SB();
    const u32x4 x2 = *(const u32x4*)(xsrc + 8 * (128 + lane));
    SB();

    const int g = lane >> 4, fr = lane & 15;
    {
        const u32x4 z = {0, 0, 0, 0};
        if (lane < 29) {
            *(u32x4*)(cb + KA_OFF + 16 * lane) = z;
            *(u32x4*)(cb + KB_OFF + 16 * lane) = z;
        }
        if (lane < 4) *(u32x4*)(cb + XA_OFF + 4096 + 16 * lane) = z;
        if (lane < 5) *(u32x4*)(cb + XB_OFF + 4080 + 16 * lane) = z;
    }
    put_k(cb, lane, k0, prev_lane(k0[3], 0u));
    const uint32_t c1 = __builtin_amdgcn_readlane(k0[3], 63);
    put_x(cb, 192 + lane, x3);
    put_k(cb, 64 + lane, k1, prev_lane(k1[3], c1));
    const uint32_t c2 = __builtin_amdgcn_readlane(k1[3], 63);
    put_x(cb, 128 + lane, x2);
    SB();
    const u32x4 k2 = *(const u32x4*)(ksrc + 8 * (128 + lane));
    SB();
    const u32x4 x1 = *(const u32x4*)(xsrc + 8 * (64 + lane));
    SB();
    const u32x4 k3 = *(const u32x4*)(ksrc + 8 * (192 + lane));
    SB();
    const u32x4 x0 = *(const u32x4*)(xsrc + 8 * lane);
    SB();
    const float Dv = bf2f((part ? sD : Dsk)[chl]);
    const int r0 = 1023 - (128 * w + lane);
    u32x4 qv[4];
#pragma unroll
    for (int i = 0; i < 4; ++i) qv[i] = (u32x4){0x3f803f80u, 0x3f803f80u, 0x3f803f80u, 0x3f803f80u};
    if (part) {
#pragma unroll
        for (int i = 0; i < 4; ++i)
            qv[i] = *(const u32x4*)(qin + (size_t)(r0 - 64 * (i & 1) + 1024 * (i >> 1)) * DCH + grp * 8);
    }
    SB();

    const lds_t lb = (lds_t)lds + w * CH_STRIDE;
    const lds_t kaddr = lb + ((fr & 1) ? KA_OFF : KB_OFF) + 2 * (32 * (fr >> 1) + 8 * g + 8);
    lds_t xaddr = lb + ((fr & 1) ? (XA_OFF + 2 * (1822 - 2 * fr + 8 * g)) : (XB_OFF + 2 * (1820 - 2 * fr + 8 * g))) - 3584;
    asm volatile("" : "+v"(xaddr));
    char* ys = lds + YS_OFF;
    const int lo_ = 64 * g + 2 * fr;
    const char* xa = cb + XA_OFF + 2 * (2046 - lo_);
    const int qq = (lane >> 2) & 3, pp = lane & 3;
    uint16_t* ob = out + part * DCH + grp * 8 + (size_t)r0 * (2 * DCH);

    f32x4 acc[8];
#pragma unroll
    for (int i = 0; i < 8; ++i) acc[i] = (f32x4){0, 0, 0, 0};
    f32x4 a7b = {0, 0, 0, 0};
    u32x2 xd[8];

    STAMP(1, 1);
    {
        bf16x8 ka[4], xa_[4], kb[4], xb_[4];
#pragma unroll
        for (int m = 0; m < 4; ++m) { ka[m] = ldk(kaddr + 512 * m); xa_[m] = ldx(xaddr + 512 * (7 - m)); }
#pragma unroll 1
        for (int c0 = 0; c0 < 8; c0 += 2) {
            const lds_t kp = kaddr + 64 * c0, xp = xaddr + 64 * c0;
#pragma unroll
            for (int m = 0; m < 4; ++m) { kb[m] = ldk(kp + 64 + 512 * m); xb_[m] = ldx(xp + 64 + 512 * (7 - m)); }
#pragma unroll
            for (int m = 0; m < 4; ++m)
#pragma unroll
                for (int t = 0; t < 4; ++t) MFMA(ka[m], xa_[t], acc[m + t]);
#pragma unroll
            for (int i = 0; i < 12; ++i) { SG_MF(1); SG_DS(1); }
            SG_MF(4);
            SB();
#pragma unroll
            for (int m = 0; m < 4; ++m) { ka[m] = ldk(kp + 128 + 512 * m); xa_[m] = ldx(xp + 128 + 512 * (7 - m)); }
#pragma unroll
            for (int m = 0; m < 4; ++m)
#pragma unroll
                for (int t = 0; t < 4; ++t) MFMA(kb[m], xb_[t], acc[m + t]);
#pragma unroll
            for (int i = 0; i < 12; ++i) { SG_MF(1); SG_DS(1); }
            SG_MF(4);
            SB();
        }
    }
    STAMP(1, 2);
    put_k(cb, 128 + lane, k2, prev_lane(k2[3], c2));
    const uint32_t c3 = __builtin_amdgcn_readlane(k2[3], 63);
    put_x(cb, 64 + lane, x1);
    put_k(cb, 192 + lane, k3, prev_lane(k3[3], c3));
    put_x(cb, lane, x0);
    SB();
    STAMP(1, 3);
    {
        bf16x8 ka[8], xa_[8], kb[8], xb_[8];
        LOADF(ka, xa_, 0);
#pragma unroll
        for (int tt = 0; tt < 8; ++tt) xd[tt] = (u32x2){*(const uint32_t*)(xa - 512 * tt), *(const uint32_t*)(xa - 512 * tt - 64)};
        SB();
        LOADF(kb, xb_, 1);
        epi_acc(acc[0], xd[0], ys + w * YS_STRIDE + 2 * (1022 - lo_), Dv);
        epi_acc(acc[1], xd[1], ys + w * YS_STRIDE + 2 * (1022 - lo_) - 512, Dv);
        MM20(ka, xa_);
        PAT(3);
        LOADF(ka, xa_, 2);
        epi_acc(acc[2], xd[2], ys + w * YS_STRIDE + 2 * (1022 - lo_) - 1024, Dv);
        epi_acc(acc[3], xd[3], ys + w * YS_STRIDE + 2 * (1022 - lo_) - 1536, Dv);
        MM20(kb, xb_);
        PAT(3);
#define ROWEPI1() do { \
            lds_barrier(); \
            epi_row(ys + qq * YS_STRIDE + (128 * w + 16 * g + 4 * pp) * 2, 4 * YS_STRIDE, qv[0], ob); \
            epi_row(ys + qq * YS_STRIDE + (128 * w + 64 + 16 * g + 4 * pp) * 2, 4 * YS_STRIDE, qv[1], ob - (size_t)64 * (2 * DCH)); \
            SB(); } while (0)
        LOADF(kb, xb_, 3);
        MM20(ka, xa_);
        PAT(0);
        if (w >= 4) ROWEPI1();
        LOADF(ka, xa_, 4);
        MM20(kb, xb_);
        PAT(0);
        LOADF(kb, xb_, 5);
        MM20(ka, xa_);
        PAT(0);
        LOADF(ka, xa_, 6);
        MM20(kb, xb_);
        PAT(0);
        if (w < 4) ROWEPI1();
        LOADF(kb, xb_, 7);
        MM20(ka, xa_);
        PAT(0);
        MM20(kb, xb_);
        SB();
        acc[7] += a7b;
    }
    STAMP(1, 4);
#pragma unroll
    for (int tt = 4; tt < 8; ++tt) epi_acc(acc[tt], xd[tt], cb + KA_OFF + 2 * (1022 - lo_) - 512 * (tt - 4), Dv);
    lds_barrier();
    STAMP(1, 5);
    epi_row(lds + qq * CH_STRIDE + KA_OFF + (128 * w + 16 * g + 4 * pp) * 2, 4 * CH_STRIDE, qv[2], ob + (size_t)1024 * (2 * DCH));
    epi_row(lds + qq * CH_STRIDE + KA_OFF + (128 * w + 64 + 16 * g + 4 * pp) * 2, 4 * CH_STRIDE, qv[3], ob + (size_t)960 * (2 * DCH));
    STAMP(1, 6);
    STAMPW(1, 7);
    KEEP(pf);
    RT(1, 1);
}

extern "C" void kernel_launch(void* const* d_in, const int* in_sizes, int n_in,
                              void* d_out, int out_size, void* d_ws, size_t ws_size,
                              hipStream_t stream) {
    const uint16_t* u = (const uint16_t*)d_in[0];
    const uint16_t* logA = (const uint16_t*)d_in[1];
    const uint16_t* Aim = (const uint16_t*)d_in[2];
    const uint16_t* Cre = (const uint16_t*)d_in[3];
    const uint16_t* Cim = (const uint16_t*)d_in[4];
    const uint16_t* logdt = (const uint16_t*)d_in[5];
    const uint16_t* Dsk = (const uint16_t*)d_in[6];
    const uint16_t* q = (const uint16_t*)d_in[7];
    const uint16_t* k = (const uint16_t*)d_in[8];
    const uint16_t* v = (const uint16_t*)d_in[9];
    const uint16_t* shk = (const uint16_t*)d_in[10];
    const uint16_t* ssmk = (const uint16_t*)d_in[11];
    const uint16_t* sD = (const uint16_t*)d_in[12];
    uint16_t* xw = (uint16_t*)d_ws;
    uint16_t* Kw = xw + (size_t)2 * DCH * LSEQ;
    prep_kernel<<<256, 512, 0, stream>>>(u, logA, Aim, Cre, Cim, logdt, k, v, shk, ssmk, q, xw, Kw);
    conv_kernel<<<256, 512, 0, stream>>>(xw, Kw, ssmk, q, Dsk, sD, (uint16_t*)d_out);
}
```

```cpp
#include <hip/hip_runtime.h>
#include <stdint.h>

typedef __attribute__((ext_vector_type(4))) unsigned int u32x4;
typedef __attribute__((ext_vector_type(2))) unsigned int u32x2;
typedef __attribute__((ext_vector_type(4))) float f32x4;
typedef __attribute__((ext_vector_type(2))) float f32x2;
typedef __attribute__((ext_vector_type(8))) _Float16 f16x8;
typedef __attribute__((ext_vector_type(2))) _Float16 f16x2;
typedef __attribute__((ext_vector_type(8))) __bf16 bf16x8;
typedef __attribute__((ext_vector_type(2))) __bf16 bf16x2;
typedef __attribute__((ext_vector_type(4))) short s16x4;

#define STAMP(k, i)
#define STAMPW(k, i)
#define RT(k, i)
#define LSEQ 2048
#define DCH 1024
#define NMODE 128

__device__ __forceinline__ float bf2f(uint16_t v) { return __uint_as_float(((uint32_t)v) << 16); }
__device__ __forceinline__ float lo_f(uint32_t w) { return __uint_as_float(w << 16); }
__device__ __forceinline__ float hi_f(uint32_t w) { return __uint_as_float(w & 0xffff0000u); }
__device__ __forceinline__ uint32_t pk_bf16(float lo, float hi) {
    f32x2 x = {lo, hi};
    bf16x2 y = __builtin_convertvector(x, bf16x2);
    return __builtin_bit_cast(uint32_t, y);
}
__device__ __forceinline__ uint32_t pk_f16(float lo, float hi) {
    f32x2 x = {lo, hi};
    f16x2 y = __builtin_convertvector(x, f16x2);
    return __builtin_bit_cast(uint32_t, y);
}
__device__ __forceinline__ void rbf2(float& a, float& b) {
    uint32_t p = pk_bf16(a, b);
    a = lo_f(p);
    b = hi_f(p);
}
__device__ __forceinline__ float rbf(float a) { return lo_f(pk_bf16(a, a)); }

struct TapTab { uint16_t v[LSEQ]; };
static constexpr TapTab make_tab() {
    TapTab t{};
    for (int j = 0; j < LSEQ; ++j) {
        int p = j;
        if (j >= 256) {
            int s = (j < 512) ? 1 : ((j < 1024) ? 2 : 3);
            int q = j >> s, r = j & ((1 << s) - 1), half = 1 << (s - 1);
            if (r > half || (r == half && (q & 1))) q++;
            p = q << s;
        }
        int col = (p >> 3) & 15, a = p >> 7, low = p & 7, row = 0;
        if (p < 256) row = 8 * a + low;
        else if (p < 512) row = 16 + 4 * (a - 2) + (low >> 1);
        else if (p < 1024) row = 24 + 2 * (a - 4) + (low >> 2);
        else row = 32 + (a - 8);
        t.v[j] = (uint16_t)(row * 16 + col);
    }
    return t;
}
__device__ const TapTab g_taptab = make_tab();

__device__ __forceinline__ uint32_t prefetch_code(int bytes) {
    uint64_t pc;
    asm volatile("s_getpc_b64 %0" : "=s"(pc));
    const int off = (int)threadIdx.x * 64;
    typedef __attribute__((address_space(1))) const uint32_t* gp_t;
    const uint32_t v = *(gp_t)((pc & ~63ull) + (off < bytes ? off : 0));
    __builtin_amdgcn_sched_barrier(0);
    return v;
}
__device__ __forceinline__ void WST(u32x4* p, u32x4 v) { asm volatile("global_store_dwordx4 %0, %1, off sc0 sc1" :: "v"(p), "v"(v) : "memory"); }
#define KEEP(v) asm volatile("" :: "v"(v))
__device__ __forceinline__ f32x2 cmul(f32x2 a, f32x2 b) {
    f32x2 t, c;
    asm("v_pk_mul_f32 %0, %1, %2 op_sel:[0,0] op_sel_hi:[0,1]" : "=v"(t) : "v"(a), "v"(b));
    asm("v_pk_fma_f32 %0, %1, %2, %3 op_sel:[1,1,0] op_sel_hi:[1,0,1] neg_lo:[1,0,0]" : "=v"(c) : "v"(a), "v"(b), "v"(t));
    return c;
}
__device__ __forceinline__ f32x2 anchor(float a, float b) {
    const float e = __builtin_amdgcn_exp2f(a);
    const float th = __builtin_amdgcn_fractf(b);
    return (f32x2){e * __builtin_amdgcn_cosf(th), e * __builtin_amdgcn_sinf(th)};
}
__device__ __forceinline__ uint32_t pk16(f32x2 c) { return pk_f16(c[0], c[1]); }

#define KG_ROWB 272
#define KG_BUF (16 * KG_ROWB)

#define KA_OFF 0
#define KB_OFF 4640
#define XA_OFF 9216
#define XB_OFF 13440
#define CH_STRIDE 17600
#define KD_OFF (8 * CH_STRIDE)
#define FLAG_OFF (KD_OFF + 8 * 1536)
#define LDS_TOTAL (FLAG_OFF + 160)

__device__ __forceinline__ void put_k(char* cb, int y, u32x4 d, uint32_t prev) {
    *(u32x4*)(cb + KA_OFF + 464 + 16 * y) = d;
    u32x4 o;
    o[0] = __builtin_amdgcn_alignbyte(d[0], prev, 2);
    o[1] = __builtin_amdgcn_alignbyte(d[1], d[0], 2);
    o[2] = __builtin_amdgcn_alignbyte(d[2], d[1], 2);
    o[3] = __builtin_amdgcn_alignbyte(d[3], d[2], 2);
    *(u32x4*)(cb + KB_OFF + 464 + 16 * y) = o;
}
__device__ __forceinline__ void put_x4(char* p, uint32_t o0, uint32_t o1) {
    *(u32x2*)(p + XA_OFF) = (u32x2){o0, o1};
    *(uint32_t*)(p + XB_OFF - 4) = o0;
    *(uint32_t*)(p + XB_OFF) = o1;
}
__device__ __forceinline__ uint32_t prev_lane(uint32_t v, uint32_t carry) {
    return (uint32_t)__builtin_amdgcn_update_dpp((int)carry, (int)v, 0x138  , 0xf, 0xf, false);
}
typedef __attribute__((address_space(3))) char* lds_t;
typedef __attribute__((address_space(3))) const volatile u32x2* lds_v2;
typedef __attribute__((address_space(3))) volatile uint32_t* lds_vu;
typedef __attribute__((address_space(3))) const bf16x8* lds_k8;
__device__ __forceinline__ bf16x8 ldx(lds_t p) {
    u32x2 a = *(lds_v2)p, b = *(lds_v2)(p + 8);
    u32x4 v = {a[0], a[1], b[0], b[1]};
    return __builtin_bit_cast(bf16x8, v);
}
__device__ __forceinline__ bf16x8 ldk(lds_t p) { return *(lds_k8)p; }
#define MFMA(a, b, c) c = __builtin_amdgcn_mfma_f32_16x16x32_bf16(a, b, c, 0, 0, 0)
#define SB() __builtin_amdgcn_sched_barrier(0)
#define SG_DS(n) __builtin_amdgcn_sched_group_barrier(0x100, n, 0)
#define SG_MF(n) __builtin_amdgcn_sched_group_barrier(0x008, n, 0)

__device__ __forceinline__ void epi_acc(const f32x4 a, const char* xa, char* dst, float Dv) {
#pragma unroll
    for (int hh = 0; hh < 2; ++hh) {
        const uint32_t xd = *(const uint32_t*)(xa - 64 * hh);
        float y1 = a[2 * hh + 1], y0 = a[2 * hh];
        rbf2(y1, y0);
        float t1 = lo_f(xd) * Dv, t0 = hi_f(xd) * Dv;
        rbf2(t1, t0);
        *(uint32_t*)(dst - 64 * hh) = pk_bf16(y1 + t1, y0 + t0);
    }
}
__device__ __forceinline__ void epi_row(const char* a0, u32x4 qq4, uint16_t* dst) {
    s16x4 v0 = __builtin_amdgcn_ds_read_tr16_b64_v4i16((__attribute__((address_space(3))) s16x4*)(a0));
    s16x4 v1 = __builtin_amdgcn_ds_read_tr16_b64_v4i16((__attribute__((address_space(3))) s16x4*)(a0 + 4 * CH_STRIDE));
    u32x2 p0 = __builtin_bit_cast(u32x2, v0), p1 = __builtin_bit_cast(u32x2, v1);
    u32x4 o = {p0[0], p0[1], p1[0], p1[1]};
#pragma unroll
    for (int j = 0; j < 4; ++j)
        o[j] = pk_bf16(lo_f(qq4[j]) * lo_f(o[j]), hi_f(qq4[j]) * hi_f(o[j]));
    *(u32x4*)dst = o;
}
__device__ __forceinline__ void lds_barrier() {
    __builtin_amdgcn_fence(__ATOMIC_RELEASE, "workgroup", "local");
    __builtin_amdgcn_s_barrier();
    __builtin_amdgcn_fence(__ATOMIC_ACQUIRE, "workgroup", "local");
}
__device__ __forceinline__ void flag_set(lds_t p) {
    __builtin_amdgcn_fence(__ATOMIC_RELEASE, "workgroup", "local");
    *(lds_vu)p = 1u;
}
__device__ __forceinline__ void flag_wait(lds_t p) {
    while (__builtin_amdgcn_ballot_w64(*(lds_vu)p == 0u) != 0ull) __builtin_amdgcn_s_sleep(1);
    __builtin_amdgcn_fence(__ATOMIC_ACQUIRE, "workgroup", "local");
}

template <int T0>
__device__ __forceinline__ void stage(lds_t kaddr, lds_t xaddr, f32x4 (&acc)[8]) {
    constexpr int NK = 8 - T0;
    bf16x8 ka[NK], xa[2], kb[NK], xb[2];
#pragma unroll
    for (int m = 0; m < NK; ++m) ka[m] = ldk(kaddr + 512 * m);
#pragma unroll
    for (int t = 0; t < 2; ++t) xa[t] = ldx(xaddr + 512 * (7 - T0 - t));
#pragma unroll 1
    for (int c0 = 0; c0 < 8; c0 += 2) {
        const lds_t kp = kaddr + 64 * c0, xp = xaddr + 64 * c0;
#pragma unroll
        for (int t = 0; t < 2; ++t) xb[t] = ldx(xp + 64 + 512 * (7 - T0 - t));
#pragma unroll
        for (int m = 0; m < NK; ++m) kb[m] = ldk(kp + 64 + 512 * m);
#pragma unroll
        for (int m = 0; m < NK; ++m) MFMA(ka[m], xa[0], acc[T0 + m]);
#pragma unroll
        for (int m = 0; m < NK - 1; ++m) MFMA(ka[m], xa[1], acc[T0 + 1 + m]);
#pragma unroll
        for (int i = 0; i < NK + 4; ++i) { SG_MF(1); SG_DS(1); }
        SG_MF(NK);
        SB();
#pragma unroll
        for (int t = 0; t < 2; ++t) xa[t] = ldx(xp + 128 + 512 * (7 - T0 - t));
#pragma unroll
        for (int m = 0; m < NK; ++m) ka[m] = ldk(kp + 128 + 512 * m);
#pragma unroll
        for (int m = 0; m < NK; ++m) MFMA(kb[m], xb[0], acc[T0 + m]);
#pragma unroll
        for (int m = 0; m < NK - 1; ++m) MFMA(kb[m], xb[1], acc[T0 + 1 + m]);
#pragma unroll
        for (int i = 0; i < NK + 4; ++i) { SG_MF(1); SG_DS(1); }
        SG_MF(NK);
        SB();
    }
}

__global__ __launch_bounds__(512) void fused_kernel(
    const uint16_t* __restrict__ u, const uint16_t* __restrict__ logA, const uint16_t* __restrict__ Aim,
    const uint16_t* __restrict__ Cre, const uint16_t* __restrict__ Cim, const uint16_t* __restrict__ logdt,
    const uint16_t* __restrict__ Dsk, const uint16_t* __restrict__ qin, const uint16_t* __restrict__ kin,
    const uint16_t* __restrict__ vin, const uint16_t* __restrict__ shk, const uint16_t* __restrict__ ssmk,
    const uint16_t* __restrict__ sD, uint16_t* __restrict__ out)
{
    __shared__ __attribute__((aligned(16))) char lds[LDS_TOTAL];
    const int b = blockIdx.x;
    const int xcd = b & 7, idx = b >> 3, part = idx >> 4, gi = idx & 15, grp = xcd * 16 + gi;
    const int lane = threadIdx.x & 63;
    const int w = __builtin_amdgcn_readfirstlane(threadIdx.x >> 6);
    const int chl = grp * 8 + w;
    char* cb = lds + w * CH_STRIDE;
    const lds_t flags = (lds_t)lds + FLAG_OFF;
    const int g = lane >> 4, fr = lane & 15;
    const int s0 = 256 * w + 4 * lane;

    RT(0, 0);
    STAMP(0, 0);
    STAMP(1, 0);
    const uint32_t pf = prefetch_code(24576);
    u32x4 kq[4], r1[4], r2[4], hk[3];
    uint32_t pr[8], pfa = 0, pfb = 0;
    u32x4 tabv[4];
    float dt = 0.f;
    const uint16_t* pfp = nullptr;
    if (part) {
        const uint16_t* ksrc = ssmk + (size_t)chl * LSEQ;
#pragma unroll
        for (int i = 0; i < 4; ++i) kq[i] = *(const u32x4*)(ksrc + 8 * (64 * i + lane));
        SB();
#pragma unroll
        for (int i = 0; i < 4; ++i) r1[i] = *(const u32x4*)(kin + (size_t)(s0 + i) * DCH + grp * 8);
#pragma unroll
        for (int i = 0; i < 4; ++i) r2[i] = *(const u32x4*)(vin + (size_t)(s0 + i) * DCH + grp * 8);
#pragma unroll
        for (int i = 0; i < 3; ++i) {
            hk[i] = *(const u32x4*)(kin + (size_t)(w ? 256 * w - 3 + i : 0) * DCH + grp * 8);
            if (!w) hk[i] = (u32x4){0, 0, 0, 0};
        }
        SB();
    } else {
        dt = bf2f(logdt[chl]);
#pragma unroll
        for (int bt = 0; bt < 2; ++bt) {
            const int n = chl * NMODE + bt * 64 + lane;
            pr[4 * bt] = logA[n]; pr[4 * bt + 1] = Aim[n]; pr[4 * bt + 2] = Cre[n]; pr[4 * bt + 3] = Cim[n];
        }
#pragma unroll
        for (int i = 0; i < 4; ++i) tabv[i] = *(const u32x4*)(g_taptab.v + 8 * (64 * i + lane));
        SB();
#pragma unroll
        for (int i = 0; i < 4; ++i) r1[i] = *(const u32x4*)(u + (size_t)(s0 + i) * DCH + grp * 8);
        SB();
        const int t = threadIdx.x;
        pfp = ((t < 128) ? kin : (t < 256) ? vin : (t < 384) ? u : qin) + (size_t)(64 * gi + ((t & 127) >> 1)) * DCH + 128 * xcd + 64 * (t & 1);
        pfa = *(const uint32_t*)pfp;
        SB();
    }
    {
        const u32x4 z = {0, 0, 0, 0};
        if (lane < 4) *(u32x4*)(cb + XA_OFF + 4096 + 16 * lane) = z;
        if (lane < 5) *(u32x4*)(cb + XB_OFF + 4080 + 16 * lane) = z;
        if (threadIdx.x < 40) *(uint32_t*)(lds + FLAG_OFF + 4 * threadIdx.x) = 0u;
    }
    lds_barrier();

    if (!part) {
        if (w < 4) __builtin_amdgcn_s_setprio(2);
        char* T = cb;
        dt = rbf(expf(dt));
        float pa[2], pb[2], pcr[2], pci[2];
        float mx = 0.f;
#pragma unroll
        for (int bt = 0; bt < 2; ++bt) {
            const float ar = -rbf(expf(lo_f(pr[4 * bt])));
            pa[bt] = (ar * dt) * 1.4426950408889634f;
            pb[bt] = (lo_f(pr[4 * bt + 1]) * dt) * 0.15915494309189535f;
            pcr[bt] = 2.f * (lo_f(pr[4 * bt + 2]) * dt);
            pci[bt] = 2.f * (lo_f(pr[4 * bt + 3]) * dt);
            mx = fmaxf(mx, fmaxf(fabsf(pcr[bt]), fabsf(pci[bt])));
        }
#pragma unroll
        for (int o = 32; o >= 1; o >>= 1) mx = fmaxf(mx, __shfl_xor(mx, o));
        int sf = 266 - (int)((__float_as_uint(mx) >> 23) & 0xff);
        sf = sf < 1 ? 1 : (sf > 239 ? 239 : sf);
        const float scl = __uint_as_float((uint32_t)sf << 23);
        const float inv = __uint_as_float((uint32_t)(240 - sf) << 23);
        STAMP(0, 1);
        pfb = *(const uint32_t*)(pfp + (size_t)1024 * DCH);
        SB();

        f32x4 ac[3] = {{0, 0, 0, 0}, {0, 0, 0, 0}, {0, 0, 0, 0}};
#pragma unroll 1
        for (int bt = 0; bt < 2; ++bt) {
            const float a = bt ? pa[1] : pa[0], bb = bt ? pb[1] : pb[0];
            const f32x2 z1 = anchor(a, bb), z8c = anchor(a * 8.f, bb * -8.f), z128 = anchor(a * 128.f, bb * 128.f);
            char* Tl = T + lane * 4;
            const char* Tr = T + fr * KG_ROWB + g * 16;
            const f32x2 z16c = cmul(z8c, z8c), z256 = cmul(z128, z128), h2 = cmul(z1, z1);
            const f32x2 z32c = cmul(z16c, z16c), z512 = cmul(z256, z256), h4 = cmul(h2, h2);
            const f32x2 z64c = cmul(z32c, z32c), z1024 = cmul(z512, z512), h3 = cmul(h2, z1);
            f32x2 f[16], cg[17], h[8];
            f[0] = (f32x2){16384.f, 0.f};
            f[1] = z8c * 16384.f;
            f[2] = z16c * 16384.f;
            f[4] = z32c * 16384.f;
            f[8] = z64c * 16384.f;
            cg[0] = (f32x2){(bt ? pcr[1] : pcr[0]) * scl, (bt ? pci[1] : pci[0]) * scl};
            cg[1] = cmul(cg[0], z128);
            cg[2] = cmul(cg[0], z256);
            cg[4] = cmul(cg[0], z512);
            cg[8] = cmul(cg[0], z1024);
            h[1] = z1; h[2] = h2; h[3] = h3; h[4] = h4;
            h[5] = cmul(h4, z1); h[6] = cmul(h4, h2); h[7] = cmul(h4, h3);
            f[3] = cmul(f[2], z8c);
            cg[3] = cmul(cg[2], z128);
            cg[16] = cmul(cg[8], z1024);
#pragma unroll
            for (int i = 1; i < 4; ++i) { f[4 + i] = cmul(f[i], z32c); cg[4 + i] = cmul(cg[i], z512); }
#pragma unroll
            for (int i = 1; i < 8; ++i) { f[8 + i] = cmul(f[i], z64c); cg[8 + i] = cmul(cg[i], z1024); }
            f16x8 bfr[4], afr[3][4];
#pragma unroll
            for (int i = 0; i < 16; ++i) *(uint32_t*)(Tl + i * KG_ROWB) = pk16(f[i]);
#pragma unroll
            for (int aa = 0; aa < 2; ++aa) {
                *(uint32_t*)(Tl + KG_BUF + (8 * aa) * KG_ROWB) = pk16(cg[aa]);
#pragma unroll
                for (int e = 1; e < 8; ++e) *(uint32_t*)(Tl + KG_BUF + (8 * aa + e) * KG_ROWB) = pk16(cmul(cg[aa], h[e]));
            }
#pragma unroll
            for (int ks = 0; ks < 4; ++ks) {
                bfr[ks] = *(const f16x8*)(Tr + ks * 64);
                afr[0][ks] = *(const f16x8*)(Tr + KG_BUF + ks * 64);
            }
#pragma unroll
            for (int aa = 2; aa < 4; ++aa) {
                *(uint32_t*)(Tl + (4 * (aa - 2)) * KG_ROWB) = pk16(cg[aa]);
#pragma unroll
                for (int e = 1; e < 4; ++e) *(uint32_t*)(Tl + (4 * (aa - 2) + e) * KG_ROWB) = pk16(cmul(cg[aa], h[2 * e]));
            }
#pragma unroll
            for (int aa = 4; aa < 8; ++aa) {
                *(uint32_t*)(Tl + (8 + 2 * (aa - 4)) * KG_ROWB) = pk16(cg[aa]);
                *(uint32_t*)(Tl + (8 + 2 * (aa - 4) + 1) * KG_ROWB) = pk16(cmul(cg[aa], h[4]));
            }
#pragma unroll
            for (int aa = 8; aa <= 16; ++aa) *(uint32_t*)(Tl + KG_BUF + (aa - 8) * KG_ROWB) = pk16(cg[aa]);
#pragma unroll
            for (int ks = 0; ks < 4; ++ks) {
                afr[1][ks] = *(const f16x8*)(Tr + ks * 64);
                afr[2][ks] = *(const f16x8*)(Tr + KG_BUF + ks * 64);
            }
#pragma unroll
            for (int ks = 0; ks < 4; ++ks)
#pragma unroll
                for (int rt = 0; rt < 3; ++rt)
                    ac[rt] = __builtin_amdgcn_mfma_f32_16x16x32_f16(afr[rt][ks], bfr[ks], ac[rt], 0, 0, 0);
        }
        STAMP(0, 2);
        uint16_t* KD = (uint16_t*)(lds + KD_OFF + w * 1536);
#pragma unroll
        for (int rt = 0; rt < 3; ++rt)
#pragma unroll
            for (int r = 0; r < 4; r += 2) {
                uint32_t p = pk_bf16(ac[rt][r] * inv, ac[rt][r + 1] * inv);
                KD[(16 * rt + 4 * g + r) * 16 + fr] = (uint16_t)p;
                KD[(16 * rt + 4 * g + r + 1) * 16 + fr] = (uint16_t)(p >> 16);
            }
#pragma unroll
        for (int i = 0; i < 4; ++i)
#pragma unroll
            for (int q = 0; q < 4; ++q) {
                const uint32_t lo = KD[tabv[i][q] & 0xffffu], hi = KD[tabv[i][q] >> 16];
                kq[i][q] = lo | (hi << 16);
            }
    }
    {
        const u32x4 z = {0, 0, 0, 0};
        if (lane < 29) {
            *(u32x4*)(cb + KA_OFF + 16 * lane) = z;
            *(u32x4*)(cb + KB_OFF + 16 * lane) = z;
        }
        uint32_t c = 0;
#pragma unroll
        for (int i = 0; i < 4; ++i) {
            put_k(cb, 64 * i + lane, kq[i], prev_lane(kq[i][3], c));
            c = __builtin_amdgcn_readlane(kq[i][3], 63);
        }
    }
    STAMP(0, 3);

    auto xfill = [&]() {
        char* xp = lds + 2 * (2044 - s0);
        if (part) {
            u32x4 kr[7];
#pragma unroll
            for (int i = 0; i < 3; ++i)
#pragma unroll
                for (int j = 0; j < 4; ++j) kr[i][j] = prev_lane(r1[1 + i][j], hk[i][j]);
#pragma unroll
            for (int i = 0; i < 4; ++i) kr[3 + i] = r1[i];
            const u32x4* wp = (const u32x4*)(shk + (size_t)grp * 32);
#pragma unroll
            for (int ch = 0; ch < 8; ++ch) {
                const u32x4 wv = wp[ch >> 1];
                const uint32_t wlo = wv[(ch & 1) * 2], whi = wv[(ch & 1) * 2 + 1];
                const float w0 = lo_f(wlo), w1 = hi_f(wlo), w2 = lo_f(whi), w3 = hi_f(whi);
                float kf[7], vf[4];
#pragma unroll
                for (int i = 0; i < 7; ++i) kf[i] = (ch & 1) ? hi_f(kr[i][ch >> 1]) : lo_f(kr[i][ch >> 1]);
#pragma unroll
                for (int i = 0; i < 4; ++i) vf[i] = (ch & 1) ? hi_f(r2[i][ch >> 1]) : lo_f(r2[i][ch >> 1]);
                uint32_t o[2];
#pragma unroll
                for (int q = 0; q < 2; ++q) {
                    const int ia = 3 - 2 * q, ib = 2 - 2 * q;
                    float a0 = kf[ia] * w3, b0 = kf[ib] * w3;
                    rbf2(a0, b0);
                    float a1 = kf[ia + 1] * w2, b1 = kf[ib + 1] * w2;
                    rbf2(a1, b1);
                    a0 += a1; b0 += b1;
                    rbf2(a0, b0);
                    a1 = kf[ia + 2] * w1; b1 = kf[ib + 2] * w1;
                    rbf2(a1, b1);
                    a0 += a1; b0 += b1;
                    rbf2(a0, b0);
                    a1 = kf[ia + 3] * w0; b1 = kf[ib + 3] * w0;
                    rbf2(a1, b1);
                    a0 += a1; b0 += b1;
                    rbf2(a0, b0);
                    o[q] = pk_bf16(a0 * vf[ia], b0 * vf[ib]);
                }
                put_x4(xp + ch * CH_STRIDE, o[0], o[1]);
            }
        } else {
#pragma unroll
            for (int ch = 0; ch < 8; ++ch) {
                const uint32_t sel = (ch & 1) ? 0x07060302u : 0x05040100u;
                put_x4(xp + ch * CH_STRIDE, __builtin_amdgcn_perm(r1[2][ch >> 1], r1[3][ch >> 1], sel),
                       __builtin_amdgcn_perm(r1[0][ch >> 1], r1[1][ch >> 1], sel));
            }
        }
        flag_set(flags + 4 * w);
    };
    if (w < 4) xfill();
    __builtin_amdgcn_s_setprio(0);
    STAMP(0, 4);

    const float Dv = bf2f((part ? sD : Dsk)[chl]);
    const int r0 = 511 - (64 * w + lane);
    u32x4 qv[4];
#pragma unroll
    for (int i = 0; i < 4; ++i) qv[i] = (u32x4){0x3f803f80u, 0x3f803f80u, 0x3f803f80u, 0x3f803f80u};
    if (part) {
#pragma unroll
        for (int i = 0; i < 4; ++i) qv[i] = *(const u32x4*)(qin + (size_t)(r0 + 512 * i) * DCH + grp * 8);
    }
    SB();

    const lds_t lb = (lds_t)lds + w * CH_STRIDE;
    const lds_t kaddr = lb + ((fr & 1) ? KA_OFF : KB_OFF) + 2 * (32 * (fr >> 1) + 8 * g + 8);
    lds_t xaddr = lb + ((fr & 1) ? (XA_OFF + 2 * (1822 - 2 * fr + 8 * g)) : (XB_OFF + 2 * (1820 - 2 * fr + 8 * g))) - 3584;
    asm volatile("" : "+v"(xaddr));
    const int lo_ = 64 * g + 2 * fr;
    const char* xa = cb + XA_OFF + 2 * (2046 - lo_);
    char* sg = cb + 2 * (510 - lo_);
    const int qq = (lane >> 2) & 3, pp = lane & 3;
    const char* rg = lds + qq * CH_STRIDE + (64 * w + 16 * g + 4 * pp) * 2;
    uint16_t* ob = out + part * DCH + grp * 8 + (size_t)r0 * (2 * DCH);
#define SG0 (KA_OFF + 3536)
#define SG1 (KB_OFF + 3536)
#define SG2 (KA_OFF + 2512)
#define SG3 (KB_OFF + 2512)
#define EPI_ACC(S, OFF) do { \
        epi_acc(acc[2 * (S)], xa - 512 * (2 * (S)), sg + (OFF), Dv); \
        epi_acc(acc[2 * (S) + 1], xa - 512 * (2 * (S) + 1), sg + (OFF) - 512, Dv); \
        flag_set(flags + 32 + 32 * (S) + 4 * w); } while (0)
#define EPI_ROW(S, OFF) do { \
        flag_wait(flags + 32 + 32 * (S) + 4 * (lane & 7)); \
        epi_row(rg + (OFF), qv[S], ob + (size_t)(512 * (S)) * (2 * DCH)); } while (0)

    f32x4 acc[8];
#pragma unroll
    for (int i = 0; i < 8; ++i) acc[i] = (f32x4){0, 0, 0, 0};

    flag_wait(flags + 4 * (lane & 1));
    STAMP(0, 5);
    stage<0>(kaddr, xaddr, acc);
    STAMP(0, 6);
    EPI_ACC(0, SG0);
    if (w >= 4) xfill();
    STAMP(0, 7);
    flag_wait(flags + 8 + 4 * (lane & 1));
    STAMP(1, 1);
    stage<2>(kaddr, xaddr, acc);
    EPI_ACC(1, SG1);
    EPI_ROW(0, SG0);
    STAMP(1, 2);
    flag_wait(flags + 16 + 4 * (lane & 1));
    STAMP(1, 3);
    stage<4>(kaddr, xaddr, acc);
    EPI_ACC(2, SG2);
    EPI_ROW(1, SG1);
    STAMP(1, 4);
    flag_wait(flags + 24 + 4 * (lane & 1));
    {
        f32x4 a7b = {0, 0, 0, 0};
#pragma unroll
        for (int c = 0; c < 8; ++c) {
            const bf16x8 k0 = ldk(kaddr + 64 * c), k1 = ldk(kaddr + 64 * c + 512);
            const bf16x8 x6 = ldx(xaddr + 64 * c + 512), x7 = ldx(xaddr + 64 * c);
            MFMA(k0, x6, acc[6]);
            MFMA(k1, x6, acc[7]);
            MFMA(k0, x7, a7b);
        }
        acc[7] += a7b;
    }
    STAMP(1, 5);
    EPI_ACC(3, SG3);
    EPI_ROW(2, SG2);
    EPI_ROW(3, SG3);
    STAMP(1, 6);
    STAMPW(1, 7);
    KEEP(pf);
    KEEP(pfa);
    KEEP(pfb);
    RT(0, 1);
}

extern "C" void kernel_launch(void* const* d_in, const int* in_sizes, int n_in,
                              void* d_out, int out_size, void* d_ws, size_t ws_size,
                              hipStream_t stream) {
    fused_kernel<<<256, 512, 0, stream>>>(
        (const uint16_t*)d_in[0], (const uint16_t*)d_in[1], (const uint16_t*)d_in[2], (const uint16_t*)d_in[3],
        (const uint16_t*)d_in[4], (const uint16_t*)d_in[5], (const uint16_t*)d_in[6], (const uint16_t*)d_in[7],
        (const uint16_t*)d_in[8], (const uint16_t*)d_in[9], (const uint16_t*)d_in[10], (const uint16_t*)d_in[11],
        (const uint16_t*)d_in[12], (uint16_t*)d_out);
}
```

```cpp
#include <hip/hip_runtime.h>
#include <stdint.h>

typedef __attribute__((ext_vector_type(4))) unsigned int u32x4;
typedef __attribute__((ext_vector_type(2))) unsigned int u32x2;
typedef __attribute__((ext_vector_type(4))) float f32x4;
typedef __attribute__((ext_vector_type(2))) float f32x2;
typedef __attribute__((ext_vector_type(8))) _Float16 f16x8;
typedef __attribute__((ext_vector_type(2))) _Float16 f16x2;
typedef __attribute__((ext_vector_type(8))) __bf16 bf16x8;
typedef __attribute__((ext_vector_type(2))) __bf16 bf16x2;
typedef __attribute__((ext_vector_type(4))) short s16x4;

#define STAMP(k, i)
#define STAMPW(k, i)
#define RT(k, i)
#define LSEQ 2048
#define DCH 1024
#define NMODE 128

__device__ __forceinline__ float bf2f(uint16_t v) { return __uint_as_float(((uint32_t)v) << 16); }
__device__ __forceinline__ float lo_f(uint32_t w) { return __uint_as_float(w << 16); }
__device__ __forceinline__ float hi_f(uint32_t w) { return __uint_as_float(w & 0xffff0000u); }
__device__ __forceinline__ uint32_t pk_bf16(float lo, float hi) {
    f32x2 x = {lo, hi};
    bf16x2 y = __builtin_convertvector(x, bf16x2);
    return __builtin_bit_cast(uint32_t, y);
}
__device__ __forceinline__ uint32_t pk_f16(float lo, float hi) {
    f32x2 x = {lo, hi};
    f16x2 y = __builtin_convertvector(x, f16x2);
    return __builtin_bit_cast(uint32_t, y);
}
__device__ __forceinline__ void rbf2(float& a, float& b) {
    uint32_t p = pk_bf16(a, b);
    a = lo_f(p);
    b = hi_f(p);
}
__device__ __forceinline__ float rbf(float a) { return lo_f(pk_bf16(a, a)); }

struct TapTab { uint16_t v[LSEQ]; };
static constexpr TapTab make_tab() {
    TapTab t{};
    for (int j = 0; j < LSEQ; ++j) {
        int p = j;
        if (j >= 256) {
            int s = (j < 512) ? 1 : ((j < 1024) ? 2 : 3);
            int q = j >> s, r = j & ((1 << s) - 1), half = 1 << (s - 1);
            if (r > half || (r == half && (q & 1))) q++;
            p = q << s;
        }
        int col = (p >> 3) & 15, a = p >> 7, low = p & 7, row = 0;
        if (p < 256) row = 8 * a + low;
        else if (p < 512) row = 16 + 4 * (a - 2) + (low >> 1);
        else if (p < 1024) row = 24 + 2 * (a - 4) + (low >> 2);
        else row = 32 + (a - 8);
        t.v[j] = (uint16_t)(row * 16 + col);
    }
    return t;
}
__device__ const TapTab g_taptab = make_tab();

__device__ __forceinline__ uint32_t prefetch_code(uint64_t pc, int bytes) {
    const int off = (int)(threadIdx.x & 255) * 128;
    typedef __attribute__((address_space(1))) const uint32_t* gp_t;
    const uint32_t v = *(gp_t)((pc & ~63ull) + (off < bytes ? off : 0));
    __builtin_amdgcn_sched_barrier(0);
    return v;
}
__device__ __forceinline__ void WST(u32x4* p, u32x4 v) { asm volatile("global_store_dwordx4 %0, %1, off sc0 sc1" :: "v"(p), "v"(v) : "memory"); }
#define KEEP(v) asm volatile("" :: "v"(v))
__device__ __forceinline__ f32x2 cmul(f32x2 a, f32x2 b) {
    f32x2 t, c;
    asm("v_pk_mul_f32 %0, %1, %2 op_sel:[0,0] op_sel_hi:[0,1]" : "=v"(t) : "v"(a), "v"(b));
    asm("v_pk_fma_f32 %0, %1, %2, %3 op_sel:[1,1,0] op_sel_hi:[1,0,1] neg_lo:[1,0,0]" : "=v"(c) : "v"(a), "v"(b), "v"(t));
    return c;
}
__device__ __forceinline__ f32x2 anchor(float a, float b) {
    const float e = __builtin_amdgcn_exp2f(a);
    const float th = __builtin_amdgcn_fractf(b);
    return (f32x2){e * __builtin_amdgcn_cosf(th), e * __builtin_amdgcn_sinf(th)};
}
__device__ __forceinline__ uint32_t pk16(f32x2 c) { return pk_f16(c[0], c[1]); }

#define KG_ROWB 272
#define KG_BUF (16 * KG_ROWB)

#define KA_OFF 0
#define KB_OFF 4640
#define XA_OFF 9216
#define XB_OFF 13440
#define CH_STRIDE 17600
#define KD_OFF (8 * CH_STRIDE)
#define FLAG_OFF (KD_OFF + 8 * 1536)
#define LDS_TOTAL (FLAG_OFF + 192)

__device__ __forceinline__ void put_k(char* cb, int y, u32x4 d, uint32_t prev) {
    *(u32x4*)(cb + KA_OFF + 464 + 16 * y) = d;
    u32x4 o;
    o[0] = __builtin_amdgcn_alignbyte(d[0], prev, 2);
    o[1] = __builtin_amdgcn_alignbyte(d[1], d[0], 2);
    o[2] = __builtin_amdgcn_alignbyte(d[2], d[1], 2);
    o[3] = __builtin_amdgcn_alignbyte(d[3], d[2], 2);
    *(u32x4*)(cb + KB_OFF + 464 + 16 * y) = o;
}
__device__ __forceinline__ void put_x4(char* p, uint32_t o0, uint32_t o1) {
    *(u32x2*)(p + XA_OFF) = (u32x2){o0, o1};
    *(uint32_t*)(p + XB_OFF - 4) = o0;
    *(uint32_t*)(p + XB_OFF) = o1;
}
__device__ __forceinline__ uint32_t prev_lane(uint32_t v, uint32_t carry) {
    return (uint32_t)__builtin_amdgcn_update_dpp((int)carry, (int)v, 0x138  , 0xf, 0xf, false);
}
typedef __attribute__((address_space(3))) char* lds_t;
typedef __attribute__((address_space(3))) const volatile u32x2* lds_v2;
typedef __attribute__((address_space(3))) volatile uint32_t* lds_vu;
typedef __attribute__((address_space(3))) const bf16x8* lds_k8;
__device__ __forceinline__ bf16x8 ldx(lds_t p) {
    u32x2 a = *(lds_v2)p, b = *(lds_v2)(p + 8);
    u32x4 v = {a[0], a[1], b[0], b[1]};
    return __builtin_bit_cast(bf16x8, v);
}
__device__ __forceinline__ bf16x8 ldk(lds_t p) { return *(lds_k8)p; }
#define MFMA(a, b, c) c = __builtin_amdgcn_mfma_f32_16x16x32_bf16(a, b, c, 0, 0, 0)
#define SB() __builtin_amdgcn_sched_barrier(0)
#define SG_DS(n) __builtin_amdgcn_sched_group_barrier(0x100, n, 0)
#define SG_MF(n) __builtin_amdgcn_sched_group_barrier(0x008, n, 0)

__device__ __forceinline__ void epi_acc(const f32x4 a, const char* xa, char* dst, float Dv) {
#pragma unroll
    for (int hh = 0; hh < 2; ++hh) {
        const uint32_t xd = *(const uint32_t*)(xa - 64 * hh);
        float y1 = a[2 * hh + 1], y0 = a[2 * hh];
        rbf2(y1, y0);
        float t1 = lo_f(xd) * Dv, t0 = hi_f(xd) * Dv;
        rbf2(t1, t0);
        *(uint32_t*)(dst - 64 * hh) = pk_bf16(y1 + t1, y0 + t0);
    }
}
__device__ __forceinline__ void epi_row(const char* a0, u32x4 qq4, uint16_t* dst) {
    s16x4 v0 = __builtin_amdgcn_ds_read_tr16_b64_v4i16((__attribute__((address_space(3))) s16x4*)(a0));
    s16x4 v1 = __builtin_amdgcn_ds_read_tr16_b64_v4i16((__attribute__((address_space(3))) s16x4*)(a0 + 4 * CH_STRIDE));
    u32x2 p0 = __builtin_bit_cast(u32x2, v0), p1 = __builtin_bit_cast(u32x2, v1);
    u32x4 o = {p0[0], p0[1], p1[0], p1[1]};
#pragma unroll
    for (int j = 0; j < 4; ++j)
        o[j] = pk_bf16(lo_f(qq4[j]) * lo_f(o[j]), hi_f(qq4[j]) * hi_f(o[j]));
    *(u32x4*)dst = o;
}
__device__ __forceinline__ void lds_barrier() {
    __builtin_amdgcn_fence(__ATOMIC_RELEASE, "workgroup", "local");
    __builtin_amdgcn_s_barrier();
    __builtin_amdgcn_fence(__ATOMIC_ACQUIRE, "workgroup", "local");
}
__device__ __forceinline__ void flag_set(lds_t p) {
    __builtin_amdgcn_fence(__ATOMIC_RELEASE, "workgroup", "local");
    *(lds_vu)p = 1u;
}
__device__ __forceinline__ void flag_wait(lds_t p) {
    while (__builtin_amdgcn_ballot_w64(*(lds_vu)p == 0u) != 0ull) __builtin_amdgcn_s_sleep(1);
    __builtin_amdgcn_fence(__ATOMIC_ACQUIRE, "workgroup", "local");
}

template <int T0>
__device__ __forceinline__ void stage(lds_t kaddr, lds_t xaddr, f32x4 (&acc)[8]) {
    constexpr int NK = 8 - T0;
    bf16x8 ka[NK], xa[2], kb[NK], xb[2];
#pragma unroll
    for (int m = 0; m < NK; ++m) ka[m] = ldk(kaddr + 512 * m);
#pragma unroll
    for (int t = 0; t < 2; ++t) xa[t] = ldx(xaddr + 512 * (7 - T0 - t));
#pragma unroll 1
    for (int c0 = 0; c0 < 8; c0 += 2) {
        const lds_t kp = kaddr + 64 * c0, xp = xaddr + 64 * c0;
#pragma unroll
        for (int t = 0; t < 2; ++t) xb[t] = ldx(xp + 64 + 512 * (7 - T0 - t));
#pragma unroll
        for (int m = 0; m < NK; ++m) kb[m] = ldk(kp + 64 + 512 * m);
#pragma unroll
        for (int m = 0; m < NK; ++m) MFMA(ka[m], xa[0], acc[T0 + m]);
#pragma unroll
        for (int m = 0; m < NK - 1; ++m) MFMA(ka[m], xa[1], acc[T0 + 1 + m]);
#pragma unroll
        for (int i = 0; i < NK + 4; ++i) { SG_MF(1); SG_DS(1); }
        SG_MF(NK);
        SB();
#pragma unroll
        for (int t = 0; t < 2; ++t) xa[t] = ldx(xp + 128 + 512 * (7 - T0 - t));
#pragma unroll
        for (int m = 0; m < NK; ++m) ka[m] = ldk(kp + 128 + 512 * m);
#pragma unroll
        for (int m = 0; m < NK; ++m) MFMA(kb[m], xb[0], acc[T0 + m]);
#pragma unroll
        for (int m = 0; m < NK - 1; ++m) MFMA(kb[m], xb[1], acc[T0 + 1 + m]);
#pragma unroll
        for (int i = 0; i < NK + 4; ++i) { SG_MF(1); SG_DS(1); }
        SG_MF(NK);
        SB();
    }
}

__global__ __launch_bounds__(768) void fused_kernel(
    const uint16_t* __restrict__ u, const uint16_t* __restrict__ logA, const uint16_t* __restrict__ Aim,
    const uint16_t* __restrict__ Cre, const uint16_t* __restrict__ Cim, const uint16_t* __restrict__ logdt,
    const uint16_t* __restrict__ Dsk, const uint16_t* __restrict__ qin, const uint16_t* __restrict__ kin,
    const uint16_t* __restrict__ vin, const uint16_t* __restrict__ shk, const uint16_t* __restrict__ ssmk,
    const uint16_t* __restrict__ sD, uint16_t* __restrict__ out)
{
    __shared__ __attribute__((aligned(16))) char lds[LDS_TOTAL];
    const int b = blockIdx.x;
    const int xcd = b & 7, idx = b >> 3, part = idx >> 4, gi = idx & 15, grp = xcd * 16 + gi;
    const int lane = threadIdx.x & 63;
    const int w = __builtin_amdgcn_readfirstlane(threadIdx.x >> 6);
    const lds_t flags = (lds_t)lds + FLAG_OFF;
    const int g = lane >> 4, fr = lane & 15;
#define SG0 (KA_OFF + 3536)
#define SG1 (KB_OFF + 3536)
#define SG2 (KA_OFF + 2512)
#define SG3 (KB_OFF + 2512)

    uint64_t pc0;
    asm volatile("s_getpc_b64 %0" : "=s"(pc0));
    RT(0, 0);
    STAMP(0, 0);
    STAMP(1, 0);
    if (w >= 8) {
        const int pw = w - 8, cq = lane & 1, rq = lane >> 1;
        const int s0 = 128 * pw + 4 * rq;
        const size_t eo = (size_t)s0 * DCH + grp * 8 + 4 * cq;
        if (threadIdx.x - 512 < 48) *(uint32_t*)(lds + FLAG_OFF + 4 * (threadIdx.x - 512)) = 0u;
        const uint32_t pf = prefetch_code(pc0, 24576);
        lds_barrier();
        char* xp = lds + 4 * cq * CH_STRIDE + 2 * (2044 - s0);
        const int qq = (lane >> 2) & 3, pp = lane & 3;
        const char* rg = lds + qq * CH_STRIDE + (128 * pw + 16 * g + 4 * pp) * 2;
        const int r0 = 511 - (128 * pw + lane);
        uint16_t* ob = out + part * DCH + grp * 8 + (size_t)r0 * (2 * DCH);
#define EPI_ROW(S, OFF, Q0, Q1) do { \
        flag_wait(flags + 64 + 32 * (S) + 4 * (lane & 7)); \
        epi_row(rg + (OFF), Q0, ob + (size_t)(512 * (S)) * (2 * DCH)); \
        epi_row(rg + (OFF) + 128, Q1, ob + (ptrdiff_t)(512 * (S) - 64) * (2 * DCH)); } while (0)
        if (part) {
            u32x2 kk[4][4], vv[4][4], hk[4][3];
            const int hs = 128 * pw + 32 * g - 3;
            const ptrdiff_t ho = (ptrdiff_t)hs * DCH + grp * 8 + 4 * cq;
#define LOADP(P) do { \
            _Pragma("unroll") for (int i = 0; i < 4; ++i) kk[P][i] = *(const u32x2*)(kin + eo + (size_t)(512 * (P) + i) * DCH); \
            _Pragma("unroll") for (int i = 0; i < 3; ++i) { \
                const bool z_ = (P) == 0 && hs < 0; \
                hk[P][i] = *(const u32x2*)(kin + (z_ ? (ptrdiff_t)0 : ho + (ptrdiff_t)(512 * (P) + i) * DCH)); \
                if (z_) hk[P][i] = (u32x2){0, 0}; } \
            _Pragma("unroll") for (int i = 0; i < 4; ++i) vv[P][i] = *(const u32x2*)(vin + eo + (size_t)(512 * (P) + i) * DCH); \
            SB(); } while (0)
            u32x4 wv[2];
#pragma unroll
            for (int i = 0; i < 2; ++i) wv[i] = *(const u32x4*)(shk + (size_t)(grp * 8 + 4 * cq) * 4 + 8 * i);
            LOADP(0);
            LOADP(1);
            LOADP(2);
            LOADP(3);
            u32x4 qv[8];
#define PASS(P) do { \
            u32x2 kr[7]; \
            _Pragma("unroll") for (int i = 0; i < 3; ++i) \
            _Pragma("unroll") for (int j = 0; j < 2; ++j) \
                kr[i][j] = (uint32_t)__builtin_amdgcn_update_dpp((int)hk[P][i][j], (int)kk[P][1 + i][j], 0x112  , 0xf, 0xf, false); \
            _Pragma("unroll") for (int i = 0; i < 4; ++i) kr[3 + i] = kk[P][i]; \
            _Pragma("unroll") for (int ch = 0; ch < 4; ++ch) { \
                const uint32_t wlo = wv[ch >> 1][(ch & 1) * 2], whi = wv[ch >> 1][(ch & 1) * 2 + 1]; \
                const float w0 = lo_f(wlo), w1 = hi_f(wlo), w2 = lo_f(whi), w3 = hi_f(whi); \
                float kf[7], vf[4]; \
                _Pragma("unroll") for (int i = 0; i < 7; ++i) kf[i] = (ch & 1) ? hi_f(kr[i][ch >> 1]) : lo_f(kr[i][ch >> 1]); \
                _Pragma("unroll") for (int i = 0; i < 4; ++i) vf[i] = (ch & 1) ? hi_f(vv[P][i][ch >> 1]) : lo_f(vv[P][i][ch >> 1]); \
                uint32_t o[2]; \
                _Pragma("unroll") for (int q = 0; q < 2; ++q) { \
                    const int ia = 3 - 2 * q, ib = 2 - 2 * q; \
                    float a0 = kf[ia] * w3, b0 = kf[ib] * w3; \
                    rbf2(a0, b0); \
                    float a1 = kf[ia + 1] * w2, b1 = kf[ib + 1] * w2; \
                    rbf2(a1, b1); \
                    a0 += a1; b0 += b1; \
                    rbf2(a0, b0); \
                    a1 = kf[ia + 2] * w1; b1 = kf[ib + 2] * w1; \
                    rbf2(a1, b1); \
                    a0 += a1; b0 += b1; \
                    rbf2(a0, b0); \
                    a1 = kf[ia + 3] * w0; b1 = kf[ib + 3] * w0; \
                    rbf2(a1, b1); \
                    a0 += a1; b0 += b1; \
                    rbf2(a0, b0); \
                    o[q] = pk_bf16(a0 * vf[ia], b0 * vf[ib]); \
                } \
                put_x4(xp + ch * CH_STRIDE - 1024 * (P), o[0], o[1]); \
            } \
            flag_set(flags + 16 * (P) + 4 * pw); \
            SB(); } while (0)
            STAMP(0, 1);
            PASS(0);
            STAMP(0, 2);
            PASS(1);
            STAMP(0, 3);
#pragma unroll
            for (int i = 0; i < 8; ++i) qv[i] = *(const u32x4*)(qin + (size_t)(r0 - 64 * (i & 1) + 512 * (i >> 1)) * DCH + grp * 8);
            SB();
            PASS(2);
            STAMP(0, 4);
            PASS(3);
            STAMP(0, 5);
            EPI_ROW(0, SG0, qv[0], qv[1]);
            STAMP(0, 6);
            EPI_ROW(1, SG1, qv[2], qv[3]);
            STAMP(0, 7);
            EPI_ROW(2, SG2, qv[4], qv[5]);
            STAMP(1, 1);
            EPI_ROW(3, SG3, qv[6], qv[7]);
        } else {
            const int t = threadIdx.x - 512;
            const size_t po = (size_t)(64 * gi + (t >> 2)) * DCH + 128 * xcd + 64 * (t & 1);
            const uint16_t* p1 = ((t & 2) ? vin : kin) + po;
            const uint16_t* p2 = ((t & 2) ? qin + (size_t)1024 * DCH : qin) + po;
            uint32_t pfv[3];
            u32x2 uu[4][4];
#pragma unroll
            for (int i = 0; i < 8; ++i) uu[i >> 2][i & 3] = *(const u32x2*)(u + eo + (size_t)(512 * (i >> 2) + (i & 3)) * DCH);
            SB();
            pfv[0] = *(const uint32_t*)(p1 + (size_t)1024 * DCH);
            SB();
#pragma unroll
            for (int i = 8; i < 16; ++i) uu[i >> 2][i & 3] = *(const u32x2*)(u + eo + (size_t)(512 * (i >> 2) + (i & 3)) * DCH);
            SB();
            pfv[1] = *(const uint32_t*)p2;
            SB();
            STAMP(0, 1);
#pragma unroll
            for (int p = 0; p < 4; ++p) {
#pragma unroll
                for (int ch = 0; ch < 4; ++ch) {
                    const uint32_t sel = (ch & 1) ? 0x07060302u : 0x05040100u;
                    put_x4(xp + ch * CH_STRIDE - 1024 * p, __builtin_amdgcn_perm(uu[p][2][ch >> 1], uu[p][3][ch >> 1], sel),
                           __builtin_amdgcn_perm(uu[p][0][ch >> 1], uu[p][1][ch >> 1], sel));
                }
                flag_set(flags + 16 * p + 4 * pw);
                SB();
                STAMP(0, 2 + p);
            }
            const u32x4 one = {0x3f803f80u, 0x3f803f80u, 0x3f803f80u, 0x3f803f80u};
            EPI_ROW(0, SG0, one, one);
            STAMP(0, 6);
            EPI_ROW(1, SG1, one, one);
            STAMP(0, 7);
            EPI_ROW(2, SG2, one, one);
            STAMP(1, 1);
            EPI_ROW(3, SG3, one, one);
            KEEP(pfv[0]);
            KEEP(pfv[1]);
        }
        STAMP(1, 6);
        STAMPW(1, 7);
        KEEP(pf);
        RT(0, 1);
        return;
    }

    const int chl = grp * 8 + w;
    char* cb = lds + w * CH_STRIDE;
    u32x4 kq[4];
    uint32_t pr[8];
    u32x4 tabv[4];
    float dt = 0.f;
    if (part) {
        const uint16_t* ksrc = ssmk + (size_t)chl * LSEQ;
#pragma unroll
        for (int i = 0; i < 4; ++i) kq[i] = *(const u32x4*)(ksrc + 8 * (64 * i + lane));
    } else {
        dt = bf2f(logdt[chl]);
#pragma unroll
        for (int bt = 0; bt < 2; ++bt) {
            const int n = chl * NMODE + bt * 64 + lane;
            pr[4 * bt] = logA[n]; pr[4 * bt + 1] = Aim[n]; pr[4 * bt + 2] = Cre[n]; pr[4 * bt + 3] = Cim[n];
        }
#pragma unroll
        for (int i = 0; i < 4; ++i) tabv[i] = *(const u32x4*)(g_taptab.v + 8 * (64 * i + lane));
    }
    const float Dv = bf2f((part ? sD : Dsk)[chl]);
    SB();
    {
        const u32x4 z = {0, 0, 0, 0};
        if (lane < 4) *(u32x4*)(cb + XA_OFF + 4096 + 16 * lane) = z;
        if (lane < 5) *(u32x4*)(cb + XB_OFF + 4080 + 16 * lane) = z;
    }
    lds_barrier();

    if (!part) {
        if (w < 4) __builtin_amdgcn_s_setprio(2);
        char* T = cb;
        dt = rbf(expf(dt));
        float pa[2], pb[2], pcr[2], pci[2];
        float mx = 0.f;
#pragma unroll
        for (int bt = 0; bt < 2; ++bt) {
            const float ar = -rbf(expf(lo_f(pr[4 * bt])));
            pa[bt] = (ar * dt) * 1.4426950408889634f;
            pb[bt] = (lo_f(pr[4 * bt + 1]) * dt) * 0.15915494309189535f;
            pcr[bt] = 2.f * (lo_f(pr[4 * bt + 2]) * dt);
            pci[bt] = 2.f * (lo_f(pr[4 * bt + 3]) * dt);
            mx = fmaxf(mx, fmaxf(fabsf(pcr[bt]), fabsf(pci[bt])));
        }
#pragma unroll
        for (int o = 32; o >= 1; o >>= 1) mx = fmaxf(mx, __shfl_xor(mx, o));
        int sf = 266 - (int)((__float_as_uint(mx) >> 23) & 0xff);
        sf = sf < 1 ? 1 : (sf > 239 ? 239 : sf);
        const float scl = __uint_as_float((uint32_t)sf << 23);
        const float inv = __uint_as_float((uint32_t)(240 - sf) << 23);
        STAMP(0, 1);

        f32x4 ac[3] = {{0, 0, 0, 0}, {0, 0, 0, 0}, {0, 0, 0, 0}};
#pragma unroll 1
        for (int bt = 0; bt < 2; ++bt) {
            const float a = bt ? pa[1] : pa[0], bb = bt ? pb[1] : pb[0];
            const f32x2 z1 = anchor(a, bb), z8c = anchor(a * 8.f, bb * -8.f), z128 = anchor(a * 128.f, bb * 128.f);
            char* Tl = T + lane * 4;
            const char* Tr = T + fr * KG_ROWB + g * 16;
            const f32x2 z16c = cmul(z8c, z8c), z256 = cmul(z128, z128), h2 = cmul(z1, z1);
            const f32x2 z32c = cmul(z16c, z16c), z512 = cmul(z256, z256), h4 = cmul(h2, h2);
            const f32x2 z64c = cmul(z32c, z32c), z1024 = cmul(z512, z512), h3 = cmul(h2, z1);
            f32x2 f[16], cg[17], h[8];
            f[0] = (f32x2){16384.f, 0.f};
            f[1] = z8c * 16384.f;
            f[2] = z16c * 16384.f;
            f[4] = z32c * 16384.f;
            f[8] = z64c * 16384.f;
            cg[0] = (f32x2){(bt ? pcr[1] : pcr[0]) * scl, (bt ? pci[1] : pci[0]) * scl};
            cg[1] = cmul(cg[0], z128);
            cg[2] = cmul(cg[0], z256);
            cg[4] = cmul(cg[0], z512);
            cg[8] = cmul(cg[0], z1024);
            h[1] = z1; h[2] = h2; h[3] = h3; h[4] = h4;
            h[5] = cmul(h4, z1); h[6] = cmul(h4, h2); h[7] = cmul(h4, h3);
            f[3] = cmul(f[2], z8c);
            cg[3] = cmul(cg[2], z128);
            cg[16] = cmul(cg[8], z1024);
#pragma unroll
            for (int i = 1; i < 4; ++i) { f[4 + i] = cmul(f[i], z32c); cg[4 + i] = cmul(cg[i], z512); }
#pragma unroll
            for (int i = 1; i < 8; ++i) { f[8 + i] = cmul(f[i], z64c); cg[8 + i] = cmul(cg[i], z1024); }
            f16x8 bfr[4], afr[3][4];
#pragma unroll
            for (int i = 0; i < 16; ++i) *(uint32_t*)(Tl + i * KG_ROWB) = pk16(f[i]);
#pragma unroll
            for (int aa = 0; aa < 2; ++aa) {
                *(uint32_t*)(Tl + KG_BUF + (8 * aa) * KG_ROWB) = pk16(cg[aa]);
#pragma unroll
                for (int e = 1; e < 8; ++e) *(uint32_t*)(Tl + KG_BUF + (8 * aa + e) * KG_ROWB) = pk16(cmul(cg[aa], h[e]));
            }
#pragma unroll
            for (int ks = 0; ks < 4; ++ks) {
                bfr[ks] = *(const f16x8*)(Tr + ks * 64);
                afr[0][ks] = *(const f16x8*)(Tr + KG_BUF + ks * 64);
            }
#pragma unroll
            for (int aa = 2; aa < 4; ++aa) {
                *(uint32_t*)(Tl + (4 * (aa - 2)) * KG_ROWB) = pk16(cg[aa]);
#pragma unroll
                for (int e = 1; e < 4; ++e) *(uint32_t*)(Tl + (4 * (aa - 2) + e) * KG_ROWB) = pk16(cmul(cg[aa], h[2 * e]));
            }
#pragma unroll
            for (int aa = 4; aa < 8; ++aa) {
                *(uint32_t*)(Tl + (8 + 2 * (aa - 4)) * KG_ROWB) = pk16(cg[aa]);
                *(uint32_t*)(Tl + (8 + 2 * (aa - 4) + 1) * KG_ROWB) = pk16(cmul(cg[aa], h[4]));
            }
#pragma unroll
            for (int aa = 8; aa <= 16; ++aa) *(uint32_t*)(Tl + KG_BUF + (aa - 8) * KG_ROWB) = pk16(cg[aa]);
#pragma unroll
            for (int ks = 0; ks < 4; ++ks) {
                afr[1][ks] = *(const f16x8*)(Tr + ks * 64);
                afr[2][ks] = *(const f16x8*)(Tr + KG_BUF + ks * 64);
            }
#pragma unroll
            for (int ks = 0; ks < 4; ++ks)
#pragma unroll
                for (int rt = 0; rt < 3; ++rt)
                    ac[rt] = __builtin_amdgcn_mfma_f32_16x16x32_f16(afr[rt][ks], bfr[ks], ac[rt], 0, 0, 0);
        }
        STAMP(0, 2);
        uint16_t* KD = (uint16_t*)(lds + KD_OFF + w * 1536);
#pragma unroll
        for (int rt = 0; rt < 3; ++rt)
#pragma unroll
            for (int r = 0; r < 4; r += 2) {
                uint32_t p = pk_bf16(ac[rt][r] * inv, ac[rt][r + 1] * inv);
                KD[(16 * rt + 4 * g + r) * 16 + fr] = (uint16_t)p;
                KD[(16 * rt + 4 * g + r + 1) * 16 + fr] = (uint16_t)(p >> 16);
            }
#pragma unroll
        for (int i = 0; i < 4; ++i)
#pragma unroll
            for (int q = 0; q < 4; ++q) {
                const uint32_t lo = KD[tabv[i][q] & 0xffffu], hi = KD[tabv[i][q] >> 16];
                kq[i][q] = lo | (hi << 16);
            }
    }
    {
        const u32x4 z = {0, 0, 0, 0};
        if (lane < 29) {
            *(u32x4*)(cb + KA_OFF + 16 * lane) = z;
            *(u32x4*)(cb + KB_OFF + 16 * lane) = z;
        }
        uint32_t c = 0;
#pragma unroll
        for (int i = 0; i < 4; ++i) {
            put_k(cb, 64 * i + lane, kq[i], prev_lane(kq[i][3], c));
            c = __builtin_amdgcn_readlane(kq[i][3], 63);
        }
    }
    __builtin_amdgcn_s_setprio(0);
    STAMP(0, 3);

    const lds_t lb = (lds_t)lds + w * CH_STRIDE;
    const lds_t kaddr = lb + ((fr & 1) ? KA_OFF : KB_OFF) + 2 * (32 * (fr >> 1) + 8 * g + 8);
    lds_t xaddr = lb + ((fr & 1) ? (XA_OFF + 2 * (1822 - 2 * fr + 8 * g)) : (XB_OFF + 2 * (1820 - 2 * fr + 8 * g))) - 3584;
    asm volatile("" : "+v"(xaddr));
    const int lo_ = 64 * g + 2 * fr;
    const char* xa = cb + XA_OFF + 2 * (2046 - lo_);
    char* sg = cb + 2 * (510 - lo_);
#define EPI_ACC(S, OFF) do { \
        epi_acc(acc[2 * (S)], xa - 512 * (2 * (S)), sg + (OFF), Dv); \
        epi_acc(acc[2 * (S) + 1], xa - 512 * (2 * (S) + 1), sg + (OFF) - 512, Dv); \
        flag_set(flags + 64 + 32 * (S) + 4 * w); } while (0)

    f32x4 acc[8];
#pragma unroll
    for (int i = 0; i < 8; ++i) acc[i] = (f32x4){0, 0, 0, 0};

    flag_wait(flags + 4 * (lane & 3));
    STAMP(0, 4);
    stage<0>(kaddr, xaddr, acc);
    STAMP(0, 5);
    EPI_ACC(0, SG0);
    STAMP(0, 6);
    flag_wait(flags + 16 + 4 * (lane & 3));
    STAMP(0, 7);
    stage<2>(kaddr, xaddr, acc);
    EPI_ACC(1, SG1);
    STAMP(1, 1);
    flag_wait(flags + 32 + 4 * (lane & 3));
    STAMP(1, 2);
    stage<4>(kaddr, xaddr, acc);
    EPI_ACC(2, SG2);
    STAMP(1, 3);
    flag_wait(flags + 48 + 4 * (lane & 3));
    STAMP(1, 4);
    {
        f32x4 a7b = {0, 0, 0, 0};
#pragma unroll
        for (int c = 0; c < 8; ++c) {
            const bf16x8 k0 = ldk(kaddr + 64 * c), k1 = ldk(kaddr + 64 * c + 512);
            const bf16x8 x6 = ldx(xaddr + 64 * c + 512), x7 = ldx(xaddr + 64 * c);
            MFMA(k0, x6, acc[6]);
            MFMA(k1, x6, acc[7]);
            MFMA(k0, x7, a7b);
        }
        acc[7] += a7b;
    }
    STAMP(1, 5);
    EPI_ACC(3, SG3);
    STAMP(1, 6);
    RT(0, 1);
}

extern "C" void kernel_launch(void* const* d_in, const int* in_sizes, int n_in,
                              void* d_out, int out_size, void* d_ws, size_t ws_size,
                              hipStream_t stream) {
    fused_kernel<<<256, 768, 0, stream>>>(
        (const uint16_t*)d_in[0], (const uint16_t*)d_in[1], (const uint16_t*)d_in[2], (const uint16_t*)d_in[3],
        (const uint16_t*)d_in[4], (const uint16_t*)d_in[5], (const uint16_t*)d_in[6], (const uint16_t*)d_in[7],
        (const uint16_t*)d_in[8], (const uint16_t*)d_in[9], (const uint16_t*)d_in[10], (const uint16_t*)d_in[11],
        (const uint16_t*)d_in[12], (uint16_t*)d_out);
}
```

```cpp
#include <hip/hip_runtime.h>
#include <stdint.h>

typedef __attribute__((ext_vector_type(4))) unsigned int u32x4;
typedef __attribute__((ext_vector_type(2))) unsigned int u32x2;
typedef __attribute__((ext_vector_type(4))) float f32x4;
typedef __attribute__((ext_vector_type(2))) float f32x2;
typedef __attribute__((ext_vector_type(8))) _Float16 f16x8;
typedef __attribute__((ext_vector_type(2))) _Float16 f16x2;
typedef __attribute__((ext_vector_type(8))) __bf16 bf16x8;
typedef __attribute__((ext_vector_type(2))) __bf16 bf16x2;
typedef __attribute__((ext_vector_type(4))) short s16x4;

#define STAMP(k, i)
#define STAMPW(k, i)
#define RT(k, i)
#define LSEQ 2048
#define DCH 1024
#define NMODE 128

__device__ __forceinline__ float bf2f(uint16_t v) { return __uint_as_float(((uint32_t)v) << 16); }
__device__ __forceinline__ float lo_f(uint32_t w) { return __uint_as_float(w << 16); }
__device__ __forceinline__ float hi_f(uint32_t w) { return __uint_as_float(w & 0xffff0000u); }
__device__ __forceinline__ uint32_t pk_bf16(float lo, float hi) {
    f32x2 x = {lo, hi};
    bf16x2 y = __builtin_convertvector(x, bf16x2);
    return __builtin_bit_cast(uint32_t, y);
}
__device__ __forceinline__ uint32_t pk_f16(float lo, float hi) {
    f32x2 x = {lo, hi};
    f16x2 y = __builtin_convertvector(x, f16x2);
    return __builtin_bit_cast(uint32_t, y);
}
__device__ __forceinline__ void rbf2(float& a, float& b) {
    uint32_t p = pk_bf16(a, b);
    a = lo_f(p);
    b = hi_f(p);
}
__device__ __forceinline__ float rbf(float a) { return lo_f(pk_bf16(a, a)); }

struct TapTab { uint16_t v[LSEQ]; };
static constexpr TapTab make_tab() {
    TapTab t{};
    for (int j = 0; j < LSEQ; ++j) {
        int p = j;
        if (j >= 256) {
            int s = (j < 512) ? 1 : ((j < 1024) ? 2 : 3);
            int q = j >> s, r = j & ((1 << s) - 1), half = 1 << (s - 1);
            if (r > half || (r == half && (q & 1))) q++;
            p = q << s;
        }
        int col = (p >> 3) & 15, a = p >> 7, low = p & 7, row = 0;
        if (p < 256) row = 8 * a + low;
        else if (p < 512) row = 16 + 4 * (a - 2) + (low >> 1);
        else if (p < 1024) row = 24 + 2 * (a - 4) + (low >> 2);
        else row = 32 + (a - 8);
        t.v[j] = (uint16_t)(row * 16 + col);
    }
    return t;
}
__device__ const TapTab g_taptab = make_tab();

__device__ __forceinline__ uint32_t prefetch_code(uint64_t pc, int bytes) {
    const int off = (int)(threadIdx.x & 255) * 128;
    typedef __attribute__((address_space(1))) const uint32_t* gp_t;
    const uint32_t v = *(gp_t)((pc & ~63ull) + (off < bytes ? off : 0));
    __builtin_amdgcn_sched_barrier(0);
    return v;
}
__device__ __forceinline__ void WST(u32x4* p, u32x4 v) { asm volatile("global_store_dwordx4 %0, %1, off sc0 sc1" :: "v"(p), "v"(v) : "memory"); }
#define KEEP(v) asm volatile("" :: "v"(v))
__device__ __forceinline__ f32x2 cmul(f32x2 a, f32x2 b) {
    f32x2 t, c;
    asm("v_pk_mul_f32 %0, %1, %2 op_sel:[0,0] op_sel_hi:[0,1]" : "=v"(t) : "v"(a), "v"(b));
    asm("v_pk_fma_f32 %0, %1, %2, %3 op_sel:[1,1,0] op_sel_hi:[1,0,1] neg_lo:[1,0,0]" : "=v"(c) : "v"(a), "v"(b), "v"(t));
    return c;
}
__device__ __forceinline__ f32x2 anchor(float a, float b) {
    const float e = __builtin_amdgcn_exp2f(a);
    const float th = __builtin_amdgcn_fractf(b);
    return (f32x2){e * __builtin_amdgcn_cosf(th), e * __builtin_amdgcn_sinf(th)};
}
__device__ __forceinline__ uint32_t pk16(f32x2 c) { return pk_f16(c[0], c[1]); }

#define KG_ROWB 272
#define KG_BUF (16 * KG_ROWB)

#define KA_OFF 0
#define KB_OFF 4640
#define XA_OFF 9216
#define XB_OFF 13440
#define CH_STRIDE 17600
#define KD_OFF (8 * CH_STRIDE)
#define FLAG_OFF (KD_OFF + 8 * 1536)
#define LDS_TOTAL (FLAG_OFF + 192)

__device__ __forceinline__ void put_k(char* cb, int y, u32x4 d, uint32_t prev) {
    *(u32x4*)(cb + KA_OFF + 464 + 16 * y) = d;
    u32x4 o;
    o[0] = __builtin_amdgcn_alignbyte(d[0], prev, 2);
    o[1] = __builtin_amdgcn_alignbyte(d[1], d[0], 2);
    o[2] = __builtin_amdgcn_alignbyte(d[2], d[1], 2);
    o[3] = __builtin_amdgcn_alignbyte(d[3], d[2], 2);
    *(u32x4*)(cb + KB_OFF + 464 + 16 * y) = o;
}
__device__ __forceinline__ void put_x4(char* p, uint32_t o0, uint32_t o1) {
    *(u32x2*)(p + XA_OFF) = (u32x2){o0, o1};
    *(uint32_t*)(p + XB_OFF - 4) = o0;
    *(uint32_t*)(p + XB_OFF) = o1;
}
__device__ __forceinline__ uint32_t prev_lane(uint32_t v, uint32_t carry) {
    return (uint32_t)__builtin_amdgcn_update_dpp((int)carry, (int)v, 0x138  , 0xf, 0xf, false);
}
typedef __attribute__((address_space(3))) char* lds_t;
typedef __attribute__((address_space(3))) const volatile u32x2* lds_v2;
typedef __attribute__((address_space(3))) volatile uint32_t* lds_vu;
typedef __attribute__((address_space(3))) const bf16x8* lds_k8;
__device__ __forceinline__ bf16x8 ldx(lds_t p) {
    u32x2 a = *(lds_v2)p, b = *(lds_v2)(p + 8);
    u32x4 v = {a[0], a[1], b[0], b[1]};
    return __builtin_bit_cast(bf16x8, v);
}
__device__ __forceinline__ bf16x8 ldk(lds_t p) { return *(lds_k8)p; }
#define MFMA(a, b, c) c = __builtin_amdgcn_mfma_f32_16x16x32_bf16(a, b, c, 0, 0, 0)
#define SB() __builtin_amdgcn_sched_barrier(0)
#define SG_DS(n) __builtin_amdgcn_sched_group_barrier(0x100, n, 0)
#define SG_MF(n) __builtin_amdgcn_sched_group_barrier(0x008, n, 0)

#define DOT(d, a, b) "v_dot2_f32_bf16 " d ", " a ", " b ", 0\n\t"
#define CVT(d, a, b) "v_cvt_pk_bf16_f32 " d ", " a ", " b "\n\t"
__device__ __forceinline__ void epi_acc(const f32x4 a, const char* xa, char* dst, uint32_t Dlo, uint32_t Dhi, uint32_t one) {
    const uint32_t x0 = *(const uint32_t*)xa, x1 = *(const uint32_t*)(xa - 64);
    uint32_t t0, t1, t2, t3;
    asm(DOT("%1", "%8", "%10") DOT("%0", "%8", "%11") DOT("%3", "%9", "%10") DOT("%2", "%9", "%11")
        CVT("%1", "%5", "%1") CVT("%0", "%4", "%0") CVT("%3", "%7", "%3") CVT("%2", "%6", "%2")
        DOT("%1", "%1", "%12") DOT("%0", "%0", "%12") DOT("%3", "%3", "%12") DOT("%2", "%2", "%12")
        "s_nop 1\n\t"
        CVT("%0", "%1", "%0") CVT("%2", "%3", "%2")
        : "=&v"(t0), "=&v"(t1), "=&v"(t2), "=&v"(t3)
        : "v"(a[0]), "v"(a[1]), "v"(a[2]), "v"(a[3]), "v"(x0), "v"(x1), "v"(Dlo), "v"(Dhi), "s"(one));
    *(uint32_t*)dst = t0;
    *(uint32_t*)(dst - 64) = t2;
}
__device__ __forceinline__ u32x4 row_read(const char* a0) {
    s16x4 v0 = __builtin_amdgcn_ds_read_tr16_b64_v4i16((__attribute__((address_space(3))) s16x4*)(a0));
    s16x4 v1 = __builtin_amdgcn_ds_read_tr16_b64_v4i16((__attribute__((address_space(3))) s16x4*)(a0 + 4 * CH_STRIDE));
    u32x2 p0 = __builtin_bit_cast(u32x2, v0), p1 = __builtin_bit_cast(u32x2, v1);
    return (u32x4){p0[0], p0[1], p1[0], p1[1]};
}
__device__ __forceinline__ u32x4 gate(u32x4 y, u32x4 q) {
    uint32_t l0, l1, l2, l3, h0, h1, h2, h3;
    asm(DOT("%0", "%8", "%12") DOT("%4", "%8", "%16") DOT("%1", "%9", "%13") DOT("%5", "%9", "%17")
        DOT("%2", "%10", "%14") DOT("%6", "%10", "%18") DOT("%3", "%11", "%15") DOT("%7", "%11", "%19")
        CVT("%0", "%0", "%4") CVT("%1", "%1", "%5") CVT("%2", "%2", "%6") CVT("%3", "%3", "%7")
        : "=&v"(l0), "=&v"(l1), "=&v"(l2), "=&v"(l3), "=&v"(h0), "=&v"(h1), "=&v"(h2), "=&v"(h3)
        : "v"(y[0]), "v"(y[1]), "v"(y[2]), "v"(y[3]),
          "v"(q[0] & 0xffffu), "v"(q[1] & 0xffffu), "v"(q[2] & 0xffffu), "v"(q[3] & 0xffffu),
          "v"(q[0] & 0xffff0000u), "v"(q[1] & 0xffff0000u), "v"(q[2] & 0xffff0000u), "v"(q[3] & 0xffff0000u));
    return (u32x4){l0, l1, l2, l3};
}
template <int HI>
__device__ __forceinline__ void kv4(uint32_t k0, uint32_t k1, uint32_t k2, uint32_t k3, uint32_t k4, uint32_t k5, uint32_t k6,
                                    uint32_t v0, uint32_t v1, uint32_t v2, uint32_t v3,
                                    uint32_t w0, uint32_t w1, uint32_t w2, uint32_t w3, uint32_t one,
                                    uint32_t& o0, uint32_t& o1) {
    uint32_t a0, a1, a2, a3, p0, p1, p2, p3;
#define CV4 CVT("%0", "%0", "%4") CVT("%1", "%1", "%5") CVT("%2", "%2", "%6") CVT("%3", "%3", "%7")
#define AD4 DOT("%0", "%0", "%23") DOT("%1", "%1", "%23") DOT("%2", "%2", "%23") DOT("%3", "%3", "%23")
#define KV_HEAD \
        DOT("%0", "%8", "%22") DOT("%1", "%9", "%22") DOT("%2", "%10", "%22") DOT("%3", "%11", "%22") \
        DOT("%4", "%9", "%21") DOT("%5", "%10", "%21") DOT("%6", "%11", "%21") DOT("%7", "%12", "%21") \
        CV4 AD4 \
        DOT("%4", "%10", "%20") DOT("%5", "%11", "%20") DOT("%6", "%12", "%20") DOT("%7", "%13", "%20") \
        CV4 AD4 \
        DOT("%4", "%11", "%19") DOT("%5", "%12", "%19") DOT("%6", "%13", "%19") DOT("%7", "%14", "%19") \
        CV4 AD4
#define KV_TAIL \
        DOT("%0", "%0", "%15") DOT("%1", "%1", "%16") DOT("%2", "%2", "%17") DOT("%3", "%3", "%18") \
        "s_nop 1\n\t" \
        CVT("%0", "%1", "%0") CVT("%2", "%3", "%2")
#define KV_OPS \
        : "=&v"(a0), "=&v"(a1), "=&v"(a2), "=&v"(a3), "=&v"(p0), "=&v"(p1), "=&v"(p2), "=&v"(p3) \
        : "v"(k0), "v"(k1), "v"(k2), "v"(k3), "v"(k4), "v"(k5), "v"(k6), "v"(v0), "v"(v1), "v"(v2), "v"(v3), \
          "v"(w0), "v"(w1), "v"(w2), "v"(w3), "s"(one)
    if (HI) asm(KV_HEAD CVT("%0", "0", "%0") CVT("%1", "0", "%1") CVT("%2", "0", "%2") CVT("%3", "0", "%3") KV_TAIL KV_OPS);
    else    asm(KV_HEAD CVT("%0", "%0", "0") CVT("%1", "%1", "0") CVT("%2", "%2", "0") CVT("%3", "%3", "0") KV_TAIL KV_OPS);
    o0 = a2;
    o1 = a0;
}
__device__ __forceinline__ void lds_barrier() {
    __builtin_amdgcn_fence(__ATOMIC_RELEASE, "workgroup", "local");
    __builtin_amdgcn_s_barrier();
    __builtin_amdgcn_fence(__ATOMIC_ACQUIRE, "workgroup", "local");
}
__device__ __forceinline__ void flag_set(lds_t p) {
    __builtin_amdgcn_fence(__ATOMIC_RELEASE, "workgroup", "local");
    *(lds_vu)p = 1u;
}
__device__ __forceinline__ void flag_wait(lds_t p) {
    while (__builtin_amdgcn_ballot_w64(*(lds_vu)p == 0u) != 0ull) __builtin_amdgcn_s_sleep(1);
    __builtin_amdgcn_fence(__ATOMIC_ACQUIRE, "workgroup", "local");
}

template <int T0>
__device__ __forceinline__ void stage(lds_t kaddr, lds_t xaddr, f32x4 (&acc)[8]) {
    constexpr int NK = 8 - T0;
    bf16x8 ka[NK], xa[2], kb[NK], xb[2];
#pragma unroll
    for (int m = 0; m < NK; ++m) ka[m] = ldk(kaddr + 512 * m);
#pragma unroll
    for (int t = 0; t < 2; ++t) xa[t] = ldx(xaddr + 512 * (7 - T0 - t));
#pragma unroll 1
    for (int c0 = 0; c0 < 8; c0 += 2) {
        const lds_t kp = kaddr + 64 * c0, xp = xaddr + 64 * c0;
#pragma unroll
        for (int t = 0; t < 2; ++t) xb[t] = ldx(xp + 64 + 512 * (7 - T0 - t));
#pragma unroll
        for (int m = 0; m < NK; ++m) kb[m] = ldk(kp + 64 + 512 * m);
#pragma unroll
        for (int m = 0; m < NK; ++m) MFMA(ka[m], xa[0], acc[T0 + m]);
#pragma unroll
        for (int m = 0; m < NK - 1; ++m) MFMA(ka[m], xa[1], acc[T0 + 1 + m]);
#pragma unroll
        for (int i = 0; i < NK + 4; ++i) { SG_MF(1); SG_DS(1); }
        SG_MF(NK);
        SB();
#pragma unroll
        for (int t = 0; t < 2; ++t) xa[t] = ldx(xp + 128 + 512 * (7 - T0 - t));
#pragma unroll
        for (int m = 0; m < NK; ++m) ka[m] = ldk(kp + 128 + 512 * m);
#pragma unroll
        for (int m = 0; m < NK; ++m) MFMA(kb[m], xb[0], acc[T0 + m]);
#pragma unroll
        for (int m = 0; m < NK - 1; ++m) MFMA(kb[m], xb[1], acc[T0 + 1 + m]);
#pragma unroll
        for (int i = 0; i < NK + 4; ++i) { SG_MF(1); SG_DS(1); }
        SG_MF(NK);
        SB();
    }
}

__global__ __launch_bounds__(768) void fused_kernel(
    const uint16_t* __restrict__ u, const uint16_t* __restrict__ logA, const uint16_t* __restrict__ Aim,
    const uint16_t* __restrict__ Cre, const uint16_t* __restrict__ Cim, const uint16_t* __restrict__ logdt,
    const uint16_t* __restrict__ Dsk, const uint16_t* __restrict__ qin, const uint16_t* __restrict__ kin,
    const uint16_t* __restrict__ vin, const uint16_t* __restrict__ shk, const uint16_t* __restrict__ ssmk,
    const uint16_t* __restrict__ sD, uint16_t* __restrict__ out)
{
    __shared__ __attribute__((aligned(16))) char lds[LDS_TOTAL];
    const int b = blockIdx.x;
    const int xcd = b & 7, idx = b >> 3, part = idx >> 4, gi = idx & 15, grp = xcd * 16 + gi;
    const int lane = threadIdx.x & 63;
    const int w = __builtin_amdgcn_readfirstlane(threadIdx.x >> 6);
    const lds_t flags = (lds_t)lds + FLAG_OFF;
    const int g = lane >> 4, fr = lane & 15;
    const uint32_t one = 0x3f803f80u;
#define SG0 (KA_OFF + 3536)
#define SG1 (KB_OFF + 3536)
#define SG2 (KA_OFF + 2512)
#define SG3 (KB_OFF + 2512)

    uint64_t pc0;
    asm volatile("s_getpc_b64 %0" : "=s"(pc0));
    RT(0, 0);
    STAMP(0, 0);
    STAMP(1, 0);
    if (w >= 8) {
        const int pw = w - 8, cq = lane & 1, rq = lane >> 1;
        const int s0 = 128 * pw + 4 * rq;
        const size_t eo = (size_t)s0 * DCH + grp * 8 + 4 * cq;
        if (threadIdx.x - 512 < 48) *(uint32_t*)(lds + FLAG_OFF + 4 * (threadIdx.x - 512)) = 0u;
        const uint32_t pf = prefetch_code(pc0, 24576);
        lds_barrier();
        char* xp = lds + 4 * cq * CH_STRIDE + 2 * (2044 - s0);
        const int qq = (lane >> 2) & 3, pp = lane & 3;
        const char* rg = lds + qq * CH_STRIDE + (128 * pw + 16 * g + 4 * pp) * 2;
        const int r0 = 511 - (128 * pw + lane);
        uint16_t* ob = out + part * DCH + grp * 8 + (size_t)r0 * (2 * DCH);
#define ROW_OUT(S) (ob + (ptrdiff_t)(512 * (S)) * (2 * DCH))
#define ROW_WAIT(S) flag_wait(flags + 64 + 32 * (S) + 4 * (lane & 7))
        if (part) {
            u32x2 kk[4][4], vv[4][4], hk[4][3];
            const int hs = 128 * pw + 32 * g - 3;
            const ptrdiff_t ho = (ptrdiff_t)hs * DCH + grp * 8 + 4 * cq;
#define LDK(P, i) kk[P][i] = *(const u32x2*)(kin + eo + (size_t)(512 * (P) + (i)) * DCH)
#define LDV(P, i) vv[P][i] = *(const u32x2*)(vin + eo + (size_t)(512 * (P) + (i)) * DCH)
#define LDH(P) do { _Pragma("unroll") for (int i = 0; i < 3; ++i) { \
                const bool z_ = (P) == 0 && hs < 0; \
                hk[P][i] = *(const u32x2*)(kin + (z_ ? (ptrdiff_t)0 : ho + (ptrdiff_t)(512 * (P) + i) * DCH)); \
                if (z_) hk[P][i] = (u32x2){0, 0}; } } while (0)
#define LDQ(i) qv[i] = *(const u32x4*)(qin + (size_t)(r0 - 64 * ((i) & 1) + 512 * ((i) >> 1)) * DCH + grp * 8)
            u32x4 wv[2], qv[8];
#pragma unroll
            for (int i = 0; i < 2; ++i) wv[i] = *(const u32x4*)(shk + (size_t)(grp * 8 + 4 * cq) * 4 + 8 * i);
#pragma unroll
            for (int p = 0; p < 2; ++p) {
                LDK(p, 0); LDK(p, 1); LDK(p, 2); LDK(p, 3); LDH(p); LDV(p, 0); LDV(p, 1); LDV(p, 2); LDV(p, 3);
                SB();
            }
            uint32_t wt[4][4];
#pragma unroll
            for (int ch = 0; ch < 4; ++ch)
#pragma unroll
                for (int j = 0; j < 4; ++j) {
                    const uint32_t d = wv[ch >> 1][(ch & 1) * 2 + (j >> 1)];
                    wt[ch][j] = (ch & 1) ? ((j & 1) ? (d & 0xffff0000u) : (d << 16)) : ((j & 1) ? (d >> 16) : (d & 0xffffu));
                }
#define KVCH(P, ch) do { uint32_t o0_, o1_; \
            kv4<(ch) & 1>(kr[0][(ch) >> 1], kr[1][(ch) >> 1], kr[2][(ch) >> 1], kk[P][0][(ch) >> 1], kk[P][1][(ch) >> 1], kk[P][2][(ch) >> 1], kk[P][3][(ch) >> 1], \
                          vv[P][0][(ch) >> 1], vv[P][1][(ch) >> 1], vv[P][2][(ch) >> 1], vv[P][3][(ch) >> 1], \
                          wt[ch][0], wt[ch][1], wt[ch][2], wt[ch][3], one, o0_, o1_); \
            put_x4(xp + (ch) * CH_STRIDE - 1024 * (P), o0_, o1_); SB(); } while (0)
#define PASS(P, L0, L1, L2, L3) do { \
            u32x2 kr[3]; \
            _Pragma("unroll") for (int i = 0; i < 3; ++i) \
            _Pragma("unroll") for (int j = 0; j < 2; ++j) \
                kr[i][j] = (uint32_t)__builtin_amdgcn_update_dpp((int)hk[P][i][j], (int)kk[P][1 + i][j], 0x112  , 0xf, 0xf, false); \
            KVCH(P, 0); L0; SB(); KVCH(P, 1); L1; SB(); KVCH(P, 2); L2; SB(); KVCH(P, 3); \
            flag_set(flags + 16 * (P) + 4 * pw); \
            L3; SB(); } while (0)
            STAMP(0, 1);
            PASS(0, { LDK(2, 0); LDK(2, 1); }, { LDK(2, 2); LDK(2, 3); LDH(2); }, { LDV(2, 0); LDV(2, 1); }, { LDV(2, 2); LDV(2, 3); });
            STAMP(0, 2);
            PASS(1, { LDK(3, 0); LDK(3, 1); }, { LDK(3, 2); LDK(3, 3); LDH(3); }, { LDV(3, 0); LDV(3, 1); }, { LDV(3, 2); LDV(3, 3); });
            STAMP(0, 3);
            PASS(2, LDQ(0), LDQ(1), LDQ(2), LDQ(3));
            STAMP(0, 4);
            PASS(3, LDQ(4), LDQ(5), LDQ(6), LDQ(7));
            STAMP(0, 5);
#define EPI_ROWB(S, OFF) do { \
            ROW_WAIT(S); \
            *(u32x4*)ROW_OUT(S) = gate(row_read(rg + (OFF)), qv[2 * (S)]); \
            *(u32x4*)(ROW_OUT(S) - 64 * (2 * DCH)) = gate(row_read(rg + (OFF) + 128), qv[2 * (S) + 1]); } while (0)
            EPI_ROWB(0, SG0);
            STAMP(0, 6);
            EPI_ROWB(1, SG1);
            STAMP(0, 7);
            EPI_ROWB(2, SG2);
            STAMP(1, 1);
            EPI_ROWB(3, SG3);
        } else {
            const int t = threadIdx.x - 512;
            const size_t po = (size_t)(64 * gi + (t >> 2)) * DCH + 128 * xcd + 64 * (t & 1);
            const uint16_t* p1 = ((t & 2) ? vin : kin) + po;
            const uint16_t* p2 = ((t & 2) ? qin + (size_t)1024 * DCH : qin) + po;
            uint32_t pfv[3];
            u32x2 uu[4][4];
#pragma unroll
            for (int i = 0; i < 8; ++i) uu[i >> 2][i & 3] = *(const u32x2*)(u + eo + (size_t)(512 * (i >> 2) + (i & 3)) * DCH);
            SB();
            pfv[0] = *(const uint32_t*)(p1 + (size_t)1024 * DCH);
            SB();
#pragma unroll
            for (int i = 8; i < 16; ++i) uu[i >> 2][i & 3] = *(const u32x2*)(u + eo + (size_t)(512 * (i >> 2) + (i & 3)) * DCH);
            SB();
            pfv[1] = *(const uint32_t*)p2;
            SB();
            STAMP(0, 1);
#pragma unroll
            for (int p = 0; p < 4; ++p) {
#pragma unroll
                for (int ch = 0; ch < 4; ++ch) {
                    const uint32_t sel = (ch & 1) ? 0x07060302u : 0x05040100u;
                    put_x4(xp + ch * CH_STRIDE - 1024 * p, __builtin_amdgcn_perm(uu[p][2][ch >> 1], uu[p][3][ch >> 1], sel),
                           __builtin_amdgcn_perm(uu[p][0][ch >> 1], uu[p][1][ch >> 1], sel));
                }
                flag_set(flags + 16 * p + 4 * pw);
                SB();
                STAMP(0, 2 + p);
            }
#define EPI_ROWA(S, OFF) do { \
            ROW_WAIT(S); \
            *(u32x4*)ROW_OUT(S) = row_read(rg + (OFF)); \
            *(u32x4*)(ROW_OUT(S) - 64 * (2 * DCH)) = row_read(rg + (OFF) + 128); } while (0)
            EPI_ROWA(0, SG0);
            STAMP(0, 6);
            EPI_ROWA(1, SG1);
            STAMP(0, 7);
            EPI_ROWA(2, SG2);
            STAMP(1, 1);
            EPI_ROWA(3, SG3);
            KEEP(pfv[0]);
            KEEP(pfv[1]);
        }
        STAMP(1, 6);
        STAMPW(1, 7);
        KEEP(pf);
        RT(0, 1);
        return;
    }

    const int chl = grp * 8 + w;
    char* cb = lds + w * CH_STRIDE;
    u32x4 kq[4];
    uint32_t pr[8];
    u32x4 tabv[4];
    float dt = 0.f;
    if (part) {
        const uint16_t* ksrc = ssmk + (size_t)chl * LSEQ;
#pragma unroll
        for (int i = 0; i < 4; ++i) kq[i] = *(const u32x4*)(ksrc + 8 * (64 * i + lane));
    } else {
        dt = bf2f(logdt[chl]);
#pragma unroll
        for (int bt = 0; bt < 2; ++bt) {
            const int n = chl * NMODE + bt * 64 + lane;
            pr[4 * bt] = logA[n]; pr[4 * bt + 1] = Aim[n]; pr[4 * bt + 2] = Cre[n]; pr[4 * bt + 3] = Cim[n];
        }
#pragma unroll
        for (int i = 0; i < 4; ++i) tabv[i] = *(const u32x4*)(g_taptab.v + 8 * (64 * i + lane));
    }
    const uint32_t Dlo = (part ? sD : Dsk)[chl], Dhi = Dlo << 16;
    SB();
    {
        const u32x4 z = {0, 0, 0, 0};
        if (lane < 4) *(u32x4*)(cb + XA_OFF + 4096 + 16 * lane) = z;
        if (lane < 5) *(u32x4*)(cb + XB_OFF + 4080 + 16 * lane) = z;
    }
    lds_barrier();

    if (!part) {
        if (w < 4) __builtin_amdgcn_s_setprio(2);
        char* T = cb;
        dt = rbf(expf(dt));
        float pa[2], pb[2], pcr[2], pci[2];
        float mx = 0.f;
#pragma unroll
        for (int bt = 0; bt < 2; ++bt) {
            const float ar = -rbf(expf(lo_f(pr[4 * bt])));
            pa[bt] = (ar * dt) * 1.4426950408889634f;
            pb[bt] = (lo_f(pr[4 * bt + 1]) * dt) * 0.15915494309189535f;
            pcr[bt] = 2.f * (lo_f(pr[4 * bt + 2]) * dt);
            pci[bt] = 2.f * (lo_f(pr[4 * bt + 3]) * dt);
            mx = fmaxf(mx, fmaxf(fabsf(pcr[bt]), fabsf(pci[bt])));
        }
#pragma unroll
        for (int o = 32; o >= 1; o >>= 1) mx = fmaxf(mx, __shfl_xor(mx, o));
        int sf = 266 - (int)((__float_as_uint(mx) >> 23) & 0xff);
        sf = sf < 1 ? 1 : (sf > 239 ? 239 : sf);
        const float scl = __uint_as_float((uint32_t)sf << 23);
        const float inv = __uint_as_float((uint32_t)(240 - sf) << 23);
        STAMP(0, 1);

        f32x4 ac[3] = {{0, 0, 0, 0}, {0, 0, 0, 0}, {0, 0, 0, 0}};
#pragma unroll 1
        for (int bt = 0; bt < 2; ++bt) {
            const float a = bt ? pa[1] : pa[0], bb = bt ? pb[1] : pb[0];
            const f32x2 z1 = anchor(a, bb), z8c = anchor(a * 8.f, bb * -8.f), z128 = anchor(a * 128.f, bb * 128.f);
            char* Tl = T + lane * 4;
            const char* Tr = T + fr * KG_ROWB + g * 16;
            const f32x2 z16c = cmul(z8c, z8c), z256 = cmul(z128, z128), h2 = cmul(z1, z1);
            const f32x2 z32c = cmul(z16c, z16c), z512 = cmul(z256, z256), h4 = cmul(h2, h2);
            const f32x2 z64c = cmul(z32c, z32c), z1024 = cmul(z512, z512), h3 = cmul(h2, z1);
            f32x2 f[16], cg[17], h[8];
            f[0] = (f32x2){16384.f, 0.f};
            f[1] = z8c * 16384.f;
            f[2] = z16c * 16384.f;
            f[4] = z32c * 16384.f;
            f[8] = z64c * 16384.f;
            cg[0] = (f32x2){(bt ? pcr[1] : pcr[0]) * scl, (bt ? pci[1] : pci[0]) * scl};
            cg[1] = cmul(cg[0], z128);
            cg[2] = cmul(cg[0], z256);
            cg[4] = cmul(cg[0], z512);
            cg[8] = cmul(cg[0], z1024);
            h[1] = z1; h[2] = h2; h[3] = h3; h[4] = h4;
            h[5] = cmul(h4, z1); h[6] = cmul(h4, h2); h[7] = cmul(h4, h3);
            f[3] = cmul(f[2], z8c);
            cg[3] = cmul(cg[2], z128);
            cg[16] = cmul(cg[8], z1024);
#pragma unroll
            for (int i = 1; i < 4; ++i) { f[4 + i] = cmul(f[i], z32c); cg[4 + i] = cmul(cg[i], z512); }
#pragma unroll
            for (int i = 1; i < 8; ++i) { f[8 + i] = cmul(f[i], z64c); cg[8 + i] = cmul(cg[i], z1024); }
            f16x8 bfr[4], afr[3][4];
#pragma unroll
            for (int i = 0; i < 16; ++i) *(uint32_t*)(Tl + i * KG_ROWB) = pk16(f[i]);
#pragma unroll
            for (int aa = 0; aa < 2; ++aa) {
                *(uint32_t*)(Tl + KG_BUF + (8 * aa) * KG_ROWB) = pk16(cg[aa]);
#pragma unroll
                for (int e = 1; e < 8; ++e) *(uint32_t*)(Tl + KG_BUF + (8 * aa + e) * KG_ROWB) = pk16(cmul(cg[aa], h[e]));
            }
#pragma unroll
            for (int ks = 0; ks < 4; ++ks) {
                bfr[ks] = *(const f16x8*)(Tr + ks * 64);
                afr[0][ks] = *(const f16x8*)(Tr + KG_BUF + ks * 64);
            }
#pragma unroll
            for (int aa = 2; aa < 4; ++aa) {
                *(uint32_t*)(Tl + (4 * (aa - 2)) * KG_ROWB) = pk16(cg[aa]);
#pragma unroll
                for (int e = 1; e < 4; ++e) *(uint32_t*)(Tl + (4 * (aa - 2) + e) * KG_ROWB) = pk16(cmul(cg[aa], h[2 * e]));
            }
#pragma unroll
            for (int aa = 4; aa < 8; ++aa) {
                *(uint32_t*)(Tl + (8 + 2 * (aa - 4)) * KG_ROWB) = pk16(cg[aa]);
                *(uint32_t*)(Tl + (8 + 2 * (aa - 4) + 1) * KG_ROWB) = pk16(cmul(cg[aa], h[4]));
            }
#pragma unroll
            for (int aa = 8; aa <= 16; ++aa) *(uint32_t*)(Tl + KG_BUF + (aa - 8) * KG_ROWB) = pk16(cg[aa]);
#pragma unroll
            for (int ks = 0; ks < 4; ++ks) {
                afr[1][ks] = *(const f16x8*)(Tr + ks * 64);
                afr[2][ks] = *(const f16x8*)(Tr + KG_BUF + ks * 64);
            }
#pragma unroll
            for (int ks = 0; ks < 4; ++ks)
#pragma unroll
                for (int rt = 0; rt < 3; ++rt)
                    ac[rt] = __builtin_amdgcn_mfma_f32_16x16x32_f16(afr[rt][ks], bfr[ks], ac[rt], 0, 0, 0);
        }
        STAMP(0, 2);
        uint16_t* KD = (uint16_t*)(lds + KD_OFF + w * 1536);
#pragma unroll
        for (int rt = 0; rt < 3; ++rt)
#pragma unroll
            for (int r = 0; r < 4; r += 2) {
                uint32_t p = pk_bf16(ac[rt][r] * inv, ac[rt][r + 1] * inv);
                KD[(16 * rt + 4 * g + r) * 16 + fr] = (uint16_t)p;
                KD[(16 * rt + 4 * g + r + 1) * 16 + fr] = (uint16_t)(p >> 16);
            }
#pragma unroll
        for (int i = 0; i < 4; ++i)
#pragma unroll
            for (int q = 0; q < 4; ++q) {
                const uint32_t lo = KD[tabv[i][q] & 0xffffu], hi = KD[tabv[i][q] >> 16];
                kq[i][q] = lo | (hi << 16);
            }
    }
    {
        const u32x4 z = {0, 0, 0, 0};
        if (lane < 29) {
            *(u32x4*)(cb + KA_OFF + 16 * lane) = z;
            *(u32x4*)(cb + KB_OFF + 16 * lane) = z;
        }
        uint32_t c = 0;
#pragma unroll
        for (int i = 0; i < 4; ++i) {
            put_k(cb, 64 * i + lane, kq[i], prev_lane(kq[i][3], c));
            c = __builtin_amdgcn_readlane(kq[i][3], 63);
        }
    }
    __builtin_amdgcn_s_setprio(0);
    STAMP(0, 3);

    const lds_t lb = (lds_t)lds + w * CH_STRIDE;
    const lds_t kaddr = lb + ((fr & 1) ? KA_OFF : KB_OFF) + 2 * (32 * (fr >> 1) + 8 * g + 8);
    lds_t xaddr = lb + ((fr & 1) ? (XA_OFF + 2 * (1822 - 2 * fr + 8 * g)) : (XB_OFF + 2 * (1820 - 2 * fr + 8 * g))) - 3584;
    asm volatile("" : "+v"(xaddr));
    const int lo_ = 64 * g + 2 * fr;
    const char* xa = cb + XA_OFF + 2 * (2046 - lo_);
    char* sg = cb + 2 * (510 - lo_);
#define EPI_ACC(S, OFF) do { \
        SB(); asm volatile("s_nop 7\n\ts_nop 7"); SB();     \
        epi_acc(acc[2 * (S)], xa - 512 * (2 * (S)), sg + (OFF), Dlo, Dhi, one); \
        epi_acc(acc[2 * (S) + 1], xa - 512 * (2 * (S) + 1), sg + (OFF) - 512, Dlo, Dhi, one); \
        flag_set(flags + 64 + 32 * (S) + 4 * w); } while (0)

    f32x4 acc[8];
#pragma unroll
    for (int i = 0; i < 8; ++i) acc[i] = (f32x4){0, 0, 0, 0};

    flag_wait(flags + 4 * (lane & 3));
    STAMP(0, 4);
    stage<0>(kaddr, xaddr, acc);
    STAMP(0, 5);
    EPI_ACC(0, SG0);
    STAMP(0, 6);
    flag_wait(flags + 16 + 4 * (lane & 3));
    STAMP(0, 7);
    stage<2>(kaddr, xaddr, acc);
    EPI_ACC(1, SG1);
    STAMP(1, 1);
    flag_wait(flags + 32 + 4 * (lane & 3));
    STAMP(1, 2);
    stage<4>(kaddr, xaddr, acc);
    EPI_ACC(2, SG2);
    STAMP(1, 3);
    flag_wait(flags + 48 + 4 * (lane & 3));
    STAMP(1, 4);
    {
        f32x4 a7b = {0, 0, 0, 0};
#pragma unroll
        for (int c = 0; c < 8; ++c) {
            const bf16x8 k0 = ldk(kaddr + 64 * c), k1 = ldk(kaddr + 64 * c + 512);
            const bf16x8 x6 = ldx(xaddr + 64 * c + 512), x7 = ldx(xaddr + 64 * c);
            MFMA(k0, x6, acc[6]);
            MFMA(k1, x6, acc[7]);
            MFMA(k0, x7, a7b);
        }
        acc[7] += a7b;
    }
    STAMP(1, 5);
    EPI_ACC(3, SG3);
    STAMP(1, 6);
    RT(0, 1);
}

extern "C" void kernel_launch(void* const* d_in, const int* in_sizes, int n_in,
                              void* d_out, int out_size, void* d_ws, size_t ws_size,
                              hipStream_t stream) {
    fused_kernel<<<256, 768, 0, stream>>>(
        (const uint16_t*)d_in[0], (const uint16_t*)d_in[1], (const uint16_t*)d_in[2], (const uint16_t*)d_in[3],
        (const uint16_t*)d_in[4], (const uint16_t*)d_in[5], (const uint16_t*)d_in[6], (const uint16_t*)d_in[7],
        (const uint16_t*)d_in[8], (const uint16_t*)d_in[9], (const uint16_t*)d_in[10], (const uint16_t*)d_in[11],
        (const uint16_t*)d_in[12], (uint16_t*)d_out);
}
```

```cpp
#include <hip/hip_runtime.h>
#include <stdint.h>

typedef __attribute__((ext_vector_type(4))) unsigned int u32x4;
typedef __attribute__((ext_vector_type(2))) unsigned int u32x2;
typedef __attribute__((ext_vector_type(4))) float f32x4;
typedef __attribute__((ext_vector_type(2))) float f32x2;
typedef __attribute__((ext_vector_type(8))) _Float16 f16x8;
typedef __attribute__((ext_vector_type(2))) _Float16 f16x2;
typedef __attribute__((ext_vector_type(8))) __bf16 bf16x8;
typedef __attribute__((ext_vector_type(2))) __bf16 bf16x2;
typedef __attribute__((ext_vector_type(4))) short s16x4;

#define STAMP(k, i)
#define STAMPW(k, i)
#define RT(k, i)
#define LSEQ 2048
#define DCH 1024
#define NMODE 128

__device__ __forceinline__ float bf2f(uint16_t v) { return __uint_as_float(((uint32_t)v) << 16); }
__device__ __forceinline__ float lo_f(uint32_t w) { return __uint_as_float(w << 16); }
__device__ __forceinline__ float hi_f(uint32_t w) { return __uint_as_float(w & 0xffff0000u); }
__device__ __forceinline__ uint32_t pk_bf16(float lo, float hi) {
    f32x2 x = {lo, hi};
    bf16x2 y = __builtin_convertvector(x, bf16x2);
    return __builtin_bit_cast(uint32_t, y);
}
__device__ __forceinline__ uint32_t pk_f16(float lo, float hi) {
    f32x2 x = {lo, hi};
    f16x2 y = __builtin_convertvector(x, f16x2);
    return __builtin_bit_cast(uint32_t, y);
}
__device__ __forceinline__ void rbf2(float& a, float& b) {
    uint32_t p = pk_bf16(a, b);
    a = lo_f(p);
    b = hi_f(p);
}
__device__ __forceinline__ float rbf(float a) { return lo_f(pk_bf16(a, a)); }

__device__ __forceinline__ uint32_t prefetch_code(uint64_t pc, int bytes) {
    const int off = (int)(threadIdx.x & 255) * 128;
    typedef __attribute__((address_space(1))) const uint32_t* gp_t;
    const uint32_t v = *(gp_t)((pc & ~63ull) + (off < bytes ? off : 0));
    __builtin_amdgcn_sched_barrier(0);
    return v;
}
#define KEEP(v) asm volatile("" :: "v"(v))
__device__ __forceinline__ f32x2 cmul(f32x2 a, f32x2 b) {
    f32x2 t, c;
    asm("v_pk_mul_f32 %0, %1, %2 op_sel:[0,0] op_sel_hi:[0,1]" : "=v"(t) : "v"(a), "v"(b));
    asm("v_pk_fma_f32 %0, %1, %2, %3 op_sel:[1,1,0] op_sel_hi:[1,0,1] neg_lo:[1,0,0]" : "=v"(c) : "v"(a), "v"(b), "v"(t));
    return c;
}
__device__ __forceinline__ f32x2 anchor(float a, float b) {
    const float e = __builtin_amdgcn_exp2f(a);
    const float th = __builtin_amdgcn_fractf(b);
    return (f32x2){e * __builtin_amdgcn_cosf(th), e * __builtin_amdgcn_sinf(th)};
}
__device__ __forceinline__ uint32_t pk16(f32x2 c) { return pk_f16(c[0], c[1]); }

#define KG_ROWB 528

#define DLY1 0
#define DLY2 0
#define DLY3 0
#define DELAY(n) do { _Pragma("unroll 1") for (int i_ = 0; i_ < (n); ++i_) __builtin_amdgcn_s_sleep(8); } while (0)
#define KA_OFF 0
#define KB_OFF 4640
#define XA_OFF 9216
#define XB_OFF 13440
#define CH_STRIDE 17600
#define KD_OFF (8 * CH_STRIDE)
#define FLAG_OFF (KD_OFF + 8 * 1536)
#define LDS_TOTAL (FLAG_OFF + 192)

__device__ __forceinline__ void put_k(char* cb, int y, u32x4 d, uint32_t prev) {
    *(u32x4*)(cb + KA_OFF + 464 + 16 * y) = d;
    u32x4 o;
    o[0] = __builtin_amdgcn_alignbyte(d[0], prev, 2);
    o[1] = __builtin_amdgcn_alignbyte(d[1], d[0], 2);
    o[2] = __builtin_amdgcn_alignbyte(d[2], d[1], 2);
    o[3] = __builtin_amdgcn_alignbyte(d[3], d[2], 2);
    *(u32x4*)(cb + KB_OFF + 464 + 16 * y) = o;
}
__device__ __forceinline__ void put_x4(char* p, uint32_t o0, uint32_t o1) {
    *(u32x2*)(p + XA_OFF) = (u32x2){o0, o1};
    *(uint32_t*)(p + XB_OFF - 4) = o0;
    *(uint32_t*)(p + XB_OFF) = o1;
}
__device__ __forceinline__ uint32_t prev_lane(uint32_t v, uint32_t carry) {
    return (uint32_t)__builtin_amdgcn_update_dpp((int)carry, (int)v, 0x138  , 0xf, 0xf, false);
}
typedef __attribute__((address_space(3))) char* lds_t;
typedef __attribute__((address_space(3))) const volatile u32x2* lds_v2;
typedef __attribute__((address_space(3))) volatile uint32_t* lds_vu;
typedef __attribute__((address_space(3))) const bf16x8* lds_k8;
__device__ __forceinline__ bf16x8 ldx(lds_t p) {
    u32x2 a = *(lds_v2)p, b = *(lds_v2)(p + 8);
    u32x4 v = {a[0], a[1], b[0], b[1]};
    return __builtin_bit_cast(bf16x8, v);
}
__device__ __forceinline__ bf16x8 ldk(lds_t p) { return *(lds_k8)p; }
#define MFMA(a, b, c) c = __builtin_amdgcn_mfma_f32_16x16x32_bf16(a, b, c, 0, 0, 0)
#define SB() __builtin_amdgcn_sched_barrier(0)
#define SG_DS(n) __builtin_amdgcn_sched_group_barrier(0x100, n, 0)
#define SG_MF(n) __builtin_amdgcn_sched_group_barrier(0x008, n, 0)

__device__ __forceinline__ f32x2 unpk(uint32_t w) { return (f32x2){lo_f(w), hi_f(w)}; }
__device__ __forceinline__ f32x2 rnd2(f32x2 a) { return unpk(pk_bf16(a[0], a[1])); }
__device__ __forceinline__ void epi_acc(const f32x4 a, const char* xa, char* dst, f32x2 D2) {
#pragma unroll
    for (int hh = 0; hh < 2; ++hh) {
        const uint32_t xd = *(const uint32_t*)(xa - 64 * hh);
        const f32x2 y = unpk(pk_bf16(a[2 * hh + 1], a[2 * hh])) + rnd2(unpk(xd) * D2);
        *(uint32_t*)(dst - 64 * hh) = pk_bf16(y[0], y[1]);
    }
}
__device__ __forceinline__ u32x4 row_read(const char* a0) {
    s16x4 v0 = __builtin_amdgcn_ds_read_tr16_b64_v4i16((__attribute__((address_space(3))) s16x4*)(a0));
    s16x4 v1 = __builtin_amdgcn_ds_read_tr16_b64_v4i16((__attribute__((address_space(3))) s16x4*)(a0 + 4 * CH_STRIDE));
    u32x2 p0 = __builtin_bit_cast(u32x2, v0), p1 = __builtin_bit_cast(u32x2, v1);
    return (u32x4){p0[0], p0[1], p1[0], p1[1]};
}
__device__ __forceinline__ u32x4 gate(u32x4 y, u32x4 q) {
    u32x4 o;
#pragma unroll
    for (int j = 0; j < 4; ++j) {
        const f32x2 p = unpk(q[j]) * unpk(y[j]);
        o[j] = pk_bf16(p[0], p[1]);
    }
    return o;
}
__device__ __forceinline__ void lds_barrier() {
    __builtin_amdgcn_fence(__ATOMIC_RELEASE, "workgroup", "local");
    __builtin_amdgcn_s_barrier();
    __builtin_amdgcn_fence(__ATOMIC_ACQUIRE, "workgroup", "local");
}
__device__ __forceinline__ void flag_set(lds_t p) {
    __builtin_amdgcn_fence(__ATOMIC_RELEASE, "workgroup", "local");
    *(lds_vu)p = 1u;
}
__device__ __forceinline__ void flag_wait(lds_t p) {
    while (__builtin_amdgcn_ballot_w64(*(lds_vu)p == 0u) != 0ull) __builtin_amdgcn_s_sleep(1);
    __builtin_amdgcn_fence(__ATOMIC_ACQUIRE, "workgroup", "local");
}

template <int T0>
__device__ __forceinline__ void stage(lds_t kaddr, lds_t xaddr, f32x4 (&acc)[8]) {
    constexpr int NK = 8 - T0;
    bf16x8 ka[NK], xa[2], kb[NK], xb[2];
#pragma unroll
    for (int m = 0; m < NK; ++m) ka[m] = ldk(kaddr + 512 * m);
#pragma unroll
    for (int t = 0; t < 2; ++t) xa[t] = ldx(xaddr + 512 * (7 - T0 - t));
#pragma unroll 1
    for (int c0 = 0; c0 < 8; c0 += 2) {
        const lds_t kp = kaddr + 64 * c0, xp = xaddr + 64 * c0;
#pragma unroll
        for (int t = 0; t < 2; ++t) xb[t] = ldx(xp + 64 + 512 * (7 - T0 - t));
#pragma unroll
        for (int m = 0; m < NK; ++m) kb[m] = ldk(kp + 64 + 512 * m);
#pragma unroll
        for (int m = 0; m < NK; ++m) MFMA(ka[m], xa[0], acc[T0 + m]);
#pragma unroll
        for (int m = 0; m < NK - 1; ++m) MFMA(ka[m], xa[1], acc[T0 + 1 + m]);
#pragma unroll
        for (int i = 0; i < NK + 4; ++i) { SG_MF(1); SG_DS(1); }
        SG_MF(NK);
        SB();
#pragma unroll
        for (int t = 0; t < 2; ++t) xa[t] = ldx(xp + 128 + 512 * (7 - T0 - t));
#pragma unroll
        for (int m = 0; m < NK; ++m) ka[m] = ldk(kp + 128 + 512 * m);
#pragma unroll
        for (int m = 0; m < NK; ++m) MFMA(kb[m], xb[0], acc[T0 + m]);
#pragma unroll
        for (int m = 0; m < NK - 1; ++m) MFMA(kb[m], xb[1], acc[T0 + 1 + m]);
#pragma unroll
        for (int i = 0; i < NK + 4; ++i) { SG_MF(1); SG_DS(1); }
        SG_MF(NK);
        SB();
    }
}

__global__ __launch_bounds__(768) void fused_kernel(
    const uint16_t* __restrict__ u, const uint16_t* __restrict__ logA, const uint16_t* __restrict__ Aim,
    const uint16_t* __restrict__ Cre, const uint16_t* __restrict__ Cim, const uint16_t* __restrict__ logdt,
    const uint16_t* __restrict__ Dsk, const uint16_t* __restrict__ qin, const uint16_t* __restrict__ kin,
    const uint16_t* __restrict__ vin, const uint16_t* __restrict__ shk, const uint16_t* __restrict__ ssmk,
    const uint16_t* __restrict__ sD, uint16_t* __restrict__ out)
{
    __shared__ __attribute__((aligned(16))) char lds[LDS_TOTAL];
    const int b = blockIdx.x;
    const int xcd = b & 7, idx = b >> 3, part = idx >> 4, gi = idx & 15, grp = xcd * 16 + gi;
    const int lane = threadIdx.x & 63;
    const int w = __builtin_amdgcn_readfirstlane(threadIdx.x >> 6);
    const lds_t flags = (lds_t)lds + FLAG_OFF;
    const int g = lane >> 4, fr = lane & 15;
#define SG0 (KA_OFF + 3536)
#define SG1 (KB_OFF + 3536)
#define SG2 (KA_OFF + 2512)
#define SG3 (KB_OFF + 2512)

    uint64_t pc0;
    asm volatile("s_getpc_b64 %0" : "=s"(pc0));
    RT(0, 0);
    STAMP(0, 0);
    STAMP(1, 0);
    if (w >= 8) {
        const int pw = w - 8, cq = lane & 1, rq = lane >> 1;
        const int s0 = 128 * pw + 4 * rq;
        const size_t eo = (size_t)s0 * DCH + grp * 8 + 4 * cq;
        if (threadIdx.x - 512 < 48) *(uint32_t*)(lds + FLAG_OFF + 4 * (threadIdx.x - 512)) = 0u;
        const uint32_t pf = prefetch_code(pc0, 24576);
        lds_barrier();
        char* xp = lds + 4 * cq * CH_STRIDE + 2 * (2044 - s0);
        const int qq = (lane >> 2) & 3, pp = lane & 3;
        const char* rg = lds + qq * CH_STRIDE + (128 * pw + 16 * g + 4 * pp) * 2;
        const int r0 = 511 - (128 * pw + lane);
        uint16_t* ob = out + part * DCH + grp * 8 + (size_t)r0 * (2 * DCH);
#define ROW_OUT(S) (ob + (ptrdiff_t)(512 * (S)) * (2 * DCH))
#define ROW_WAIT(S) flag_wait(flags + 64 + 32 * (S) + 4 * (lane & 7))
        if (part) {
            u32x2 kk[4][4], vv[4][4], hk[4][3];
            const u32x2 zero2 = {0, 0};
            const int hs = 128 * pw + 32 * g - 3;
            const ptrdiff_t ho = (ptrdiff_t)hs * DCH + grp * 8 + 4 * cq;
#define LDK(P, i) kk[P][i] = *(const u32x2*)(kin + eo + (size_t)(512 * (P) + (i)) * DCH)
#define LDV(P, i) vv[P][i] = *(const u32x2*)(vin + eo + (size_t)(512 * (P) + (i)) * DCH)
#define LDH(P) do { _Pragma("unroll") for (int i = 0; i < 3; ++i) { \
                const bool z_ = (P) == 0 && hs < 0; \
                hk[P][i] = *(const u32x2*)(kin + (z_ ? (ptrdiff_t)0 : ho + (ptrdiff_t)(512 * (P) + i) * DCH)); \
                if (z_) hk[P][i] = zero2; } } while (0)
#define LDQ(i) qv[i] = *(const u32x4*)(qin + (size_t)(r0 - 64 * ((i) & 1) + 512 * ((i) >> 1)) * DCH + grp * 8)
            u32x4 wv[2], qv[8];
#pragma unroll
            for (int i = 0; i < 2; ++i) wv[i] = *(const u32x4*)(shk + (size_t)(grp * 8 + 4 * cq) * 4 + 8 * i);
#pragma unroll
            for (int p = 0; p < 2; ++p) {
                LDK(p, 0); LDK(p, 1); LDK(p, 2); LDK(p, 3); LDH(p); LDV(p, 0); LDV(p, 1); LDV(p, 2); LDV(p, 3);
                SB();
            }
            f32x2 wt[2][4];
#pragma unroll
            for (int j = 0; j < 2; ++j) {
                wt[j][0] = (f32x2){lo_f(wv[j][0]), lo_f(wv[j][2])};
                wt[j][1] = (f32x2){hi_f(wv[j][0]), hi_f(wv[j][2])};
                wt[j][2] = (f32x2){lo_f(wv[j][1]), lo_f(wv[j][3])};
                wt[j][3] = (f32x2){hi_f(wv[j][1]), hi_f(wv[j][3])};
            }
#define KVROW(P, j, r) (rnd2(rnd2(rnd2(rnd2(kf[r] * wt[j][3]) + rnd2(kf[(r) + 1] * wt[j][2])) + rnd2(kf[(r) + 2] * wt[j][1])) \
                             + rnd2(kf[(r) + 3] * wt[j][0])) * unpk(vv[P][r][j]))
#define KVCOL(P, j, LA, LB) do { \
            f32x2 kf[7]; \
            _Pragma("unroll") for (int i = 0; i < 3; ++i) \
                kf[i] = unpk((uint32_t)__builtin_amdgcn_update_dpp((int)hk[P][i][j], (int)kk[P][1 + i][j], 0x112  , 0xf, 0xf, false)); \
            _Pragma("unroll") for (int i = 0; i < 4; ++i) kf[3 + i] = unpk(kk[P][i][j]); \
            const f32x2 y0 = KVROW(P, j, 0), y1 = KVROW(P, j, 1); \
            SB(); LA; SB(); \
            const f32x2 y2 = KVROW(P, j, 2), y3 = KVROW(P, j, 3); \
            put_x4(xp + (2 * (j)) * CH_STRIDE - 1024 * (P), pk_bf16(y3[0], y2[0]), pk_bf16(y1[0], y0[0])); \
            put_x4(xp + (2 * (j) + 1) * CH_STRIDE - 1024 * (P), pk_bf16(y3[1], y2[1]), pk_bf16(y1[1], y0[1])); \
            SB(); LB; SB(); } while (0)
#define PASS(P, L0, L1, L2, L3) do { \
            KVCOL(P, 0, L0, L1); \
            KVCOL(P, 1, L2, flag_set(flags + 16 * (P) + 4 * pw)); \
            L3; SB(); } while (0)
            STAMP(0, 1);
            PASS(0, { LDK(2, 0); LDK(2, 1); }, { LDK(2, 2); LDK(2, 3); LDH(2); }, { LDV(2, 0); LDV(2, 1); }, { LDV(2, 2); LDV(2, 3); });
            STAMP(0, 2);
            PASS(1, { LDK(3, 0); LDK(3, 1); }, { LDK(3, 2); LDK(3, 3); LDH(3); }, { LDV(3, 0); LDV(3, 1); }, { LDV(3, 2); LDV(3, 3); });
            STAMP(0, 3);
            PASS(2, LDQ(0), LDQ(1), LDQ(2), LDQ(3));
            STAMP(0, 4);
            PASS(3, LDQ(4), LDQ(5), LDQ(6), LDQ(7));
            STAMP(0, 5);
#define EPI_ROWB(S, OFF) do { \
            ROW_WAIT(S); \
            *(u32x4*)ROW_OUT(S) = gate(row_read(rg + (OFF)), qv[2 * (S)]); \
            *(u32x4*)(ROW_OUT(S) - 64 * (2 * DCH)) = gate(row_read(rg + (OFF) + 128), qv[2 * (S) + 1]); } while (0)
            EPI_ROWB(0, SG0);
            STAMP(0, 6);
            EPI_ROWB(1, SG1);
            STAMP(0, 7);
            EPI_ROWB(2, SG2);
            STAMP(1, 1);
            EPI_ROWB(3, SG3);
        } else {
            const int t = threadIdx.x - 512;
            const size_t po = (size_t)(64 * gi + (t >> 2)) * DCH + 128 * xcd + 64 * (t & 1);
            const uint16_t* p1 = ((t & 2) ? vin : kin) + po;
            const uint16_t* p2 = ((t & 2) ? qin + (size_t)1024 * DCH : qin) + po;
            uint32_t pfv[3];
            u32x2 uu[4][4];
            pfv[0] = *(const uint32_t*)p1;
            SB();
            DELAY(DLY1);
#pragma unroll
            for (int i = 0; i < 8; ++i) uu[i >> 2][i & 3] = *(const u32x2*)(u + eo + (size_t)(512 * (i >> 2) + (i & 3)) * DCH);
            SB();
            DELAY(DLY2);
            pfv[1] = *(const uint32_t*)(p1 + (size_t)1024 * DCH);
            SB();
            DELAY(DLY3);
#pragma unroll
            for (int i = 8; i < 16; ++i) uu[i >> 2][i & 3] = *(const u32x2*)(u + eo + (size_t)(512 * (i >> 2) + (i & 3)) * DCH);
            SB();
            pfv[2] = *(const uint32_t*)p2;
            SB();
            STAMP(0, 1);
#pragma unroll
            for (int p = 0; p < 4; ++p) {
#pragma unroll
                for (int ch = 0; ch < 4; ++ch) {
                    const uint32_t sel = (ch & 1) ? 0x07060302u : 0x05040100u;
                    put_x4(xp + ch * CH_STRIDE - 1024 * p, __builtin_amdgcn_perm(uu[p][2][ch >> 1], uu[p][3][ch >> 1], sel),
                           __builtin_amdgcn_perm(uu[p][0][ch >> 1], uu[p][1][ch >> 1], sel));
                }
                flag_set(flags + 16 * p + 4 * pw);
                SB();
                STAMP(0, 2 + p);
            }
#define EPI_ROWA(S, OFF) do { \
            ROW_WAIT(S); \
            *(u32x4*)ROW_OUT(S) = row_read(rg + (OFF)); \
            *(u32x4*)(ROW_OUT(S) - 64 * (2 * DCH)) = row_read(rg + (OFF) + 128); } while (0)
            EPI_ROWA(0, SG0);
            STAMP(0, 6);
            EPI_ROWA(1, SG1);
            STAMP(0, 7);
            EPI_ROWA(2, SG2);
            STAMP(1, 1);
            EPI_ROWA(3, SG3);
            KEEP(pfv[0]);
            KEEP(pfv[1]);
            KEEP(pfv[2]);
        }
        STAMP(1, 6);
        STAMPW(1, 7);
        KEEP(pf);
        RT(0, 1);
        return;
    }

    const int chl = grp * 8 + w;
    char* cb = lds + w * CH_STRIDE;
    u32x4 kq[4];
    uint32_t pr[8];
    float dt = 0.f;
    if (part) {
        const uint16_t* ksrc = ssmk + (size_t)chl * LSEQ;
#pragma unroll
        for (int i = 0; i < 4; ++i) kq[i] = *(const u32x4*)(ksrc + 8 * (64 * i + lane));
    } else {
        dt = bf2f(logdt[chl]);
#pragma unroll
        for (int bt = 0; bt < 2; ++bt) {
            const int n = chl * NMODE + bt * 64 + lane;
            pr[4 * bt] = logA[n]; pr[4 * bt + 1] = Aim[n]; pr[4 * bt + 2] = Cre[n]; pr[4 * bt + 3] = Cim[n];
        }
    }
    const float Dv = bf2f((part ? sD : Dsk)[chl]);
    const f32x2 D2 = {Dv, Dv};
    SB();
    {
        const u32x4 z = {0, 0, 0, 0};
        if (lane < 4) *(u32x4*)(cb + XA_OFF + 4096 + 16 * lane) = z;
        if (lane < 5) *(u32x4*)(cb + XB_OFF + 4080 + 16 * lane) = z;
    }
    lds_barrier();

    if (!part) {
        if (w < 4) __builtin_amdgcn_s_setprio(2);
        dt = rbf(expf(dt));
        float pa[2], pb[2], pcr[2], pci[2];
        float mx = 0.f;
#pragma unroll
        for (int bt = 0; bt < 2; ++bt) {
            const float ar = -rbf(expf(lo_f(pr[4 * bt])));
            pa[bt] = (ar * dt) * 1.4426950408889634f;
            pb[bt] = (lo_f(pr[4 * bt + 1]) * dt) * 0.15915494309189535f;
            pcr[bt] = 2.f * (lo_f(pr[4 * bt + 2]) * dt);
            pci[bt] = 2.f * (lo_f(pr[4 * bt + 3]) * dt);
            mx = fmaxf(mx, fmaxf(fabsf(pcr[bt]), fabsf(pci[bt])));
        }
        int mi = (int)__float_as_uint(mx);
#define DPPMAX(ctrl) mi = max(mi, __builtin_amdgcn_update_dpp(0, mi, ctrl, 0xf, 0xf, false))
        DPPMAX(0x111); DPPMAX(0x112); DPPMAX(0x114); DPPMAX(0x118); DPPMAX(0x142); DPPMAX(0x143);
        int sf = 266 - ((__builtin_amdgcn_readlane(mi, 63) >> 23) & 0xff);
        sf = sf < 1 ? 1 : (sf > 239 ? 239 : sf);
        const float scl = __uint_as_float((uint32_t)sf << 23);
        const float inv = __uint_as_float((uint32_t)(240 - sf) << 23);
        STAMP(0, 1);

        char* Tl = cb + lane * 8;
        const char* Tr = cb + fr * KG_ROWB + g * 16;
#define TW(row, v0, v1) *(u32x2*)(Tl + (row) * KG_ROWB) = (u32x2){pk16(v0), pk16(v1)}
        f16x8 bfr[8];
        {
            f32x2 f[2][16];
#pragma unroll
            for (int bt = 0; bt < 2; ++bt) {
                const f32x2 z8c = anchor(pa[bt] * 8.f, pb[bt] * -8.f);
                const f32x2 z16c = cmul(z8c, z8c), z32c = cmul(z16c, z16c), z64c = cmul(z32c, z32c);
                f[bt][0] = (f32x2){16384.f, 0.f};
                f[bt][1] = z8c * 16384.f;
                f[bt][2] = z16c * 16384.f;
                f[bt][4] = z32c * 16384.f;
                f[bt][8] = z64c * 16384.f;
                f[bt][3] = cmul(f[bt][2], z8c);
#pragma unroll
                for (int i = 1; i < 4; ++i) f[bt][4 + i] = cmul(f[bt][i], z32c);
#pragma unroll
                for (int i = 1; i < 8; ++i) f[bt][8 + i] = cmul(f[bt][i], z64c);
            }
#pragma unroll
            for (int i = 0; i < 16; ++i) TW(i, f[0][i], f[1][i]);
#pragma unroll
            for (int ks = 0; ks < 8; ++ks) bfr[ks] = *(const f16x8*)(Tr + ks * 64);
        }
        f32x4 ac[3][2];
#define TMM(rt) do { \
            ac[rt][0] = (f32x4){0, 0, 0, 0}; ac[rt][1] = (f32x4){0, 0, 0, 0}; \
            _Pragma("unroll") for (int ks = 0; ks < 8; ++ks) \
                ac[rt][ks & 1] = __builtin_amdgcn_mfma_f32_16x16x32_f16(*(const f16x8*)(Tr + ks * 64), bfr[ks], ac[rt][ks & 1], 0, 0, 0); } while (0)
        f32x2 h[2][8], cg[2][17], z512[2], z1024[2];
#pragma unroll
        for (int bt = 0; bt < 2; ++bt) {
            const f32x2 z1 = anchor(pa[bt], pb[bt]), z128 = anchor(pa[bt] * 128.f, pb[bt] * 128.f);
            const f32x2 z256 = cmul(z128, z128);
            z512[bt] = cmul(z256, z256);
            z1024[bt] = cmul(z512[bt], z512[bt]);
            h[bt][1] = z1; h[bt][2] = cmul(z1, z1); h[bt][3] = cmul(h[bt][2], z1); h[bt][4] = cmul(h[bt][2], h[bt][2]);
            h[bt][5] = cmul(h[bt][4], z1); h[bt][6] = cmul(h[bt][4], h[bt][2]); h[bt][7] = cmul(h[bt][4], h[bt][3]);
            cg[bt][0] = (f32x2){pcr[bt] * scl, pci[bt] * scl};
            cg[bt][1] = cmul(cg[bt][0], z128);
            cg[bt][2] = cmul(cg[bt][0], z256);
            cg[bt][3] = cmul(cg[bt][2], z128);
        }
#pragma unroll
        for (int aa = 0; aa < 2; ++aa) {
            TW(8 * aa, cg[0][aa], cg[1][aa]);
#pragma unroll
            for (int e = 1; e < 8; ++e) TW(8 * aa + e, cmul(cg[0][aa], h[0][e]), cmul(cg[1][aa], h[1][e]));
        }
        TMM(0);
#pragma unroll
        for (int bt = 0; bt < 2; ++bt)
#pragma unroll
            for (int i = 0; i < 4; ++i) cg[bt][4 + i] = cmul(cg[bt][i], z512[bt]);
#pragma unroll
        for (int aa = 2; aa < 4; ++aa) {
            TW(4 * (aa - 2), cg[0][aa], cg[1][aa]);
#pragma unroll
            for (int e = 1; e < 4; ++e) TW(4 * (aa - 2) + e, cmul(cg[0][aa], h[0][2 * e]), cmul(cg[1][aa], h[1][2 * e]));
        }
#pragma unroll
        for (int aa = 4; aa < 8; ++aa) {
            TW(8 + 2 * (aa - 4), cg[0][aa], cg[1][aa]);
            TW(8 + 2 * (aa - 4) + 1, cmul(cg[0][aa], h[0][4]), cmul(cg[1][aa], h[1][4]));
        }
        TMM(1);
#pragma unroll
        for (int bt = 0; bt < 2; ++bt) {
#pragma unroll
            for (int i = 0; i < 8; ++i) cg[bt][8 + i] = cmul(cg[bt][i], z1024[bt]);
            cg[bt][16] = cmul(cg[bt][8], z1024[bt]);
        }
#pragma unroll
        for (int aa = 8; aa <= 16; ++aa) TW(aa - 8, cg[0][aa], cg[1][aa]);
        TMM(2);
        STAMP(0, 2);
        uint16_t* KD = (uint16_t*)(lds + KD_OFF + w * 1536);
#pragma unroll
        for (int rt = 0; rt < 3; ++rt) {
            const f32x4 v = (ac[rt][0] + ac[rt][1]) * inv;
#pragma unroll
            for (int r = 0; r < 4; r += 2) {
                uint32_t p = pk_bf16(v[r], v[r + 1]);
                KD[(16 * rt + 4 * g + r) * 16 + fr] = (uint16_t)p;
                KD[(16 * rt + 4 * g + r + 1) * 16 + fr] = (uint16_t)(p >> 16);
            }
        }
        {
            const int hi = lane >> 5, last = (fr == 15);
            const uint16_t* p0 = KD + (hi ? 16 + 4 * (g - 2) : 8 * g) * 16 + fr;
            const uint16_t* p1 = p0 - 16 * hi, * p2 = p0 - 32 * hi, * p3 = p0 - 48 * hi;
            const uint16_t* p7 = p0 + (hi ? (last ? 49 : 1) : 112);
            kq[0][0] = (uint32_t)p0[0] | ((uint32_t)p1[16] << 16);
            kq[0][1] = (uint32_t)p1[32] | ((uint32_t)p1[48] << 16);
            kq[0][2] = (uint32_t)p2[64] | ((uint32_t)p3[80] << 16);
            kq[0][3] = (uint32_t)p3[96] | ((uint32_t)p7[0] << 16);
            const uint16_t* q0 = KD + (24 + 2 * g) * 16 + fr;
            const uint32_t a = q0[0], bq = q0[16], c = q0[last ? 17 : 1];
            kq[1][0] = __builtin_amdgcn_perm(a, a, 0x01000100u);
            kq[1][1] = __builtin_amdgcn_perm(bq, a, 0x05040100u);
            kq[1][2] = __builtin_amdgcn_perm(bq, bq, 0x01000100u);
            kq[1][3] = __builtin_amdgcn_perm(c, c, 0x01000100u);
            const uint32_t sel = (lane & 1) ? 0x05040504u : 0x05040100u;
#pragma unroll
            for (int i = 2; i < 4; ++i) {
                const uint32_t e0 = KD[512 + 64 * (i - 2) + lane], e1 = KD[513 + 64 * (i - 2) + lane];
                kq[i][0] = kq[i][1] = __builtin_amdgcn_perm(e0, e0, 0x01000100u);
                kq[i][2] = __builtin_amdgcn_perm(e1, e0, sel);
                kq[i][3] = __builtin_amdgcn_perm(e1, e1, 0x01000100u);
            }
        }
    }
    {
        const u32x4 z = {0, 0, 0, 0};
        if (lane < 29) {
            *(u32x4*)(cb + KA_OFF + 16 * lane) = z;
            *(u32x4*)(cb + KB_OFF + 16 * lane) = z;
        }
        uint32_t c = 0;
#pragma unroll
        for (int i = 0; i < 4; ++i) {
            put_k(cb, 64 * i + lane, kq[i], prev_lane(kq[i][3], c));
            c = __builtin_amdgcn_readlane(kq[i][3], 63);
        }
    }
    __builtin_amdgcn_s_setprio(0);
    STAMP(0, 3);

    const lds_t lb = (lds_t)lds + w * CH_STRIDE;
    const lds_t kaddr = lb + ((fr & 1) ? KA_OFF : KB_OFF) + 2 * (32 * (fr >> 1) + 8 * g + 8);
    lds_t xaddr = lb + ((fr & 1) ? (XA_OFF + 2 * (1822 - 2 * fr + 8 * g)) : (XB_OFF + 2 * (1820 - 2 * fr + 8 * g))) - 3584;
    asm volatile("" : "+v"(xaddr));
    const int lo_ = 64 * g + 2 * fr;
    const char* xa = cb + XA_OFF + 2 * (2046 - lo_);
    char* sg = cb + 2 * (510 - lo_);
#define EPI_ACC(S, OFF) do { \
        epi_acc(acc[2 * (S)], xa - 512 * (2 * (S)), sg + (OFF), D2); \
        epi_acc(acc[2 * (S) + 1], xa - 512 * (2 * (S) + 1), sg + (OFF) - 512, D2); \
        flag_set(flags + 64 + 32 * (S) + 4 * w); } while (0)

    f32x4 acc[8];
#pragma unroll
    for (int i = 0; i < 8; ++i) acc[i] = (f32x4){0, 0, 0, 0};

    flag_wait(flags + 4 * (lane & 3));
    STAMP(0, 4);
    stage<0>(kaddr, xaddr, acc);
    STAMP(0, 5);
    EPI_ACC(0, SG0);
    STAMP(0, 6);
    flag_wait(flags + 16 + 4 * (lane & 3));
    STAMP(0, 7);
    stage<2>(kaddr, xaddr, acc);
    EPI_ACC(1, SG1);
    STAMP(1, 1);
    flag_wait(flags + 32 + 4 * (lane & 3));
    STAMP(1, 2);
    stage<4>(kaddr, xaddr, acc);
    EPI_ACC(2, SG2);
    STAMP(1, 3);
    flag_wait(flags + 48 + 4 * (lane & 3));
    STAMP(1, 4);
    {
        f32x4 a7b = {0, 0, 0, 0};
#pragma unroll
        for (int c = 0; c < 8; ++c) {
            const bf16x8 k0 = ldk(kaddr + 64 * c), k1 = ldk(kaddr + 64 * c + 512);
            const bf16x8 x6 = ldx(xaddr + 64 * c + 512), x7 = ldx(xaddr + 64 * c);
            MFMA(k0, x6, acc[6]);
            MFMA(k1, x6, acc[7]);
            MFMA(k0, x7, a7b);
        }
        acc[7] += a7b;
    }
    STAMP(1, 5);
    EPI_ACC(3, SG3);
    STAMP(1, 6);
    RT(0, 1);
}

extern "C" void kernel_launch(void* const* d_in, const int* in_sizes, int n_in,
                              void* d_out, int out_size, void* d_ws, size_t ws_size,
                              hipStream_t stream) {
    fused_kernel<<<256, 768, 0, stream>>>(
        (const uint16_t*)d_in[0], (const uint16_t*)d_in[1], (const uint16_t*)d_in[2], (const uint16_t*)d_in[3],
        (const uint16_t*)d_in[4], (const uint16_t*)d_in[5], (const uint16_t*)d_in[6], (const uint16_t*)d_in[7],
        (const uint16_t*)d_in[8], (const uint16_t*)d_in[9], (const uint16_t*)d_in[10], (const uint16_t*)d_in[11],
        (const uint16_t*)d_in[12], (uint16_t*)d_out);
}
```

```cpp
#include <hip/hip_runtime.h>
#include <stdint.h>

typedef __attribute__((ext_vector_type(4))) unsigned int u32x4;
typedef __attribute__((ext_vector_type(2))) unsigned int u32x2;
typedef __attribute__((ext_vector_type(4))) float f32x4;
typedef __attribute__((ext_vector_type(2))) float f32x2;
typedef __attribute__((ext_vector_type(8))) _Float16 f16x8;
typedef __attribute__((ext_vector_type(2))) _Float16 f16x2;
typedef __attribute__((ext_vector_type(8))) __bf16 bf16x8;
typedef __attribute__((ext_vector_type(2))) __bf16 bf16x2;
typedef __attribute__((ext_vector_type(4))) short s16x4;

#define STAMP(k, i)
#define STAMPW(k, i)
#define RT(k, i)
#define LSEQ 2048
#define DCH 1024
#define NMODE 128

__device__ __forceinline__ float bf2f(uint16_t v) { return __uint_as_float(((uint32_t)v) << 16); }
__device__ __forceinline__ float lo_f(uint32_t w) { return __uint_as_float(w << 16); }
__device__ __forceinline__ float hi_f(uint32_t w) { return __uint_as_float(w & 0xffff0000u); }
__device__ __forceinline__ uint32_t pk_bf16(float lo, float hi) {
    f32x2 x = {lo, hi};
    bf16x2 y = __builtin_convertvector(x, bf16x2);
    return __builtin_bit_cast(uint32_t, y);
}
__device__ __forceinline__ uint32_t pk_f16(float lo, float hi) {
    f32x2 x = {lo, hi};
    f16x2 y = __builtin_convertvector(x, f16x2);
    return __builtin_bit_cast(uint32_t, y);
}
__device__ __forceinline__ float rbf(float a) { return lo_f(pk_bf16(a, a)); }

#define KEEP(v) asm volatile("" :: "v"(v))
#define KEEPS(v) asm volatile("" :: "s"(v))
#define OPAQUE(v) asm volatile("" : "+v"(v))
#define SB() __builtin_amdgcn_sched_barrier(0)
__device__ __forceinline__ f32x2 cm1(f32x2 a, f32x2 b) {
    f32x2 t;
    asm("v_pk_mul_f32 %0, %1, %2 op_sel:[0,0] op_sel_hi:[0,1]" : "=v"(t) : "v"(a), "v"(b));
    return t;
}
__device__ __forceinline__ f32x2 cm2(f32x2 a, f32x2 b, f32x2 t) {
    f32x2 c;
    asm("v_pk_fma_f32 %0, %1, %2, %3 op_sel:[1,1,0] op_sel_hi:[1,0,1] neg_lo:[1,0,0]" : "=v"(c) : "v"(a), "v"(b), "v"(t));
    return c;
}
__device__ __forceinline__ f32x2 cmul(f32x2 a, f32x2 b) { return cm2(a, b, cm1(a, b)); }
__device__ __forceinline__ f32x2 anchor(float a, float b) {
    const float e = __builtin_amdgcn_exp2f(a);
    const float th = __builtin_amdgcn_fractf(b);
    return (f32x2){e * __builtin_amdgcn_cosf(th), e * __builtin_amdgcn_sinf(th)};
}
__device__ __forceinline__ uint32_t pk16(f32x2 c) { return pk_f16(c[0], c[1]); }

#define KG_ROWB 528

#define KA_OFF 0
#define KB_OFF 4640
#define XA_OFF 9216
#define XB_OFF 13440
#define CH_STRIDE 17696
#define KD_OFF (8 * CH_STRIDE)
#define FLAG_OFF (KD_OFF + 8 * 1536)
#define LDS_TOTAL (FLAG_OFF + 288)
#define CODE_BYTES 23424

__device__ __forceinline__ void put_k(char* cb, int y, u32x4 d, uint32_t prev) {
    *(u32x4*)(cb + KA_OFF + 464 + 16 * y) = d;
    u32x4 o;
    o[0] = __builtin_amdgcn_alignbyte(d[0], prev, 2);
    o[1] = __builtin_amdgcn_alignbyte(d[1], d[0], 2);
    o[2] = __builtin_amdgcn_alignbyte(d[2], d[1], 2);
    o[3] = __builtin_amdgcn_alignbyte(d[3], d[2], 2);
    *(u32x4*)(cb + KB_OFF + 464 + 16 * y) = o;
}
__device__ __forceinline__ void put_x4(char* p, uint32_t o0, uint32_t o1) {
    *(u32x2*)(p + XA_OFF) = (u32x2){o0, o1};
    *(uint32_t*)(p + XB_OFF - 4) = o0;
    *(uint32_t*)(p + XB_OFF) = o1;
}
__device__ __forceinline__ uint32_t prev_lane(uint32_t v, uint32_t carry) {
    return (uint32_t)__builtin_amdgcn_update_dpp((int)carry, (int)v, 0x138  , 0xf, 0xf, false);
}
typedef __attribute__((address_space(3))) char* lds_t;
typedef __attribute__((address_space(3))) const volatile u32x2* lds_v2;
typedef __attribute__((address_space(3))) volatile uint32_t* lds_vu;
typedef __attribute__((address_space(3))) const bf16x8* lds_k8;
typedef __attribute__((address_space(1))) const char* gc_t;
typedef __attribute__((address_space(1))) char* gw_t;
#define GLD(T, base, voff, imm) (*(__attribute__((address_space(1))) const T*)((base) + (voff) + (imm)))
__device__ __forceinline__ bf16x8 ldx(lds_t p) {
    u32x2 a = *(lds_v2)p, b = *(lds_v2)(p + 8);
    u32x4 v = {a[0], a[1], b[0], b[1]};
    return __builtin_bit_cast(bf16x8, v);
}
__device__ __forceinline__ bf16x8 ldk(lds_t p) { return *(lds_k8)p; }
#define MFMA(a, b, c) c = __builtin_amdgcn_mfma_f32_16x16x32_bf16(a, b, c, 0, 0, 0)
#define SG_DS(n) __builtin_amdgcn_sched_group_barrier(0x100, n, 0)
#define SG_MF(n) __builtin_amdgcn_sched_group_barrier(0x008, n, 0)

#define RB(r) "v_cvt_pk_bf16_f32 " r ", 0, " r "\n\t"
__device__ __forceinline__ void kv4(const float (&k)[7], const float (&wt)[4], const float (&v)[4], uint32_t& o0, uint32_t& o1) {
    float a0, a1, b0, b1;
#define KVPAIR(A, B, C, D, E, V0, V1, O) \
    "v_mul_f32 %2, " A ", %16\n\t" "v_mul_f32 %3, " B ", %16\n\t" "v_mul_f32 %4, " B ", %15\n\t" "v_mul_f32 %5, " C ", %15\n\t" \
    RB("%2") RB("%3") RB("%4") RB("%5") \
    "v_add_f32 %2, %2, %4\n\t" "v_add_f32 %3, %3, %5\n\t" "v_mul_f32 %4, " C ", %14\n\t" "v_mul_f32 %5, " D ", %14\n\t" \
    RB("%2") RB("%3") RB("%4") RB("%5") \
    "v_add_f32 %2, %2, %4\n\t" "v_add_f32 %3, %3, %5\n\t" "v_mul_f32 %4, " D ", %13\n\t" "v_mul_f32 %5, " E ", %13\n\t" \
    RB("%2") RB("%3") RB("%4") RB("%5") \
    "v_add_f32 %2, %2, %4\n\t" "v_add_f32 %3, %3, %5\n\t" \
    RB("%2") RB("%3") \
    "v_mul_f32 %2, %2, " V0 "\n\t" "v_mul_f32 %3, %3, " V1 "\n\t" \
    "v_cvt_pk_bf16_f32 " O ", %3, %2\n\t"
    asm(KVPAIR("%6", "%7", "%8", "%9", "%10", "%17", "%18", "%1")
        KVPAIR("%8", "%9", "%10", "%11", "%12", "%19", "%20", "%0")
        : "=&v"(o0), "=&v"(o1), "=&v"(a0), "=&v"(a1), "=&v"(b0), "=&v"(b1)
        : "v"(k[0]), "v"(k[1]), "v"(k[2]), "v"(k[3]), "v"(k[4]), "v"(k[5]), "v"(k[6]),
          "v"(wt[0]), "v"(wt[1]), "v"(wt[2]), "v"(wt[3]), "v"(v[0]), "v"(v[1]), "v"(v[2]), "v"(v[3]));
}
__device__ __forceinline__ void epi_acc(const f32x4 a, const char* xa, char* dst, float D) {
    const uint32_t x0 = *(const uint32_t*)(xa), x1 = *(const uint32_t*)(xa - 64);
    uint32_t o0, o1;
    float t0, t1, t2, t3;
    asm("v_lshlrev_b32 %2, 16, %10\n\t" "v_and_b32 %3, 0xffff0000, %10\n\t" "v_lshlrev_b32 %4, 16, %11\n\t" "v_and_b32 %5, 0xffff0000, %11\n\t"
        "v_mul_f32 %2, %2, %12\n\t" "v_mul_f32 %3, %3, %12\n\t" "v_mul_f32 %4, %4, %12\n\t" "v_mul_f32 %5, %5, %12\n\t"
        RB("%2") RB("%3") RB("%4") RB("%5")
        "v_cvt_pk_bf16_f32 %0, 0, %7\n\t" "v_cvt_pk_bf16_f32 %1, 0, %6\n\t"
        "v_add_f32 %2, %2, %0\n\t" "v_add_f32 %3, %3, %1\n\t"
        "v_cvt_pk_bf16_f32 %0, 0, %9\n\t" "v_cvt_pk_bf16_f32 %1, 0, %8\n\t"
        "v_add_f32 %4, %4, %0\n\t" "v_add_f32 %5, %5, %1\n\t"
        "v_cvt_pk_bf16_f32 %0, %2, %3\n\t" "v_cvt_pk_bf16_f32 %1, %4, %5\n\t"
        : "=&v"(o0), "=&v"(o1), "=&v"(t0), "=&v"(t1), "=&v"(t2), "=&v"(t3)
        : "v"(a[0]), "v"(a[1]), "v"(a[2]), "v"(a[3]), "v"(x0), "v"(x1), "v"(D));
    *(uint32_t*)(dst) = o0;
    *(uint32_t*)(dst - 64) = o1;
}
__device__ __forceinline__ u32x4 row_read(const char* a0) {
    s16x4 v0 = __builtin_amdgcn_ds_read_tr16_b64_v4i16((__attribute__((address_space(3))) s16x4*)(a0));
    s16x4 v1 = __builtin_amdgcn_ds_read_tr16_b64_v4i16((__attribute__((address_space(3))) s16x4*)(a0 + 4 * CH_STRIDE));
    u32x2 p0 = __builtin_bit_cast(u32x2, v0), p1 = __builtin_bit_cast(u32x2, v1);
    return (u32x4){p0[0], p0[1], p1[0], p1[1]};
}
__device__ __forceinline__ void gate2(uint32_t y0, uint32_t y1, uint32_t q0, uint32_t q1, uint32_t& o0, uint32_t& o1) {
    float t0, t1, t2, t3;
    asm("v_lshlrev_b32 %2, 16, %6\n\t" "v_lshlrev_b32 %0, 16, %8\n\t" "v_and_b32 %3, 0xffff0000, %6\n\t" "v_and_b32 %1, 0xffff0000, %8\n\t"
        "v_mul_f32 %2, %2, %0\n\t" "v_mul_f32 %3, %3, %1\n\t"
        "v_lshlrev_b32 %4, 16, %7\n\t" "v_lshlrev_b32 %0, 16, %9\n\t" "v_and_b32 %5, 0xffff0000, %7\n\t" "v_and_b32 %1, 0xffff0000, %9\n\t"
        "v_mul_f32 %4, %4, %0\n\t" "v_mul_f32 %5, %5, %1\n\t"
        "v_cvt_pk_bf16_f32 %0, %2, %3\n\t" "v_cvt_pk_bf16_f32 %1, %4, %5\n\t"
        : "=&v"(o0), "=&v"(o1), "=&v"(t0), "=&v"(t1), "=&v"(t2), "=&v"(t3)
        : "v"(y0), "v"(y1), "v"(q0), "v"(q1));
}
__device__ __forceinline__ u32x4 gate(u32x4 y, u32x4 q) {
    uint32_t o0, o1, o2, o3;
    gate2(y[0], y[1], q[0], q[1], o0, o1);
    gate2(y[2], y[3], q[2], q[3], o2, o3);
    return (u32x4){o0, o1, o2, o3};
}
__device__ __forceinline__ void lds_barrier() {
    __builtin_amdgcn_fence(__ATOMIC_RELEASE, "workgroup", "local");
    __builtin_amdgcn_s_barrier();
    __builtin_amdgcn_fence(__ATOMIC_ACQUIRE, "workgroup", "local");
}
__device__ __forceinline__ void flag_set(lds_t p) {
    __builtin_amdgcn_fence(__ATOMIC_RELEASE, "workgroup", "local");
    *(lds_vu)p = 1u;
}
__device__ __forceinline__ void flag_wait(lds_t p) {
    while (__builtin_amdgcn_ballot_w64(*(lds_vu)p == 0u) != 0ull) __builtin_amdgcn_s_sleep(1);
    __builtin_amdgcn_fence(__ATOMIC_ACQUIRE, "workgroup", "local");
}

template <int T0, int NT>
__device__ __forceinline__ void stage(lds_t kaddr, lds_t xaddr, f32x4 (&acc)[8]) {
    constexpr int NK = 8 - T0, ND = NK + 2 * NT;
    bf16x8 ka[NK], xa[NT], kb[NK], xb[NT];
#pragma unroll
    for (int t = 0; t < NT; ++t) xa[t] = ldx(xaddr + 512 * (7 - T0 - t));
#pragma unroll
    for (int m = 0; m < NK; ++m) ka[m] = ldk(kaddr + 512 * m);
#pragma unroll 1
    for (int c0 = 0; c0 < 8; c0 += 2) {
        const lds_t kp = kaddr + 64 * c0, xp = xaddr + 64 * c0;
#pragma unroll
        for (int t = 0; t < NT; ++t) xb[t] = ldx(xp + 64 + 512 * (7 - T0 - t));
#pragma unroll
        for (int m = 0; m < NK; ++m) kb[m] = ldk(kp + 64 + 512 * m);
#pragma unroll
        for (int t = 0; t < NT; ++t)
#pragma unroll
            for (int m = 0; m < NK - t; ++m) MFMA(ka[m], xa[t], acc[T0 + t + m]);
#pragma unroll
        for (int i = 0; i < ND; ++i) { SG_MF(1); SG_DS(1); }
        SG_MF(NK);
        SB();
#pragma unroll
        for (int t = 0; t < NT; ++t) xa[t] = ldx(xp + 128 + 512 * (7 - T0 - t));
#pragma unroll
        for (int m = 0; m < NK; ++m) ka[m] = ldk(kp + 128 + 512 * m);
#pragma unroll
        for (int t = 0; t < NT; ++t)
#pragma unroll
            for (int m = 0; m < NK - t; ++m) MFMA(kb[m], xb[t], acc[T0 + t + m]);
#pragma unroll
        for (int i = 0; i < ND; ++i) { SG_MF(1); SG_DS(1); }
        SG_MF(NK);
        SB();
    }
}

__global__ __launch_bounds__(1024) void fused_kernel(
    const uint16_t* __restrict__ u, const uint16_t* __restrict__ logA, const uint16_t* __restrict__ Aim,
    const uint16_t* __restrict__ Cre, const uint16_t* __restrict__ Cim, const uint16_t* __restrict__ logdt,
    const uint16_t* __restrict__ Dsk, const uint16_t* __restrict__ qin, const uint16_t* __restrict__ kin,
    const uint16_t* __restrict__ vin, const uint16_t* __restrict__ shk, const uint16_t* __restrict__ ssmk,
    const uint16_t* __restrict__ sD, uint16_t* __restrict__ out)
{
    __shared__ __attribute__((aligned(16))) char lds[LDS_TOTAL];
    uint64_t pc0;
    asm volatile("s_getpc_b64 %0" : "=s"(pc0));
    uint32_t pf = 0;
    if (threadIdx.x < CODE_BYTES / 128) pf = GLD(uint32_t, (gc_t)(pc0 & ~63ull), threadIdx.x * 128u, 0);
    SB();
    RT(0, 0);
    STAMP(0, 0);
    STAMP(1, 0);
    const int b = blockIdx.x;
    const int xcd = b & 7, idx = b >> 3, part = idx >> 4, gi = idx & 15, grp = xcd * 16 + gi;
    const int lane = threadIdx.x & 63;
    const int w = __builtin_amdgcn_readfirstlane(threadIdx.x >> 6);
    const lds_t flags = (lds_t)lds + FLAG_OFF;
    const int g = lane >> 4, fr = lane & 15;
    {
        const u32x4 z = {0, 0, 0, 0};
        char* zp = w < 8 ? lds + w * CH_STRIDE + (lane < 4 ? XA_OFF + 4096 : XB_OFF + 4080 - 64) + 16 * lane : lds + FLAG_OFF + 16 * lane;
        if (lane < (w < 8 ? 9 : 18)) *(u32x4*)zp = z;
    }
    KEEPS(u); KEEPS(logA); KEEPS(Aim); KEEPS(Cre); KEEPS(Cim); KEEPS(logdt); KEEPS(Dsk);
    KEEPS(qin); KEEPS(kin); KEEPS(vin); KEEPS(shk); KEEPS(ssmk); KEEPS(sD); KEEPS(out);
    SB();
    lds_barrier();
    SB();
    if (w >= 12) {
        const int rw = w - 12;
        const int qq = (lane >> 2) & 3, pp = lane & 3;
        const char* rg = lds + qq * CH_STRIDE + KA_OFF + 464 + 3584 + (64 * rw + 16 * g + 4 * pp) * 2;
        const uint32_t r0 = 255 - (64 * rw + lane);
        uint32_t oo = r0 * (4 * DCH) + part * (2 * DCH) + grp * 16;
        OPAQUE(oo);
#define ROW_OUT(T) (*(__attribute__((address_space(1))) u32x4*)((gw_t)out + (uint32_t)(oo + (T) * (256u * 4 * DCH))))
#define ROW_WAIT(S) flag_wait(flags + 128 + 32 * (S) + 4 * (lane & 7))
        if (part) {
            uint32_t qo = r0 * (2 * DCH) + grp * 16;
            OPAQUE(qo);
            u32x4 qv[8];
#define LDQ(T) qv[T] = GLD(u32x4, (gc_t)qin, (uint32_t)(qo + (T) * (256u * 2 * DCH)), 0)
#define ROWB(T) ROW_OUT(T) = gate(row_read(rg - 512 * (T)), qv[T])
            flag_wait(flags + 4 * (lane & 3));
            LDQ(0); LDQ(1); LDQ(2);
            SB();
            STAMP(0, 1);
            ROW_WAIT(0); ROWB(0); SB(); LDQ(3); LDQ(4); SB();
            STAMP(0, 2);
            ROW_WAIT(1); ROWB(1); ROWB(2); SB(); LDQ(5); LDQ(6); LDQ(7); SB();
            STAMP(0, 3);
            ROW_WAIT(2); ROWB(3); ROWB(4); SB();
            STAMP(0, 4);
            ROW_WAIT(3); ROWB(5); ROWB(6); SB();
            STAMP(0, 5);
            ROW_WAIT(4); ROWB(7);
        } else {
            const int t = threadIdx.x - 768;
            const uint32_t po = (uint32_t)(32 * gi + (t >> 3)) * (2 * DCH) + 256 * xcd + 128 * (t & 1);
            const int sel = (t >> 1) & 3;
            const gc_t pb = (gc_t)(sel == 0 ? kin : sel == 1 ? vin : sel == 2 ? u : qin) + po;
            uint32_t pv[4];
#pragma unroll
            for (int i = 0; i < 4; ++i) {
                pv[i] = GLD(uint32_t, pb, (size_t)i * (512 * 2 * DCH), 0);
                SB();
                if (i >= 1) KEEP(pv[i - 1]);
                SB();
            }
#define ROWA(T) ROW_OUT(T) = row_read(rg - 512 * (T))
            STAMP(0, 1);
            ROW_WAIT(0); ROWA(0); SB();
            STAMP(0, 2);
            ROW_WAIT(1); ROWA(1); ROWA(2); SB();
            STAMP(0, 3);
            ROW_WAIT(2); ROWA(3); ROWA(4); SB();
            STAMP(0, 4);
            ROW_WAIT(3); ROWA(5); ROWA(6); SB();
            STAMP(0, 5);
            ROW_WAIT(4); ROWA(7);
            KEEP(pv[3]);
        }
        STAMP(1, 6);
        STAMPW(1, 7);
        RT(0, 1);
        return;
    }
    if (w >= 8) {
        const int pw = w - 8, c2 = lane & 3, rq = lane >> 2;
        const int s0 = 64 * pw + 4 * rq;
        uint32_t eo = (uint32_t)(s0 + 2) * (2 * DCH) + grp * 16 + 4 * c2;
        OPAQUE(eo);
        char* xp = lds + 2 * c2 * CH_STRIDE + 2 * (2044 - s0);
#define BLK(o, P) ((uint32_t)((o) + (P) * (256u * 2 * DCH)))
        if (part) {
            uint32_t kk[8][4], vv[8][4], hk[8][3];
            uint32_t ho = (uint32_t)(64 * pw + 16 * g - 2) * (2 * DCH) + grp * 16 + 4 * c2;
            OPAQUE(ho);
            const bool hz = (pw | g) == 0;
            const uint32_t ho0 = hz ? ho + 4 * (2 * DCH) : ho;
#define LDK(P) do { _Pragma("unroll") for (int i = 0; i < 4; ++i) kk[P][i] = GLD(uint32_t, (gc_t)kin, BLK(eo, P), (i - 2) * (2 * DCH)); } while (0)
#define LDV(P) do { _Pragma("unroll") for (int i = 0; i < 4; ++i) vv[P][i] = GLD(uint32_t, (gc_t)vin, BLK(eo, P), (i - 2) * (2 * DCH)); } while (0)
#define LDH(P) do { _Pragma("unroll") for (int i = 0; i < 3; ++i) hk[P][i] = GLD(uint32_t, (gc_t)kin, ((P) ? BLK(ho, P) : ho0), (i - 1) * (2 * DCH)); } while (0)
            const u32x4 wv = GLD(u32x4, (gc_t)shk, (uint32_t)(grp * 8 + 2 * c2) * 8, 0);
#pragma unroll
            for (int p = 0; p < 3; ++p) { LDK(p); LDH(p); LDV(p); SB(); }
            const float wt[2][4] = {{lo_f(wv[0]), hi_f(wv[0]), lo_f(wv[1]), hi_f(wv[1])}, {lo_f(wv[2]), hi_f(wv[2]), lo_f(wv[3]), hi_f(wv[3])}};
#pragma unroll
            for (int i = 0; i < 3; ++i) hk[0][i] = hz ? 0u : hk[0][i];
            STAMP(0, 1);
#pragma unroll
            for (int p = 0; p < 8; ++p) {
                uint32_t kp[7];
#pragma unroll
                for (int i = 0; i < 3; ++i)
                    kp[i] = (uint32_t)__builtin_amdgcn_update_dpp((int)hk[p][i], (int)kk[p][1 + i], 0x114  , 0xf, 0xf, false);
#pragma unroll
                for (int i = 0; i < 4; ++i) kp[3 + i] = kk[p][i];
#pragma unroll
                for (int h = 0; h < 2; ++h) {
                    float kf[7], vf[4];
#pragma unroll
                    for (int i = 0; i < 7; ++i) kf[i] = h ? hi_f(kp[i]) : lo_f(kp[i]);
#pragma unroll
                    for (int i = 0; i < 4; ++i) vf[i] = h ? hi_f(vv[p][i]) : lo_f(vv[p][i]);
                    uint32_t o0, o1;
                    kv4(kf, wt[h], vf, o0, o1);
                    put_x4(xp + h * CH_STRIDE - 512 * p, o0, o1);
                    SB();
                    if (h == 0 && p + 3 < 8) { LDK(p + 3); LDH(p + 3); }
                    SB();
                }
                flag_set(flags + 16 * p + 4 * pw);
                SB();
                if (p + 3 < 8) LDV(p + 3);
                SB();
                if (p < 6) STAMP(0, 2 + p); else STAMP(1, p - 5);
            }
        } else {
            uint32_t uu[8][4];
#pragma unroll
            for (int p = 0; p < 8; ++p) {
#pragma unroll
                for (int i = 0; i < 4; ++i) uu[p][i] = GLD(uint32_t, (gc_t)u, BLK(eo, p), (i - 2) * (2 * DCH));
                SB();
            }
            STAMP(0, 1);
#pragma unroll
            for (int p = 0; p < 8; ++p) {
#pragma unroll
                for (int h = 0; h < 2; ++h) {
                    const uint32_t sel = h ? 0x07060302u : 0x05040100u;
                    put_x4(xp + h * CH_STRIDE - 512 * p, __builtin_amdgcn_perm(uu[p][2], uu[p][3], sel),
                           __builtin_amdgcn_perm(uu[p][0], uu[p][1], sel));
                }
                flag_set(flags + 16 * p + 4 * pw);
                SB();
                if (p < 6) STAMP(0, 2 + p); else STAMP(1, p - 5);
            }
        }
        RT(0, 1);
        return;
    }

    const int chl = grp * 8 + w;
    char* cb = lds + w * CH_STRIDE;
    u32x4 kq[4];
    if (part) {
        const uint16_t* ksrc = ssmk + (size_t)chl * LSEQ;
#pragma unroll
        for (int i = 0; i < 4; ++i) kq[i] = *(const u32x4*)(ksrc + 8 * (64 * i + lane));
        SB();
        KEEP(pf);
    } else {
        float dt = bf2f(logdt[chl]);
        uint32_t pr[8];
#pragma unroll
        for (int bt = 0; bt < 2; ++bt) {
            const int n = chl * NMODE + bt * 64 + lane;
            pr[4 * bt] = logA[n]; pr[4 * bt + 1] = Aim[n]; pr[4 * bt + 2] = Cre[n]; pr[4 * bt + 3] = Cim[n];
        }
        SB();
        dt = rbf(expf(dt));
        float pa[2], pb[2], pcr[2], pci[2];
        float mx = 0.f;
#pragma unroll
        for (int bt = 0; bt < 2; ++bt) {
            const float ar = -rbf(expf(lo_f(pr[4 * bt])));
            pa[bt] = (ar * dt) * 1.4426950408889634f;
            pb[bt] = (lo_f(pr[4 * bt + 1]) * dt) * 0.15915494309189535f;
            pcr[bt] = 2.f * (lo_f(pr[4 * bt + 2]) * dt);
            pci[bt] = 2.f * (lo_f(pr[4 * bt + 3]) * dt);
            mx = fmaxf(mx, fmaxf(fabsf(pcr[bt]), fabsf(pci[bt])));
        }
        KEEP(pf);
        int mi = (int)__float_as_uint(mx);
#define DPPMAX(ctrl) mi = max(mi, __builtin_amdgcn_update_dpp(0, mi, ctrl, 0xf, 0xf, false))
        DPPMAX(0x111); DPPMAX(0x112); DPPMAX(0x114); DPPMAX(0x118); DPPMAX(0x142); DPPMAX(0x143);
        int sf = 266 - ((__builtin_amdgcn_readlane(mi, 63) >> 23) & 0xff);
        sf = sf < 1 ? 1 : (sf > 239 ? 239 : sf);
        const float scl = __uint_as_float((uint32_t)sf << 23);
        const float inv = __uint_as_float((uint32_t)(240 - sf) << 23);
        STAMP(0, 1);

        char* Tl = cb + lane * 8;
        const char* Tr = cb + fr * KG_ROWB + g * 16;
#define TW(row, v0, v1) *(u32x2*)(Tl + (row) * KG_ROWB) = (u32x2){pk16(v0), pk16(v1)}
#define B2(X) do { _Pragma("unroll") for (int bt = 0; bt < 2; ++bt) { X; } } while (0)
        f16x8 bfr[8], af[8];
        f32x2 z8c[2], z1[2], z128[2];
        B2(z8c[bt] = anchor(pa[bt] * 8.f, pb[bt] * -8.f));
        B2(z1[bt] = anchor(pa[bt], pb[bt]));
        B2(z128[bt] = anchor(pa[bt] * 128.f, pb[bt] * 128.f));
        f32x2 f[2][16], h[2][8], cg[2][17], zs[2][3], zl[2][3], t[2][8];
        B2(t[bt][0] = cm1(z8c[bt], z8c[bt]); t[bt][1] = cm1(z128[bt], z128[bt]); t[bt][2] = cm1(z1[bt], z1[bt]));
        B2(cg[bt][0] = ((f32x2){pcr[bt] * scl, pci[bt] * scl}); f[bt][1] = z8c[bt] * 16384.f);
        SB();
        B2(zs[bt][0] = cm2(z8c[bt], z8c[bt], t[bt][0]); zl[bt][0] = cm2(z128[bt], z128[bt], t[bt][1]); h[bt][2] = cm2(z1[bt], z1[bt], t[bt][2]));
        B2(t[bt][3] = cm1(cg[bt][0], z128[bt]));
        SB();
        B2(t[bt][0] = cm1(zs[bt][0], zs[bt][0]); t[bt][1] = cm1(zl[bt][0], zl[bt][0]); t[bt][2] = cm1(h[bt][2], h[bt][2]);
           t[bt][4] = cm1(h[bt][2], z1[bt]); t[bt][5] = cm1(f[bt][1], zs[bt][0]); t[bt][6] = cm1(cg[bt][0], zl[bt][0]);
           cg[bt][1] = cm2(cg[bt][0], z128[bt], t[bt][3]); f[bt][2] = zs[bt][0] * 16384.f);
        SB();
        B2(zs[bt][1] = cm2(zs[bt][0], zs[bt][0], t[bt][0]); zl[bt][1] = cm2(zl[bt][0], zl[bt][0], t[bt][1]); h[bt][4] = cm2(h[bt][2], h[bt][2], t[bt][2]);
           h[bt][3] = cm2(h[bt][2], z1[bt], t[bt][4]); f[bt][3] = cm2(f[bt][1], zs[bt][0], t[bt][5]); cg[bt][2] = cm2(cg[bt][0], zl[bt][0], t[bt][6]);
           t[bt][3] = cm1(cg[bt][1], zl[bt][0]));
        SB();
        B2(t[bt][0] = cm1(zs[bt][1], zs[bt][1]); t[bt][1] = cm1(zl[bt][1], zl[bt][1]);
           t[bt][4] = cm1(f[bt][1], zs[bt][1]); t[bt][5] = cm1(f[bt][2], zs[bt][1]); t[bt][6] = cm1(f[bt][3], zs[bt][1]);
           cg[bt][3] = cm2(cg[bt][1], zl[bt][0], t[bt][3]); f[bt][4] = zs[bt][1] * 16384.f);
        SB();
        B2(zs[bt][2] = cm2(zs[bt][1], zs[bt][1], t[bt][0]); zl[bt][2] = cm2(zl[bt][1], zl[bt][1], t[bt][1]);
           f[bt][5] = cm2(f[bt][1], zs[bt][1], t[bt][4]); f[bt][6] = cm2(f[bt][2], zs[bt][1], t[bt][5]); f[bt][7] = cm2(f[bt][3], zs[bt][1], t[bt][6]));
        B2(f[bt][0] = ((f32x2){16384.f, 0.f}));
#pragma unroll
        for (int i = 0; i < 5; ++i) TW(i, f[0][i], f[1][i]);
        SB();
        B2(_Pragma("unroll") for (int i = 1; i < 8; ++i) t[bt][i] = cm1(f[bt][i], zs[bt][2]); f[bt][8] = zs[bt][2] * 16384.f);
#pragma unroll
        for (int i = 5; i < 8; ++i) TW(i, f[0][i], f[1][i]);
        SB();
        B2(_Pragma("unroll") for (int i = 1; i < 8; ++i) f[bt][8 + i] = cm2(f[bt][i], zs[bt][2], t[bt][i]));
        SB();
        B2(t[bt][0] = cm1(h[bt][4], z1[bt]); t[bt][1] = cm1(h[bt][4], h[bt][2]); t[bt][2] = cm1(h[bt][4], h[bt][3]));
#pragma unroll
        for (int i = 8; i < 16; ++i) TW(i, f[0][i], f[1][i]);
#pragma unroll
        for (int ks = 0; ks < 8; ++ks) bfr[ks] = *(const f16x8*)(Tr + ks * 64);
        B2(h[bt][1] = z1[bt]; h[bt][5] = cm2(h[bt][4], z1[bt], t[bt][0]); h[bt][6] = cm2(h[bt][4], h[bt][2], t[bt][1]); h[bt][7] = cm2(h[bt][4], h[bt][3], t[bt][2]));
        SB();
        f32x4 ac[3][2];
#define TRD() do { _Pragma("unroll") for (int ks = 0; ks < 8; ++ks) af[ks] = *(const f16x8*)(Tr + ks * 64); SB(); } while (0)
#define TMM(rt) do { \
            ac[rt][0] = (f32x4){0, 0, 0, 0}; ac[rt][1] = (f32x4){0, 0, 0, 0}; \
            _Pragma("unroll") for (int ks = 0; ks < 8; ++ks) \
                ac[rt][ks & 1] = __builtin_amdgcn_mfma_f32_16x16x32_f16(af[ks], bfr[ks], ac[rt][ks & 1], 0, 0, 0); SB(); } while (0)
#pragma unroll
        for (int aa = 0; aa < 2; ++aa) {
            TW(8 * aa, cg[0][aa], cg[1][aa]);
#pragma unroll
            for (int e0 = 1; e0 < 8; e0 += 4) {
                B2(_Pragma("unroll") for (int e = e0; e < e0 + 4 && e < 8; ++e) t[bt][e] = cm1(cg[bt][aa], h[bt][e]));
                SB();
                B2(_Pragma("unroll") for (int e = e0; e < e0 + 4 && e < 8; ++e) t[bt][e] = cm2(cg[bt][aa], h[bt][e], t[bt][e]));
                SB();
#pragma unroll
                for (int e = e0; e < e0 + 4 && e < 8; ++e) TW(8 * aa + e, t[0][e], t[1][e]);
                SB();
            }
        }
        TRD();
        uint16_t* KD = (uint16_t*)(lds + KD_OFF + w * 1536);
#define KDW(rt) do { \
            const f32x4 v = (ac[rt][0] + ac[rt][1]) * inv; \
            _Pragma("unroll") for (int r = 0; r < 4; r += 2) { \
                uint32_t p = pk_bf16(v[r], v[r + 1]); \
                KD[(16 * (rt) + 4 * g + r) * 16 + fr] = (uint16_t)p; \
                KD[(16 * (rt) + 4 * g + r + 1) * 16 + fr] = (uint16_t)(p >> 16); } } while (0)
#pragma unroll
        for (int aa = 2; aa < 4; ++aa) {
            B2(_Pragma("unroll") for (int e = 1; e < 4; ++e) t[bt][e] = cm1(cg[bt][aa], h[bt][2 * e]));
            TW(4 * (aa - 2), cg[0][aa], cg[1][aa]);
            SB();
            B2(_Pragma("unroll") for (int e = 1; e < 4; ++e) t[bt][e] = cm2(cg[bt][aa], h[bt][2 * e], t[bt][e]));
            SB();
#pragma unroll
            for (int e = 1; e < 4; ++e) TW(4 * (aa - 2) + e, t[0][e], t[1][e]);
            SB();
        }
        TMM(0);
        B2(_Pragma("unroll") for (int i = 0; i < 4; ++i) { cg[bt][4 + i] = cm1(cg[bt][i], zl[bt][1]); cg[bt][8 + i] = cm1(cg[bt][i], zl[bt][2]); });
        SB();
        B2(_Pragma("unroll") for (int i = 0; i < 4; ++i) { cg[bt][4 + i] = cm2(cg[bt][i], zl[bt][1], cg[bt][4 + i]); cg[bt][8 + i] = cm2(cg[bt][i], zl[bt][2], cg[bt][8 + i]); });
        KDW(0);
        SB();
        B2(_Pragma("unroll") for (int i = 0; i < 4; ++i) t[bt][i] = cm1(cg[bt][4 + i], h[bt][4]); cg[bt][16] = cm1(cg[bt][8], zl[bt][2]));
#pragma unroll
        for (int i = 0; i < 4; ++i) TW(8 + 2 * i, cg[0][4 + i], cg[1][4 + i]);
        SB();
        B2(_Pragma("unroll") for (int i = 0; i < 4; ++i) t[bt][i] = cm2(cg[bt][4 + i], h[bt][4], t[bt][i]); cg[bt][16] = cm2(cg[bt][8], zl[bt][2], cg[bt][16]));
        SB();
#pragma unroll
        for (int i = 0; i < 4; ++i) TW(9 + 2 * i, t[0][i], t[1][i]);
        SB();
        TRD();
        B2(_Pragma("unroll") for (int i = 0; i < 4; ++i) cg[bt][12 + i] = cm1(cg[bt][8 + i], zl[bt][1]));
        TW(8, cg[0][16], cg[1][16]);
        SB();
        B2(_Pragma("unroll") for (int i = 0; i < 4; ++i) cg[bt][12 + i] = cm2(cg[bt][8 + i], zl[bt][1], cg[bt][12 + i]));
#pragma unroll
        for (int aa = 8; aa < 12; ++aa) TW(aa - 8, cg[0][aa], cg[1][aa]);
        SB();
#pragma unroll
        for (int aa = 12; aa < 16; ++aa) TW(aa - 8, cg[0][aa], cg[1][aa]);
        SB();
        TMM(1);
        TRD();
        KDW(1);
        SB();
        TMM(2);
        STAMP(0, 2);
        KDW(2);
        {
            int tid = threadIdx.x;
            asm volatile("" : "+v"(tid));
            const int lane = tid & 63, g = lane >> 4, fr = lane & 15;
            const int hi = lane >> 5, last = (fr == 15);
            const uint16_t* p0 = KD + (hi ? 16 + 4 * (g - 2) : 8 * g) * 16 + fr;
            const uint16_t* p1 = p0 - 16 * hi, * p2 = p0 - 32 * hi, * p3 = p0 - 48 * hi;
            const uint16_t* p7 = p0 + (hi ? (last ? 49 : 1) : 112);
            kq[0][0] = (uint32_t)p0[0] | ((uint32_t)p1[16] << 16);
            kq[0][1] = (uint32_t)p1[32] | ((uint32_t)p1[48] << 16);
            kq[0][2] = (uint32_t)p2[64] | ((uint32_t)p3[80] << 16);
            kq[0][3] = (uint32_t)p3[96] | ((uint32_t)p7[0] << 16);
            const uint16_t* q0 = KD + (24 + 2 * g) * 16 + fr;
            const uint32_t a = q0[0], bq = q0[16], c = q0[last ? 17 : 1];
            kq[1][0] = __builtin_amdgcn_perm(a, a, 0x01000100u);
            kq[1][1] = __builtin_amdgcn_perm(bq, a, 0x05040100u);
            kq[1][2] = __builtin_amdgcn_perm(bq, bq, 0x01000100u);
            kq[1][3] = __builtin_amdgcn_perm(c, c, 0x01000100u);
            const uint32_t sel = (lane & 1) ? 0x05040504u : 0x05040100u;
#pragma unroll
            for (int i = 2; i < 4; ++i) {
                const uint32_t e0 = KD[512 + 64 * (i - 2) + lane], e1 = KD[513 + 64 * (i - 2) + lane];
                kq[i][0] = kq[i][1] = __builtin_amdgcn_perm(e0, e0, 0x01000100u);
                kq[i][2] = __builtin_amdgcn_perm(e1, e0, sel);
                kq[i][3] = __builtin_amdgcn_perm(e1, e1, 0x01000100u);
            }
        }
    }
    const float Dv = bf2f((part ? sD : Dsk)[chl]);
    int tid2 = threadIdx.x;
    asm volatile("" : "+v"(tid2));
#define lane (tid2 & 63)
#define g ((tid2 >> 4) & 3)
#define fr (tid2 & 15)
    {
        const u32x4 z = {0, 0, 0, 0};
        if (lane < 29) {
            *(u32x4*)(cb + KA_OFF + 16 * lane) = z;
            *(u32x4*)(cb + KB_OFF + 16 * lane) = z;
        }
        uint32_t c = 0;
#pragma unroll
        for (int i = 0; i < 4; ++i) {
            put_k(cb, 64 * i + lane, kq[i], prev_lane(kq[i][3], c));
            c = __builtin_amdgcn_readlane(kq[i][3], 63);
        }
    }
    STAMP(0, 3);

    const lds_t lb = (lds_t)lds + w * CH_STRIDE;
    const lds_t kaddr = lb + ((fr & 1) ? KA_OFF : KB_OFF) + 2 * (32 * (fr >> 1) + 8 * g + 8);
    lds_t xaddr = lb + ((fr & 1) ? (XA_OFF + 2 * (1822 - 2 * fr + 8 * g)) : (XB_OFF + 2 * (1820 - 2 * fr + 8 * g))) - 3584;
    asm volatile("" : "+v"(xaddr));
    const int lo_ = 64 * g + 2 * fr;
    const char* xa = cb + XA_OFF + 2 * (2046 - lo_);
    char* sg = cb + KA_OFF + 464 + 2 * (2046 - lo_);
#define EPI(T) epi_acc(acc[T], xa - 512 * (T), sg - 512 * (T), Dv)
#define SFLAG(S) flag_set(flags + 128 + 32 * (S) + 4 * w)

    f32x4 acc[8];
#pragma unroll
    for (int i = 0; i < 8; ++i) acc[i] = (f32x4){0, 0, 0, 0};

    flag_wait(flags + 4 * (lane & 3));
    STAMP(0, 4);
    stage<0, 1>(kaddr, xaddr, acc);
    STAMP(0, 5);
    EPI(0); SFLAG(0);
    flag_wait(flags + 16 + 4 * (lane & 7));
    STAMP(0, 6);
    stage<1, 2>(kaddr, xaddr, acc);
    STAMP(0, 7);
    EPI(1); EPI(2); SFLAG(1);
    flag_wait(flags + 48 + 4 * (lane & 7));
    STAMP(1, 1);
    stage<3, 2>(kaddr, xaddr, acc);
    STAMP(1, 2);
    EPI(3); EPI(4); SFLAG(2);
    flag_wait(flags + 80 + 4 * (lane & 7));
    STAMP(1, 3);
    stage<5, 2>(kaddr, xaddr, acc);
    STAMP(1, 4);
    EPI(5); EPI(6); SFLAG(3);
    flag_wait(flags + 112 + 4 * (lane & 3));
    STAMP(1, 5);
    {
        f32x4 a7b = {0, 0, 0, 0};
#pragma unroll
        for (int c = 0; c < 8; c += 2) {
            MFMA(ldk(kaddr + 64 * c), ldx(xaddr + 64 * c), acc[7]);
            MFMA(ldk(kaddr + 64 * c + 64), ldx(xaddr + 64 * c + 64), a7b);
        }
#pragma unroll
        for (int i = 0; i < 4; ++i) {
            float s = acc[7][i] + a7b[i];
            asm volatile("" : "+v"(s));
            acc[7][i] = s;
        }
    }
    EPI(7); SFLAG(4);
    STAMP(1, 6);
    RT(0, 1);
}

extern "C" void kernel_launch(void* const* d_in, const int* in_sizes, int n_in,
                              void* d_out, int out_size, void* d_ws, size_t ws_size,
                              hipStream_t stream) {
    fused_kernel<<<256, 1024, 0, stream>>>(
        (const uint16_t*)d_in[0], (const uint16_t*)d_in[1], (const uint16_t*)d_in[2], (const uint16_t*)d_in[3],
        (const uint16_t*)d_in[4], (const uint16_t*)d_in[5], (const uint16_t*)d_in[6], (const uint16_t*)d_in[7],
        (const uint16_t*)d_in[8], (const uint16_t*)d_in[9], (const uint16_t*)d_in[10], (const uint16_t*)d_in[11],
        (const uint16_t*)d_in[12], (uint16_t*)d_out);
}
```

```cpp
#include <hip/hip_runtime.h>
#include <stdint.h>

typedef __attribute__((ext_vector_type(4))) unsigned int u32x4;
typedef __attribute__((ext_vector_type(2))) unsigned int u32x2;
typedef __attribute__((ext_vector_type(4))) float f32x4;
typedef __attribute__((ext_vector_type(2))) float f32x2;
typedef __attribute__((ext_vector_type(8))) _Float16 f16x8;
typedef __attribute__((ext_vector_type(2))) _Float16 f16x2;
typedef __attribute__((ext_vector_type(8))) __bf16 bf16x8;
typedef __attribute__((ext_vector_type(2))) __bf16 bf16x2;
typedef __attribute__((ext_vector_type(4))) short s16x4;

#define STAMP(k, i)
#define STAMPW(k, i)
#define RT(k, i)
#define STDECL
#define LSEQ 2048
#define DCH 1024
#define NMODE 128

__device__ __forceinline__ float bf2f(uint16_t v) { return __uint_as_float(((uint32_t)v) << 16); }
__device__ __forceinline__ float lo_f(uint32_t w) { return __uint_as_float(w << 16); }
__device__ __forceinline__ float hi_f(uint32_t w) { return __uint_as_float(w & 0xffff0000u); }
__device__ __forceinline__ uint32_t pk_bf16(float lo, float hi) {
    f32x2 x = {lo, hi};
    bf16x2 y = __builtin_convertvector(x, bf16x2);
    return __builtin_bit_cast(uint32_t, y);
}
__device__ __forceinline__ uint32_t pk_f16(float lo, float hi) {
    f32x2 x = {lo, hi};
    f16x2 y = __builtin_convertvector(x, f16x2);
    return __builtin_bit_cast(uint32_t, y);
}
__device__ __forceinline__ float rbf(float a) { return lo_f(pk_bf16(a, a)); }

#define KEEP(v) asm volatile("" :: "v"(v))
#define KEEPS(v) asm volatile("" :: "s"(v))
#define OPAQUE(v) asm volatile("" : "+v"(v))
#define SB() __builtin_amdgcn_sched_barrier(0)
__device__ __forceinline__ f32x2 cm1(f32x2 a, f32x2 b) {
    f32x2 t;
    asm("v_pk_mul_f32 %0, %1, %2 op_sel:[0,0] op_sel_hi:[0,1]" : "=v"(t) : "v"(a), "v"(b));
    return t;
}
__device__ __forceinline__ f32x2 cm2(f32x2 a, f32x2 b, f32x2 t) {
    f32x2 c;
    asm("v_pk_fma_f32 %0, %1, %2, %3 op_sel:[1,1,0] op_sel_hi:[1,0,1] neg_lo:[1,0,0]" : "=v"(c) : "v"(a), "v"(b), "v"(t));
    return c;
}
__device__ __forceinline__ f32x2 cmul(f32x2 a, f32x2 b) { return cm2(a, b, cm1(a, b)); }
__device__ __forceinline__ f32x2 anchor(float a, float b) {
    const float e = __builtin_amdgcn_exp2f(a);
    const float th = __builtin_amdgcn_fractf(b);
    return (f32x2){e * __builtin_amdgcn_cosf(th), e * __builtin_amdgcn_sinf(th)};
}
__device__ __forceinline__ uint32_t pk16(f32x2 c) { return pk_f16(c[0], c[1]); }

#define KG_ROWB 528

#define KA_OFF 0
#define KB_OFF 4640
#define XA_OFF 9216
#define XB_OFF 13440
#define CH_STRIDE 17696
#define KD_OFF (8 * CH_STRIDE)
#define FLAG_OFF (KD_OFF + 8 * 1536)
#define LDS_TOTAL (FLAG_OFF + 304)
#define CODE_BYTES 19072

__device__ __forceinline__ void put_k(char* cb, int y, u32x4 d, uint32_t prev) {
    *(u32x4*)(cb + KA_OFF + 464 + 16 * y) = d;
    u32x4 o;
    o[0] = __builtin_amdgcn_alignbyte(d[0], prev, 2);
    o[1] = __builtin_amdgcn_alignbyte(d[1], d[0], 2);
    o[2] = __builtin_amdgcn_alignbyte(d[2], d[1], 2);
    o[3] = __builtin_amdgcn_alignbyte(d[3], d[2], 2);
    *(u32x4*)(cb + KB_OFF + 464 + 16 * y) = o;
}
__device__ __forceinline__ void put_x4(char* p, uint32_t o0, uint32_t o1) {
    *(u32x2*)(p + XA_OFF) = (u32x2){o0, o1};
    *(uint32_t*)(p + XB_OFF - 4) = o0;
    *(uint32_t*)(p + XB_OFF) = o1;
}
__device__ __forceinline__ uint32_t prev_lane(uint32_t v, uint32_t carry) {
    return (uint32_t)__builtin_amdgcn_update_dpp((int)carry, (int)v, 0x138  , 0xf, 0xf, false);
}
typedef __attribute__((address_space(3))) char* lds_t;
typedef __attribute__((address_space(3))) const volatile u32x2* lds_v2;
typedef __attribute__((address_space(3))) volatile uint32_t* lds_vu;
typedef __attribute__((address_space(3))) const bf16x8* lds_k8;
typedef __attribute__((address_space(1))) const char* gc_t;
typedef __attribute__((address_space(1))) char* gw_t;
#define GLD(T, base, voff, imm) (*(__attribute__((address_space(1))) const T*)((base) + (voff) + (imm)))
__device__ __forceinline__ bf16x8 ldx(lds_t p) {
    u32x2 a = *(lds_v2)p, b = *(lds_v2)(p + 8);
    u32x4 v = {a[0], a[1], b[0], b[1]};
    return __builtin_bit_cast(bf16x8, v);
}
__device__ __forceinline__ bf16x8 ldk(lds_t p) { return *(lds_k8)p; }
#define MFMA(a, b, c) c = __builtin_amdgcn_mfma_f32_16x16x32_bf16(a, b, c, 0, 0, 0)
#define SG_DS(n) __builtin_amdgcn_sched_group_barrier(0x100, n, 0)
#define SG_MF(n) __builtin_amdgcn_sched_group_barrier(0x008, n, 0)

#define RB(r) "v_cvt_pk_bf16_f32 " r ", 0, " r "\n\t"
__device__ __forceinline__ void kv4(const float (&k)[7], const float (&wt)[4], const float (&v)[4], uint32_t& o0, uint32_t& o1) {
    float a0, a1;
    asm("v_mul_f32 %2, %4, %14\n\t" "v_mul_f32 %3, %5, %14\n\t" "v_mul_f32 %0, %6, %14\n\t" "v_mul_f32 %1, %7, %14\n\t"
        "v_fmac_f32 %2, %5, %13\n\t" "v_fmac_f32 %3, %6, %13\n\t" "v_fmac_f32 %0, %7, %13\n\t" "v_fmac_f32 %1, %8, %13\n\t"
        "v_fmac_f32 %2, %6, %12\n\t" "v_fmac_f32 %3, %7, %12\n\t" "v_fmac_f32 %0, %8, %12\n\t" "v_fmac_f32 %1, %9, %12\n\t"
        "v_fmac_f32 %2, %7, %11\n\t" "v_fmac_f32 %3, %8, %11\n\t" "v_fmac_f32 %0, %9, %11\n\t" "v_fmac_f32 %1, %10, %11\n\t"
        RB("%2") RB("%3") RB("%0") RB("%1")
        "v_mul_f32 %2, %2, %15\n\t" "v_mul_f32 %3, %3, %16\n\t" "v_mul_f32 %0, %0, %17\n\t" "v_mul_f32 %1, %1, %18\n\t"
        "v_cvt_pk_bf16_f32 %0, %1, %0\n\t" "v_cvt_pk_bf16_f32 %1, %3, %2\n\t"
        : "=&v"(o0), "=&v"(o1), "=&v"(a0), "=&v"(a1)
        : "v"(k[0]), "v"(k[1]), "v"(k[2]), "v"(k[3]), "v"(k[4]), "v"(k[5]), "v"(k[6]),
          "v"(wt[0]), "v"(wt[1]), "v"(wt[2]), "v"(wt[3]), "v"(v[0]), "v"(v[1]), "v"(v[2]), "v"(v[3]));
}
__device__ __forceinline__ void epi_acc(const f32x4 a, char* dst) {
    uint32_t o0, o1;
    asm("v_cvt_pk_bf16_f32 %0, %3, %2\n\tv_cvt_pk_bf16_f32 %1, %5, %4" : "=&v"(o0), "=v"(o1) : "v"(a[0]), "v"(a[1]), "v"(a[2]), "v"(a[3]));
    *(uint32_t*)(dst) = o0;
    *(uint32_t*)(dst - 64) = o1;
}
__device__ __forceinline__ u32x4 row_read(const char* a0) {
    s16x4 v0 = __builtin_amdgcn_ds_read_tr16_b64_v4i16((__attribute__((address_space(3))) s16x4*)(a0));
    s16x4 v1 = __builtin_amdgcn_ds_read_tr16_b64_v4i16((__attribute__((address_space(3))) s16x4*)(a0 + 4 * CH_STRIDE));
    u32x2 p0 = __builtin_bit_cast(u32x2, v0), p1 = __builtin_bit_cast(u32x2, v1);
    return (u32x4){p0[0], p0[1], p1[0], p1[1]};
}
__device__ __forceinline__ void gate2(uint32_t y0, uint32_t y1, uint32_t q0, uint32_t q1, uint32_t& o0, uint32_t& o1) {
    float t0, t1, t2, t3;
    asm("v_lshlrev_b32 %2, 16, %6\n\t" "v_lshlrev_b32 %0, 16, %8\n\t" "v_and_b32 %3, 0xffff0000, %6\n\t" "v_and_b32 %1, 0xffff0000, %8\n\t"
        "v_mul_f32 %2, %2, %0\n\t" "v_mul_f32 %3, %3, %1\n\t"
        "v_lshlrev_b32 %4, 16, %7\n\t" "v_lshlrev_b32 %0, 16, %9\n\t" "v_and_b32 %5, 0xffff0000, %7\n\t" "v_and_b32 %1, 0xffff0000, %9\n\t"
        "v_mul_f32 %4, %4, %0\n\t" "v_mul_f32 %5, %5, %1\n\t"
        "v_cvt_pk_bf16_f32 %0, %2, %3\n\t" "v_cvt_pk_bf16_f32 %1, %4, %5\n\t"
        : "=&v"(o0), "=&v"(o1), "=&v"(t0), "=&v"(t1), "=&v"(t2), "=&v"(t3)
        : "v"(y0), "v"(y1), "v"(q0), "v"(q1));
}
__device__ __forceinline__ u32x4 gate(u32x4 y, u32x4 q) {
    uint32_t o0, o1, o2, o3;
    gate2(y[0], y[1], q[0], q[1], o0, o1);
    gate2(y[2], y[3], q[2], q[3], o2, o3);
    return (u32x4){o0, o1, o2, o3};
}
__device__ __forceinline__ void lds_barrier() {
    __builtin_amdgcn_fence(__ATOMIC_RELEASE, "workgroup", "local");
    __builtin_amdgcn_s_barrier();
    __builtin_amdgcn_fence(__ATOMIC_ACQUIRE, "workgroup", "local");
}
__device__ __forceinline__ void flag_set(lds_t p) {
    __builtin_amdgcn_fence(__ATOMIC_RELEASE, "workgroup", "local");
    *(lds_vu)p = 1u;
}
__device__ __forceinline__ void flag_wait(lds_t p) {
    while (__builtin_amdgcn_ballot_w64(*(lds_vu)p == 0u) != 0ull) __builtin_amdgcn_s_sleep(1);
    __builtin_amdgcn_fence(__ATOMIC_ACQUIRE, "workgroup", "local");
}

template <int T0, int NT>
__device__ __forceinline__ void stage(lds_t kaddr, lds_t xaddr, f32x4 (&acc)[8]) {
    constexpr int NK = 8 - T0, ND = NK + 2 * NT;
    bf16x8 ka[NK], xa[NT], kb[NK], xb[NT];
#pragma unroll
    for (int t = 0; t < NT; ++t) xa[t] = ldx(xaddr + 512 * (7 - T0 - t));
#pragma unroll
    for (int m = 0; m < NK; ++m) ka[m] = ldk(kaddr + 512 * m);
#pragma unroll 1
    for (int c0 = 0; c0 < 8; c0 += 2) {
        const lds_t kp = kaddr + 64 * c0, xp = xaddr + 64 * c0;
#pragma unroll
        for (int t = 0; t < NT; ++t) xb[t] = ldx(xp + 64 + 512 * (7 - T0 - t));
#pragma unroll
        for (int m = 0; m < NK; ++m) kb[m] = ldk(kp + 64 + 512 * m);
#pragma unroll
        for (int t = 0; t < NT; ++t)
#pragma unroll
            for (int m = 0; m < NK - t; ++m) MFMA(ka[m], xa[t], acc[T0 + t + m]);
#pragma unroll
        for (int i = 0; i < ND; ++i) { SG_MF(1); SG_DS(1); }
        SG_MF(NK);
        SB();
#pragma unroll
        for (int t = 0; t < NT; ++t) xa[t] = ldx(xp + 128 + 512 * (7 - T0 - t));
#pragma unroll
        for (int m = 0; m < NK; ++m) ka[m] = ldk(kp + 128 + 512 * m);
#pragma unroll
        for (int t = 0; t < NT; ++t)
#pragma unroll
            for (int m = 0; m < NK - t; ++m) MFMA(kb[m], xb[t], acc[T0 + t + m]);
#pragma unroll
        for (int i = 0; i < ND; ++i) { SG_MF(1); SG_DS(1); }
        SG_MF(NK);
        SB();
    }
}

__global__ __launch_bounds__(1024) void fused_kernel(
    const uint16_t* __restrict__ u, const uint16_t* __restrict__ logA, const uint16_t* __restrict__ Aim,
    const uint16_t* __restrict__ Cre, const uint16_t* __restrict__ Cim, const uint16_t* __restrict__ logdt,
    const uint16_t* __restrict__ Dsk, const uint16_t* __restrict__ qin, const uint16_t* __restrict__ kin,
    const uint16_t* __restrict__ vin, const uint16_t* __restrict__ shk, const uint16_t* __restrict__ ssmk,
    const uint16_t* __restrict__ sD, uint16_t* __restrict__ out)
{
    __shared__ __attribute__((aligned(16))) char lds[LDS_TOTAL];
    uint64_t pc0;
    asm volatile("s_getpc_b64 %0" : "=s"(pc0));
    uint32_t pf = 0;
    if (threadIdx.x < CODE_BYTES / 128) pf = GLD(uint32_t, (gc_t)(pc0 & ~63ull), threadIdx.x * 128u, 0);
    SB();
    STDECL;
    RT(0, 0);
    STAMP(0, 0);
    STAMP(1, 0);
    const int b = blockIdx.x;
    const int xcd = b & 7, idx = b >> 3, part = idx >> 4, gi = idx & 15, grp = xcd * 16 + gi;
    const int lane = threadIdx.x & 63;
    const int w = __builtin_amdgcn_readfirstlane(threadIdx.x >> 6);
    const lds_t flags = (lds_t)lds + FLAG_OFF;
    const int g = lane >> 4, fr = lane & 15;
    {
        const u32x4 z = {0, 0, 0, 0};
        char* zp = w < 8 ? lds + w * CH_STRIDE + (lane < 4 ? XA_OFF + 4096 : XB_OFF + 4080 - 64) + 16 * lane : lds + FLAG_OFF + 16 * lane;
        if (lane < (w < 8 ? 9 : 19)) *(u32x4*)zp = z;
    }
    KEEPS(u); KEEPS(logA); KEEPS(Aim); KEEPS(Cre); KEEPS(Cim); KEEPS(logdt); KEEPS(Dsk);
    KEEPS(qin); KEEPS(kin); KEEPS(vin); KEEPS(shk); KEEPS(ssmk); KEEPS(sD); KEEPS(out);
    SB();
    lds_barrier();
    SB();
    if (w >= 12) {
        const int rw = w - 12;
        const int qq = (lane >> 2) & 3, pp = lane & 3;
        const char* rg = lds + qq * CH_STRIDE + KA_OFF + 464 + 3584 + (64 * rw + 16 * g + 4 * pp) * 2;
        const uint32_t r0 = 255 - (64 * rw + lane);
        uint32_t oo = r0 * (4 * DCH) + part * (2 * DCH) + grp * 16;
        OPAQUE(oo);
#define ROW_OUT(T) (*(__attribute__((address_space(1))) u32x4*)((gw_t)out + (uint32_t)(oo + (T) * (256u * 4 * DCH))))
#define ROW_WAIT(S) flag_wait(flags + 128 + 32 * (S) + 4 * (lane & 7))
        if (part) {
            uint32_t qo = r0 * (2 * DCH) + grp * 16;
            OPAQUE(qo);
            u32x4 qv[8];
#define LDQ(T) qv[T] = GLD(u32x4, (gc_t)qin, (uint32_t)(qo + (T) * (256u * 2 * DCH)), 0)
#define ROWB(T) ROW_OUT(T) = gate(row_read(rg - 512 * (T)), qv[T])
            flag_wait(flags + 4 * (lane & 3));
            LDQ(0); LDQ(1); LDQ(2);
            SB();
            STAMP(0, 1);
            ROW_WAIT(0); ROWB(0); SB(); LDQ(3); LDQ(4); SB();
            STAMP(0, 2);
            ROW_WAIT(1); ROWB(1); ROWB(2); SB(); LDQ(5); LDQ(6); LDQ(7); SB();
            STAMP(0, 3);
            ROW_WAIT(2); ROWB(3); ROWB(4); SB();
            STAMP(0, 4);
            ROW_WAIT(3); ROWB(5); ROWB(6); SB();
            STAMP(0, 5);
            ROW_WAIT(4); ROWB(7);
        } else {
            const uint32_t t = gi * 64 + lane;
            const gc_t pkv = (gc_t)((t & 2) ? vin : kin) + ((t >> 2) * (2 * DCH) + 256 * xcd + 128 * (t & 1));
            const uint32_t ouq = (t >> 1) * (2 * DCH) + 256 * xcd + 128 * (t & 1);
            const gc_t pu = (gc_t)u + ouq, pq = (gc_t)qin + ouq;
            const gc_t a1 = rw == 0 ? pu : rw == 3 ? pq : pkv + (size_t)(rw + 3) * (256 * 2 * DCH);
            const gc_t a2 = rw < 2 ? pkv + (size_t)(rw + 6) * (256 * 2 * DCH) : pu + (size_t)(rw - 1) * (512 * 2 * DCH);
            const gc_t a3 = rw == 0 ? pu + (size_t)3 * (512 * 2 * DCH) : pq + (size_t)rw * (512 * 2 * DCH);
            uint32_t pv = GLD(uint32_t, pkv, (size_t)rw * (256 * 2 * DCH), 0);
            SB(); KEEP(pv); SB();
            pv = GLD(uint32_t, a1, 0, 0);
            SB(); KEEP(pv); SB();
            if (rw == 0) flag_set(flags + 288);
            pv = GLD(uint32_t, a2, 0, 0);
            SB(); KEEP(pv); SB();
            if (rw >= 2) flag_set(flags + 288 + 4 * (rw - 1));
            pv = GLD(uint32_t, a3, 0, 0);
            SB(); KEEP(pv); SB();
            if (rw == 0) flag_set(flags + 300);
#define ROWA(T) ROW_OUT(T) = row_read(rg - 512 * (T))
            STAMP(0, 1);
            ROW_WAIT(0); ROWA(0); SB();
            STAMP(0, 2);
            ROW_WAIT(1); ROWA(1); ROWA(2); SB();
            STAMP(0, 3);
            ROW_WAIT(2); ROWA(3); ROWA(4); SB();
            STAMP(0, 4);
            ROW_WAIT(3); ROWA(5); ROWA(6); SB();
            STAMP(0, 5);
            ROW_WAIT(4); ROWA(7);
        }
        STAMP(1, 6);
        STAMPW(1, 7);
        RT(0, 1);
        return;
    }
    if (w >= 8) {
        const int pw = w - 8, c2 = lane & 3, rq = lane >> 2;
        const int s0 = 64 * pw + 4 * rq;
        uint32_t eo = (uint32_t)(s0 + 2) * (2 * DCH) + grp * 16 + 4 * c2;
        OPAQUE(eo);
        char* xp = lds + 2 * c2 * CH_STRIDE + 2 * (2044 - s0);
#define BLK(o, P) ((uint32_t)((o) + (P) * (256u * 2 * DCH)))
        if (part) {
            uint32_t kk[8][4], vv[8][4], hk[8][3];
            uint32_t ho = (uint32_t)(64 * pw + 16 * g - 2) * (2 * DCH) + grp * 16 + 4 * c2;
            OPAQUE(ho);
            const bool hz = (pw | g) == 0;
            const uint32_t ho0 = hz ? ho + 4 * (2 * DCH) : ho;
#define LDK(P) do { _Pragma("unroll") for (int i = 0; i < 4; ++i) kk[P][i] = GLD(uint32_t, (gc_t)kin, BLK(eo, P), (i - 2) * (2 * DCH)); } while (0)
#define LDV(P) do { _Pragma("unroll") for (int i = 0; i < 4; ++i) vv[P][i] = GLD(uint32_t, (gc_t)vin, BLK(eo, P), (i - 2) * (2 * DCH)); } while (0)
#define LDH(P) do { _Pragma("unroll") for (int i = 0; i < 3; ++i) hk[P][i] = GLD(uint32_t, (gc_t)kin, ((P) ? BLK(ho, P) : ho0), (i - 1) * (2 * DCH)); } while (0)
            const u32x4 wv = GLD(u32x4, (gc_t)shk, (uint32_t)(grp * 8 + 2 * c2) * 8, 0);
#pragma unroll
            for (int p = 0; p < 2; ++p) { LDK(p); LDH(p); LDV(p); SB(); }
            const float wt[2][4] = {{lo_f(wv[0]), hi_f(wv[0]), lo_f(wv[1]), hi_f(wv[1])}, {lo_f(wv[2]), hi_f(wv[2]), lo_f(wv[3]), hi_f(wv[3])}};
#pragma unroll
            for (int i = 0; i < 3; ++i) hk[0][i] = hz ? 0u : hk[0][i];
            STAMP(0, 1);
#pragma unroll
            for (int p = 0; p < 8; ++p) {
                uint32_t kp[7];
#pragma unroll
                for (int i = 0; i < 3; ++i)
                    kp[i] = (uint32_t)__builtin_amdgcn_update_dpp((int)hk[p][i], (int)kk[p][1 + i], 0x114  , 0xf, 0xf, false);
#pragma unroll
                for (int i = 0; i < 4; ++i) kp[3 + i] = kk[p][i];
                if (p == 0) { SB(); LDK(2); LDH(2); LDV(2); SB(); }
#pragma unroll
                for (int h = 0; h < 2; ++h) {
                    float kf[7], vf[4];
#pragma unroll
                    for (int i = 0; i < 7; ++i) kf[i] = h ? hi_f(kp[i]) : lo_f(kp[i]);
#pragma unroll
                    for (int i = 0; i < 4; ++i) vf[i] = h ? hi_f(vv[p][i]) : lo_f(vv[p][i]);
                    uint32_t o0, o1;
                    kv4(kf, wt[h], vf, o0, o1);
                    put_x4(xp + h * CH_STRIDE - 512 * p, o0, o1);
                    SB();
                    if (h == 0 && p + 3 < 8) { LDK(p + 3); LDH(p + 3); }
                    SB();
                }
                flag_set(flags + 16 * p + 4 * pw);
                SB();
                if (p + 3 < 8) LDV(p + 3);
                SB();
                if (p < 6) STAMP(0, 2 + p); else STAMP(1, p - 5);
            }
        } else {
            uint32_t uu[8][4];
#define LDU(P) do { _Pragma("unroll") for (int i = 0; i < 4; ++i) uu[P][i] = GLD(uint32_t, (gc_t)u, BLK(eo, P), (i - 2) * (2 * DCH)); SB(); } while (0)
#define PUTU(P) do { _Pragma("unroll") for (int h = 0; h < 2; ++h) { \
                    const uint32_t sel = h ? 0x07060302u : 0x05040100u; \
                    put_x4(xp + h * CH_STRIDE - 512 * (P), __builtin_amdgcn_perm(uu[P][2], uu[P][3], sel), __builtin_amdgcn_perm(uu[P][0], uu[P][1], sel)); } \
                flag_set(flags + 16 * (P) + 4 * pw); SB(); \
                if ((P) < 6) STAMP(0, 2 + (P)); else STAMP(1, (P) - 5); } while (0)
            flag_wait(flags + 288);
            STAMP(0, 1);
            LDU(0); LDU(1);
            PUTU(0); PUTU(1);
            flag_wait(flags + 292 + 4 * (lane & 1));
            LDU(2); LDU(3); LDU(4); LDU(5);
            PUTU(2); PUTU(3);
            flag_wait(flags + 300);
            LDU(6); LDU(7);
            PUTU(4); PUTU(5); PUTU(6); PUTU(7);
        }
        RT(0, 1);
        return;
    }

    const int chl = grp * 8 + w;
    char* cb = lds + w * CH_STRIDE;
    u32x4 kq[4];
    if (part) {
        const uint16_t* ksrc = ssmk + (size_t)chl * LSEQ;
#pragma unroll
        for (int i = 0; i < 4; ++i) kq[i] = *(const u32x4*)(ksrc + 8 * (64 * i + lane));
        uint32_t dr = sD[chl];
        SB();
        KEEP(pf);
        OPAQUE(dr);
        kq[0][0] = pk_bf16(lo_f(kq[0][0]) + (lane ? 0.f : lo_f(dr)), hi_f(kq[0][0]));
    } else {
        float dt = bf2f(logdt[chl]);
        uint32_t pr[8];
#pragma unroll
        for (int bt = 0; bt < 2; ++bt) {
            const int n = chl * NMODE + bt * 64 + lane;
            pr[4 * bt] = logA[n]; pr[4 * bt + 1] = Aim[n]; pr[4 * bt + 2] = Cre[n]; pr[4 * bt + 3] = Cim[n];
        }
        uint32_t dr = Dsk[chl];
        SB();
        dt = rbf(expf(dt));
        float pa[2], pb[2], pcr[2], pci[2];
        float mx = 0.f;
#pragma unroll
        for (int bt = 0; bt < 2; ++bt) {
            const float ar = -rbf(expf(lo_f(pr[4 * bt])));
            pa[bt] = (ar * dt) * 1.4426950408889634f;
            pb[bt] = (lo_f(pr[4 * bt + 1]) * dt) * 0.15915494309189535f;
            pcr[bt] = 2.f * (lo_f(pr[4 * bt + 2]) * dt);
            pci[bt] = 2.f * (lo_f(pr[4 * bt + 3]) * dt);
            mx = fmaxf(mx, fmaxf(fabsf(pcr[bt]), fabsf(pci[bt])));
        }
        KEEP(pf);
        OPAQUE(dr);
        const float Dv = lane ? 0.f : lo_f(dr);
        int mi = (int)__float_as_uint(mx);
#define DPPMAX(ctrl) mi = max(mi, __builtin_amdgcn_update_dpp(0, mi, ctrl, 0xf, 0xf, false))
        DPPMAX(0x111); DPPMAX(0x112); DPPMAX(0x114); DPPMAX(0x118); DPPMAX(0x142); DPPMAX(0x143);
        int sf = 266 - ((__builtin_amdgcn_readlane(mi, 63) >> 23) & 0xff);
        sf = sf < 1 ? 1 : (sf > 239 ? 239 : sf);
        const float scl = __uint_as_float((uint32_t)sf << 23);
        const float inv = __uint_as_float((uint32_t)(240 - sf) << 23);
        STAMP(0, 1);

        char* Tl = cb + lane * 8;
        const char* Tr = cb + fr * KG_ROWB + g * 16;
#define TW(row, v0, v1) *(u32x2*)(Tl + (row) * KG_ROWB) = (u32x2){pk16(v0), pk16(v1)}
#define B2(X) do { _Pragma("unroll") for (int bt = 0; bt < 2; ++bt) { X; } } while (0)
        f16x8 bfr[8], af[8];
        f32x2 z8c[2], z1[2], z128[2];
        B2(z8c[bt] = anchor(pa[bt] * 8.f, pb[bt] * -8.f));
        B2(z1[bt] = anchor(pa[bt], pb[bt]));
        B2(z128[bt] = anchor(pa[bt] * 128.f, pb[bt] * 128.f));
        f32x2 f[2][16], h[2][8], cg[2][17], zs[2][3], zl[2][3], t[2][8];
        B2(t[bt][0] = cm1(z8c[bt], z8c[bt]); t[bt][1] = cm1(z128[bt], z128[bt]); t[bt][2] = cm1(z1[bt], z1[bt]));
        B2(cg[bt][0] = ((f32x2){pcr[bt] * scl, pci[bt] * scl}); f[bt][1] = z8c[bt] * 16384.f);
        SB();
        B2(zs[bt][0] = cm2(z8c[bt], z8c[bt], t[bt][0]); zl[bt][0] = cm2(z128[bt], z128[bt], t[bt][1]); h[bt][2] = cm2(z1[bt], z1[bt], t[bt][2]));
        B2(t[bt][3] = cm1(cg[bt][0], z128[bt]));
        SB();
        B2(t[bt][0] = cm1(zs[bt][0], zs[bt][0]); t[bt][1] = cm1(zl[bt][0], zl[bt][0]); t[bt][2] = cm1(h[bt][2], h[bt][2]);
           t[bt][4] = cm1(h[bt][2], z1[bt]); t[bt][5] = cm1(f[bt][1], zs[bt][0]); t[bt][6] = cm1(cg[bt][0], zl[bt][0]);
           cg[bt][1] = cm2(cg[bt][0], z128[bt], t[bt][3]); f[bt][2] = zs[bt][0] * 16384.f);
        SB();
        B2(zs[bt][1] = cm2(zs[bt][0], zs[bt][0], t[bt][0]); zl[bt][1] = cm2(zl[bt][0], zl[bt][0], t[bt][1]); h[bt][4] = cm2(h[bt][2], h[bt][2], t[bt][2]);
           h[bt][3] = cm2(h[bt][2], z1[bt], t[bt][4]); f[bt][3] = cm2(f[bt][1], zs[bt][0], t[bt][5]); cg[bt][2] = cm2(cg[bt][0], zl[bt][0], t[bt][6]);
           t[bt][3] = cm1(cg[bt][1], zl[bt][0]));
        SB();
        B2(t[bt][0] = cm1(zs[bt][1], zs[bt][1]); t[bt][1] = cm1(zl[bt][1], zl[bt][1]);
           t[bt][4] = cm1(f[bt][1], zs[bt][1]); t[bt][5] = cm1(f[bt][2], zs[bt][1]); t[bt][6] = cm1(f[bt][3], zs[bt][1]);
           cg[bt][3] = cm2(cg[bt][1], zl[bt][0], t[bt][3]); f[bt][4] = zs[bt][1] * 16384.f);
        SB();
        B2(zs[bt][2] = cm2(zs[bt][1], zs[bt][1], t[bt][0]); zl[bt][2] = cm2(zl[bt][1], zl[bt][1], t[bt][1]);
           f[bt][5] = cm2(f[bt][1], zs[bt][1], t[bt][4]); f[bt][6] = cm2(f[bt][2], zs[bt][1], t[bt][5]); f[bt][7] = cm2(f[bt][3], zs[bt][1], t[bt][6]));
        B2(f[bt][0] = ((f32x2){16384.f, 0.f}));
#pragma unroll
        for (int i = 0; i < 5; ++i) TW(i, f[0][i], f[1][i]);
        SB();
        B2(_Pragma("unroll") for (int i = 1; i < 8; ++i) t[bt][i] = cm1(f[bt][i], zs[bt][2]); f[bt][8] = zs[bt][2] * 16384.f);
#pragma unroll
        for (int i = 5; i < 8; ++i) TW(i, f[0][i], f[1][i]);
        SB();
        B2(_Pragma("unroll") for (int i = 1; i < 8; ++i) f[bt][8 + i] = cm2(f[bt][i], zs[bt][2], t[bt][i]));
        SB();
        B2(t[bt][0] = cm1(h[bt][4], z1[bt]); t[bt][1] = cm1(h[bt][4], h[bt][2]); t[bt][2] = cm1(h[bt][4], h[bt][3]));
#pragma unroll
        for (int i = 8; i < 16; ++i) TW(i, f[0][i], f[1][i]);
#pragma unroll
        for (int ks = 0; ks < 8; ++ks) bfr[ks] = *(const f16x8*)(Tr + ks * 64);
        B2(h[bt][1] = z1[bt]; h[bt][5] = cm2(h[bt][4], z1[bt], t[bt][0]); h[bt][6] = cm2(h[bt][4], h[bt][2], t[bt][1]); h[bt][7] = cm2(h[bt][4], h[bt][3], t[bt][2]));
        SB();
        f32x4 ac[3][2];
#define TRD() do { _Pragma("unroll") for (int ks = 0; ks < 8; ++ks) af[ks] = *(const f16x8*)(Tr + ks * 64); SB(); } while (0)
#define TMM(rt) do { \
            ac[rt][0] = (f32x4){0, 0, 0, 0}; ac[rt][1] = (f32x4){0, 0, 0, 0}; \
            _Pragma("unroll") for (int ks = 0; ks < 8; ++ks) \
                ac[rt][ks & 1] = __builtin_amdgcn_mfma_f32_16x16x32_f16(af[ks], bfr[ks], ac[rt][ks & 1], 0, 0, 0); SB(); } while (0)
#pragma unroll
        for (int aa = 0; aa < 2; ++aa) {
            TW(8 * aa, cg[0][aa], cg[1][aa]);
#pragma unroll
            for (int e0 = 1; e0 < 8; e0 += 4) {
                B2(_Pragma("unroll") for (int e = e0; e < e0 + 4 && e < 8; ++e) t[bt][e] = cm1(cg[bt][aa], h[bt][e]));
                SB();
                B2(_Pragma("unroll") for (int e = e0; e < e0 + 4 && e < 8; ++e) t[bt][e] = cm2(cg[bt][aa], h[bt][e], t[bt][e]));
                SB();
#pragma unroll
                for (int e = e0; e < e0 + 4 && e < 8; ++e) TW(8 * aa + e, t[0][e], t[1][e]);
                SB();
            }
        }
        TRD();
        uint16_t* KD = (uint16_t*)(lds + KD_OFF + w * 1536);
#define KDW(rt) do { \
            f32x4 v = (ac[rt][0] + ac[rt][1]) * inv; \
            if ((rt) == 0) v[0] += Dv; \
            _Pragma("unroll") for (int r = 0; r < 4; r += 2) { \
                uint32_t p = pk_bf16(v[r], v[r + 1]); \
                KD[(16 * (rt) + 4 * g + r) * 16 + fr] = (uint16_t)p; \
                KD[(16 * (rt) + 4 * g + r + 1) * 16 + fr] = (uint16_t)(p >> 16); } } while (0)
#pragma unroll
        for (int aa = 2; aa < 4; ++aa) {
            B2(_Pragma("unroll") for (int e = 1; e < 4; ++e) t[bt][e] = cm1(cg[bt][aa], h[bt][2 * e]));
            TW(4 * (aa - 2), cg[0][aa], cg[1][aa]);
            SB();
            B2(_Pragma("unroll") for (int e = 1; e < 4; ++e) t[bt][e] = cm2(cg[bt][aa], h[bt][2 * e], t[bt][e]));
            SB();
#pragma unroll
            for (int e = 1; e < 4; ++e) TW(4 * (aa - 2) + e, t[0][e], t[1][e]);
            SB();
        }
        TMM(0);
        B2(_Pragma("unroll") for (int i = 0; i < 4; ++i) { cg[bt][4 + i] = cm1(cg[bt][i], zl[bt][1]); cg[bt][8 + i] = cm1(cg[bt][i], zl[bt][2]); });
        SB();
        B2(_Pragma("unroll") for (int i = 0; i < 4; ++i) { cg[bt][4 + i] = cm2(cg[bt][i], zl[bt][1], cg[bt][4 + i]); cg[bt][8 + i] = cm2(cg[bt][i], zl[bt][2], cg[bt][8 + i]); });
        KDW(0);
        SB();
        B2(_Pragma("unroll") for (int i = 0; i < 4; ++i) t[bt][i] = cm1(cg[bt][4 + i], h[bt][4]); cg[bt][16] = cm1(cg[bt][8], zl[bt][2]));
#pragma unroll
        for (int i = 0; i < 4; ++i) TW(8 + 2 * i, cg[0][4 + i], cg[1][4 + i]);
        SB();
        B2(_Pragma("unroll") for (int i = 0; i < 4; ++i) t[bt][i] = cm2(cg[bt][4 + i], h[bt][4], t[bt][i]); cg[bt][16] = cm2(cg[bt][8], zl[bt][2], cg[bt][16]));
        SB();
#pragma unroll
        for (int i = 0; i < 4; ++i) TW(9 + 2 * i, t[0][i], t[1][i]);
        SB();
        TRD();
        B2(_Pragma("unroll") for (int i = 0; i < 4; ++i) cg[bt][12 + i] = cm1(cg[bt][8 + i], zl[bt][1]));
        TW(8, cg[0][16], cg[1][16]);
        SB();
        B2(_Pragma("unroll") for (int i = 0; i < 4; ++i) cg[bt][12 + i] = cm2(cg[bt][8 + i], zl[bt][1], cg[bt][12 + i]));
#pragma unroll
        for (int aa = 8; aa < 12; ++aa) TW(aa - 8, cg[0][aa], cg[1][aa]);
        SB();
#pragma unroll
        for (int aa = 12; aa < 16; ++aa) TW(aa - 8, cg[0][aa], cg[1][aa]);
        SB();
        TMM(1);
        TRD();
        KDW(1);
        SB();
        TMM(2);
        STAMP(0, 2);
        KDW(2);
        {
            int tid = threadIdx.x;
            asm volatile("" : "+v"(tid));
            const int lane = tid & 63, g = lane >> 4, fr = lane & 15;
            const int hi = lane >> 5, last = (fr == 15);
            const uint16_t* p0 = KD + (hi ? 16 + 4 * (g - 2) : 8 * g) * 16 + fr;
            const uint16_t* p1 = p0 - 16 * hi, * p2 = p0 - 32 * hi, * p3 = p0 - 48 * hi;
            const uint16_t* p7 = p0 + (hi ? (last ? 49 : 1) : 112);
            kq[0][0] = (uint32_t)p0[0] | ((uint32_t)p1[16] << 16);
            kq[0][1] = (uint32_t)p1[32] | ((uint32_t)p1[48] << 16);
            kq[0][2] = (uint32_t)p2[64] | ((uint32_t)p3[80] << 16);
            kq[0][3] = (uint32_t)p3[96] | ((uint32_t)p7[0] << 16);
            const uint16_t* q0 = KD + (24 + 2 * g) * 16 + fr;
            const uint32_t a = q0[0], bq = q0[16], c = q0[last ? 17 : 1];
            kq[1][0] = __builtin_amdgcn_perm(a, a, 0x01000100u);
            kq[1][1] = __builtin_amdgcn_perm(bq, a, 0x05040100u);
            kq[1][2] = __builtin_amdgcn_perm(bq, bq, 0x01000100u);
            kq[1][3] = __builtin_amdgcn_perm(c, c, 0x01000100u);
            const uint32_t sel = (lane & 1) ? 0x05040504u : 0x05040100u;
#pragma unroll
            for (int i = 2; i < 4; ++i) {
                const uint32_t e0 = KD[512 + 64 * (i - 2) + lane], e1 = KD[513 + 64 * (i - 2) + lane];
                kq[i][0] = kq[i][1] = __builtin_amdgcn_perm(e0, e0, 0x01000100u);
                kq[i][2] = __builtin_amdgcn_perm(e1, e0, sel);
                kq[i][3] = __builtin_amdgcn_perm(e1, e1, 0x01000100u);
            }
        }
    }
    int tid2 = threadIdx.x;
    asm volatile("" : "+v"(tid2));
#define lane (tid2 & 63)
#define g ((tid2 >> 4) & 3)
#define fr (tid2 & 15)
    {
        const u32x4 z = {0, 0, 0, 0};
        if (lane < 29) {
            *(u32x4*)(cb + KA_OFF + 16 * lane) = z;
            *(u32x4*)(cb + KB_OFF + 16 * lane) = z;
        }
        uint32_t c = 0;
#pragma unroll
        for (int i = 0; i < 4; ++i) {
            put_k(cb, 64 * i + lane, kq[i], prev_lane(kq[i][3], c));
            c = __builtin_amdgcn_readlane(kq[i][3], 63);
        }
    }
    STAMP(0, 3);

    const lds_t lb = (lds_t)lds + w * CH_STRIDE;
    const lds_t kaddr = lb + ((fr & 1) ? KA_OFF : KB_OFF) + 2 * (32 * (fr >> 1) + 8 * g + 8);
    lds_t xaddr = lb + ((fr & 1) ? (XA_OFF + 2 * (1822 - 2 * fr + 8 * g)) : (XB_OFF + 2 * (1820 - 2 * fr + 8 * g))) - 3584;
    asm volatile("" : "+v"(xaddr));
    const int lo_ = 64 * g + 2 * fr;
    char* sg = cb + KA_OFF + 464 + 2 * (2046 - lo_);
#define EPI(T) epi_acc(acc[T], sg - 512 * (T))
#define SFLAG(S) flag_set(flags + 128 + 32 * (S) + 4 * w)

    f32x4 acc[8];
#pragma unroll
    for (int i = 0; i < 8; ++i) acc[i] = (f32x4){0, 0, 0, 0};

    flag_wait(flags + 4 * (lane & 3));
    STAMP(0, 4);
    stage<0, 1>(kaddr, xaddr, acc);
    STAMP(0, 5);
    EPI(0); SFLAG(0);
    flag_wait(flags + 16 + 4 * (lane & 7));
    STAMP(0, 6);
    stage<1, 2>(kaddr, xaddr, acc);
    STAMP(0, 7);
    EPI(1); EPI(2); SFLAG(1);
    flag_wait(flags + 48 + 4 * (lane & 7));
    STAMP(1, 1);
    stage<3, 2>(kaddr, xaddr, acc);
    STAMP(1, 2);
    EPI(3); EPI(4); SFLAG(2);
    flag_wait(flags + 80 + 4 * (lane & 7));
    STAMP(1, 3);
    stage<5, 2>(kaddr, xaddr, acc);
    STAMP(1, 4);
    EPI(5); EPI(6); SFLAG(3);
    flag_wait(flags + 112 + 4 * (lane & 3));
    STAMP(1, 5);
    {
        f32x4 a7b = {0, 0, 0, 0};
#pragma unroll
        for (int c = 0; c < 8; c += 2) {
            MFMA(ldk(kaddr + 64 * c), ldx(xaddr + 64 * c), acc[7]);
            MFMA(ldk(kaddr + 64 * c + 64), ldx(xaddr + 64 * c + 64), a7b);
        }
#pragma unroll
        for (int i = 0; i < 4; ++i) {
            float s = acc[7][i] + a7b[i];
            asm volatile("" : "+v"(s));
            acc[7][i] = s;
        }
    }
    EPI(7); SFLAG(4);
    STAMP(1, 6);
    RT(0, 1);
}

extern "C" void kernel_launch(void* const* d_in, const int* in_sizes, int n_in,
                              void* d_out, int out_size, void* d_ws, size_t ws_size,
                              hipStream_t stream) {
    fused_kernel<<<256, 1024, 0, stream>>>(
        (const uint16_t*)d_in[0], (const uint16_t*)d_in[1], (const uint16_t*)d_in[2], (const uint16_t*)d_in[3],
        (const uint16_t*)d_in[4], (const uint16_t*)d_in[5], (const uint16_t*)d_in[6], (const uint16_t*)d_in[7],
        (const uint16_t*)d_in[8], (const uint16_t*)d_in[9], (const uint16_t*)d_in[10], (const uint16_t*)d_in[11],
        (const uint16_t*)d_in[12], (uint16_t*)d_out);
}
```

```cpp
#include <hip/hip_runtime.h>
#include <stdint.h>

typedef __attribute__((ext_vector_type(4))) unsigned int u32x4;
typedef __attribute__((ext_vector_type(2))) unsigned int u32x2;
typedef __attribute__((ext_vector_type(4))) float f32x4;
typedef __attribute__((ext_vector_type(2))) float f32x2;
typedef __attribute__((ext_vector_type(8))) _Float16 f16x8;
typedef __attribute__((ext_vector_type(2))) _Float16 f16x2;
typedef __attribute__((ext_vector_type(8))) __bf16 bf16x8;
typedef __attribute__((ext_vector_type(2))) __bf16 bf16x2;
typedef __attribute__((ext_vector_type(4))) short s16x4;

#define STAMP(k, i)
#define STAMPW(k, i)
#define RT(k, i)
#define STDECL
#define LSEQ 2048
#define DCH 1024
#define NMODE 128

__device__ __forceinline__ float bf2f(uint16_t v) { return __uint_as_float(((uint32_t)v) << 16); }
__device__ __forceinline__ float lo_f(uint32_t w) { return __uint_as_float(w << 16); }
__device__ __forceinline__ float hi_f(uint32_t w) { return __uint_as_float(w & 0xffff0000u); }
__device__ __forceinline__ uint32_t pk_bf16(float lo, float hi) {
    f32x2 x = {lo, hi};
    bf16x2 y = __builtin_convertvector(x, bf16x2);
    return __builtin_bit_cast(uint32_t, y);
}
__device__ __forceinline__ uint32_t pk_f16(float lo, float hi) {
    f32x2 x = {lo, hi};
    f16x2 y = __builtin_convertvector(x, f16x2);
    return __builtin_bit_cast(uint32_t, y);
}
__device__ __forceinline__ float rbf(float a) { return lo_f(pk_bf16(a, a)); }

#define KEEP(v) asm volatile("" :: "v"(v))
#define KEEPS(v) asm volatile("" :: "s"(v))
#define OPAQUE(v) asm volatile("" : "+v"(v))
#define SB() __builtin_amdgcn_sched_barrier(0)
__device__ __forceinline__ f32x2 cm1(f32x2 a, f32x2 b) {
    f32x2 t;
    asm("v_pk_mul_f32 %0, %1, %2 op_sel:[0,0] op_sel_hi:[0,1]" : "=v"(t) : "v"(a), "v"(b));
    return t;
}
__device__ __forceinline__ f32x2 cm2(f32x2 a, f32x2 b, f32x2 t) {
    f32x2 c;
    asm("v_pk_fma_f32 %0, %1, %2, %3 op_sel:[1,1,0] op_sel_hi:[1,0,1] neg_lo:[1,0,0]" : "=v"(c) : "v"(a), "v"(b), "v"(t));
    return c;
}
__device__ __forceinline__ f32x2 cmul(f32x2 a, f32x2 b) { return cm2(a, b, cm1(a, b)); }
__device__ __forceinline__ f32x2 anchor(float a, float b) {
    const float e = __builtin_amdgcn_exp2f(a);
    const float th = __builtin_amdgcn_fractf(b);
    return (f32x2){e * __builtin_amdgcn_cosf(th), e * __builtin_amdgcn_sinf(th)};
}
__device__ __forceinline__ uint32_t pk16(f32x2 c) { return pk_f16(c[0], c[1]); }

#define KG_ROWB 528

#define D_PF 32
#define D_SSM 0
#define NUP 1
#define LATE 1
#define SEQ0 0xff0c06040100ull
#define SEQ1 0xff0d070902ull
#define SEQ2 0xff0e0b0508ull
#define SEQ3 0xff0f0a03ull
#define DL0 0
#define DL1 7
#define DL2 9
#define DL3 11
#define KA_OFF 0
#define KB_OFF 4640
#define XA_OFF 9216
#define XB_OFF 13440
#define CH_STRIDE 17696
#define KD_OFF (8 * CH_STRIDE)
#define FLAG_OFF (KD_OFF + 8 * 1536)
#define LDS_TOTAL (FLAG_OFF + 304)
#define CODE_BYTES 19072

__device__ __forceinline__ void put_k(char* cb, int y, u32x4 d, uint32_t prev) {
    *(u32x4*)(cb + KA_OFF + 464 + 16 * y) = d;
    u32x4 o;
    o[0] = __builtin_amdgcn_alignbyte(d[0], prev, 2);
    o[1] = __builtin_amdgcn_alignbyte(d[1], d[0], 2);
    o[2] = __builtin_amdgcn_alignbyte(d[2], d[1], 2);
    o[3] = __builtin_amdgcn_alignbyte(d[3], d[2], 2);
    *(u32x4*)(cb + KB_OFF + 464 + 16 * y) = o;
}
__device__ __forceinline__ void put_x4(char* p, uint32_t o0, uint32_t o1) {
    *(u32x2*)(p + XA_OFF) = (u32x2){o0, o1};
    *(uint32_t*)(p + XB_OFF - 4) = o0;
    *(uint32_t*)(p + XB_OFF) = o1;
}
__device__ __forceinline__ uint32_t prev_lane(uint32_t v, uint32_t carry) {
    return (uint32_t)__builtin_amdgcn_update_dpp((int)carry, (int)v, 0x138  , 0xf, 0xf, false);
}
typedef __attribute__((address_space(3))) char* lds_t;
typedef __attribute__((address_space(3))) const volatile u32x2* lds_v2;
typedef __attribute__((address_space(3))) volatile uint32_t* lds_vu;
typedef __attribute__((address_space(3))) const bf16x8* lds_k8;
typedef __attribute__((address_space(1))) const char* gc_t;
typedef __attribute__((address_space(1))) char* gw_t;
#define GLD(T, base, voff, imm) (*(__attribute__((address_space(1))) const T*)((base) + (voff) + (imm)))
__device__ __forceinline__ bf16x8 ldx(lds_t p) {
    u32x2 a = *(lds_v2)p, b = *(lds_v2)(p + 8);
    u32x4 v = {a[0], a[1], b[0], b[1]};
    return __builtin_bit_cast(bf16x8, v);
}
__device__ __forceinline__ bf16x8 ldk(lds_t p) { return *(lds_k8)p; }
#define MFMA(a, b, c) c = __builtin_amdgcn_mfma_f32_16x16x32_bf16(a, b, c, 0, 0, 0)
#define SG_DS(n) __builtin_amdgcn_sched_group_barrier(0x100, n, 0)
#define SG_MF(n) __builtin_amdgcn_sched_group_barrier(0x008, n, 0)

#define RB(r) "v_cvt_pk_bf16_f32 " r ", 0, " r "\n\t"
__device__ __forceinline__ void kv4(const float (&k)[7], const float (&wt)[4], const float (&v)[4], uint32_t& o0, uint32_t& o1) {
    float a0, a1;
    asm("v_mul_f32 %2, %4, %14\n\t" "v_mul_f32 %3, %5, %14\n\t" "v_mul_f32 %0, %6, %14\n\t" "v_mul_f32 %1, %7, %14\n\t"
        "v_fmac_f32 %2, %5, %13\n\t" "v_fmac_f32 %3, %6, %13\n\t" "v_fmac_f32 %0, %7, %13\n\t" "v_fmac_f32 %1, %8, %13\n\t"
        "v_fmac_f32 %2, %6, %12\n\t" "v_fmac_f32 %3, %7, %12\n\t" "v_fmac_f32 %0, %8, %12\n\t" "v_fmac_f32 %1, %9, %12\n\t"
        "v_fmac_f32 %2, %7, %11\n\t" "v_fmac_f32 %3, %8, %11\n\t" "v_fmac_f32 %0, %9, %11\n\t" "v_fmac_f32 %1, %10, %11\n\t"
        RB("%2") RB("%3") RB("%0") RB("%1")
        "v_mul_f32 %2, %2, %15\n\t" "v_mul_f32 %3, %3, %16\n\t" "v_mul_f32 %0, %0, %17\n\t" "v_mul_f32 %1, %1, %18\n\t"
        "v_cvt_pk_bf16_f32 %0, %1, %0\n\t" "v_cvt_pk_bf16_f32 %1, %3, %2\n\t"
        : "=&v"(o0), "=&v"(o1), "=&v"(a0), "=&v"(a1)
        : "v"(k[0]), "v"(k[1]), "v"(k[2]), "v"(k[3]), "v"(k[4]), "v"(k[5]), "v"(k[6]),
          "v"(wt[0]), "v"(wt[1]), "v"(wt[2]), "v"(wt[3]), "v"(v[0]), "v"(v[1]), "v"(v[2]), "v"(v[3]));
}
__device__ __forceinline__ void epi_acc(const f32x4 a, char* dst) {
    uint32_t o0, o1;
    asm("v_cvt_pk_bf16_f32 %0, %3, %2\n\tv_cvt_pk_bf16_f32 %1, %5, %4" : "=&v"(o0), "=v"(o1) : "v"(a[0]), "v"(a[1]), "v"(a[2]), "v"(a[3]));
    *(uint32_t*)(dst) = o0;
    *(uint32_t*)(dst - 64) = o1;
}
__device__ __forceinline__ u32x4 row_read(const char* a0) {
    s16x4 v0 = __builtin_amdgcn_ds_read_tr16_b64_v4i16((__attribute__((address_space(3))) s16x4*)(a0));
    s16x4 v1 = __builtin_amdgcn_ds_read_tr16_b64_v4i16((__attribute__((address_space(3))) s16x4*)(a0 + 4 * CH_STRIDE));
    u32x2 p0 = __builtin_bit_cast(u32x2, v0), p1 = __builtin_bit_cast(u32x2, v1);
    return (u32x4){p0[0], p0[1], p1[0], p1[1]};
}
__device__ __forceinline__ void gate2(uint32_t y0, uint32_t y1, uint32_t q0, uint32_t q1, uint32_t& o0, uint32_t& o1) {
    float t0, t1, t2, t3;
    asm("v_lshlrev_b32 %2, 16, %6\n\t" "v_lshlrev_b32 %0, 16, %8\n\t" "v_and_b32 %3, 0xffff0000, %6\n\t" "v_and_b32 %1, 0xffff0000, %8\n\t"
        "v_mul_f32 %2, %2, %0\n\t" "v_mul_f32 %3, %3, %1\n\t"
        "v_lshlrev_b32 %4, 16, %7\n\t" "v_lshlrev_b32 %0, 16, %9\n\t" "v_and_b32 %5, 0xffff0000, %7\n\t" "v_and_b32 %1, 0xffff0000, %9\n\t"
        "v_mul_f32 %4, %4, %0\n\t" "v_mul_f32 %5, %5, %1\n\t"
        "v_cvt_pk_bf16_f32 %0, %2, %3\n\t" "v_cvt_pk_bf16_f32 %1, %4, %5\n\t"
        : "=&v"(o0), "=&v"(o1), "=&v"(t0), "=&v"(t1), "=&v"(t2), "=&v"(t3)
        : "v"(y0), "v"(y1), "v"(q0), "v"(q1));
}
__device__ __forceinline__ u32x4 gate(u32x4 y, u32x4 q) {
    uint32_t o0, o1, o2, o3;
    gate2(y[0], y[1], q[0], q[1], o0, o1);
    gate2(y[2], y[3], q[2], q[3], o2, o3);
    return (u32x4){o0, o1, o2, o3};
}
__device__ __forceinline__ void lds_barrier() {
    __builtin_amdgcn_fence(__ATOMIC_RELEASE, "workgroup", "local");
    __builtin_amdgcn_s_barrier();
    __builtin_amdgcn_fence(__ATOMIC_ACQUIRE, "workgroup", "local");
}
__device__ __forceinline__ void flag_set(lds_t p) {
    __builtin_amdgcn_fence(__ATOMIC_RELEASE, "workgroup", "local");
    *(lds_vu)p = 1u;
}
__device__ __forceinline__ void flag_wait(lds_t p) {
    while (__builtin_amdgcn_ballot_w64(*(lds_vu)p == 0u) != 0ull) __builtin_amdgcn_s_sleep(1);
    __builtin_amdgcn_fence(__ATOMIC_ACQUIRE, "workgroup", "local");
}

template <int T0, int NT>
__device__ __forceinline__ void stage(lds_t kaddr, lds_t xaddr, f32x4 (&acc)[8]) {
    constexpr int NK = 8 - T0, ND = NK + 2 * NT;
    bf16x8 ka[NK], xa[NT], kb[NK], xb[NT];
#pragma unroll
    for (int t = 0; t < NT; ++t) xa[t] = ldx(xaddr + 512 * (7 - T0 - t));
#pragma unroll
    for (int m = 0; m < NK; ++m) ka[m] = ldk(kaddr + 512 * m);
#pragma unroll 1
    for (int c0 = 0; c0 < 8; c0 += 2) {
        const lds_t kp = kaddr + 64 * c0, xp = xaddr + 64 * c0;
#pragma unroll
        for (int t = 0; t < NT; ++t) xb[t] = ldx(xp + 64 + 512 * (7 - T0 - t));
#pragma unroll
        for (int m = 0; m < NK; ++m) kb[m] = ldk(kp + 64 + 512 * m);
#pragma unroll
        for (int t = 0; t < NT; ++t)
#pragma unroll
            for (int m = 0; m < NK - t; ++m) MFMA(ka[m], xa[t], acc[T0 + t + m]);
#pragma unroll
        for (int i = 0; i < ND; ++i) { SG_MF(1); SG_DS(1); }
        SG_MF(NK);
        SB();
#pragma unroll
        for (int t = 0; t < NT; ++t) xa[t] = ldx(xp + 128 + 512 * (7 - T0 - t));
#pragma unroll
        for (int m = 0; m < NK; ++m) ka[m] = ldk(kp + 128 + 512 * m);
#pragma unroll
        for (int t = 0; t < NT; ++t)
#pragma unroll
            for (int m = 0; m < NK - t; ++m) MFMA(kb[m], xb[t], acc[T0 + t + m]);
#pragma unroll
        for (int i = 0; i < ND; ++i) { SG_MF(1); SG_DS(1); }
        SG_MF(NK);
        SB();
    }
}

__global__ __launch_bounds__(1024) void fused_kernel(
    const uint16_t* __restrict__ u, const uint16_t* __restrict__ logA, const uint16_t* __restrict__ Aim,
    const uint16_t* __restrict__ Cre, const uint16_t* __restrict__ Cim, const uint16_t* __restrict__ logdt,
    const uint16_t* __restrict__ Dsk, const uint16_t* __restrict__ qin, const uint16_t* __restrict__ kin,
    const uint16_t* __restrict__ vin, const uint16_t* __restrict__ shk, const uint16_t* __restrict__ ssmk,
    const uint16_t* __restrict__ sD, uint16_t* __restrict__ out)
{
    __shared__ __attribute__((aligned(16))) char lds[LDS_TOTAL];
    uint64_t pc0;
    asm volatile("s_getpc_b64 %0" : "=s"(pc0));
    uint32_t pf = 0;
    if (threadIdx.x < CODE_BYTES / 128) pf = GLD(uint32_t, (gc_t)(pc0 & ~63ull), threadIdx.x * 128u, 0);
    SB();
    STDECL;
    RT(0, 0);
    STAMP(0, 0);
    STAMP(1, 0);
    const int b = blockIdx.x;
    const int xcd = b & 7, idx = b >> 3, part = idx >> 4, gi = idx & 15, grp = xcd * 16 + gi;
    const int lane = threadIdx.x & 63;
    const int w = __builtin_amdgcn_readfirstlane(threadIdx.x >> 6);
    const lds_t flags = (lds_t)lds + FLAG_OFF;
    const int g = lane >> 4, fr = lane & 15;
    {
        const u32x4 z = {0, 0, 0, 0};
        char* zp = w < 8 ? lds + w * CH_STRIDE + (lane < 4 ? XA_OFF + 4096 : XB_OFF + 4080 - 64) + 16 * lane : lds + FLAG_OFF + 16 * lane;
        if (lane < (w < 8 ? 9 : 19)) *(u32x4*)zp = z;
    }
    KEEPS(u); KEEPS(logA); KEEPS(Aim); KEEPS(Cre); KEEPS(Cim); KEEPS(logdt); KEEPS(Dsk);
    KEEPS(qin); KEEPS(kin); KEEPS(vin); KEEPS(shk); KEEPS(ssmk); KEEPS(sD); KEEPS(out);
    SB();
    lds_barrier();
    SB();
    if (w >= 12) {
        const int rw = w - 12;
        const int qq = (lane >> 2) & 3, pp = lane & 3;
        const char* rg = lds + qq * CH_STRIDE + KA_OFF + 464 + 3584 + (64 * rw + 16 * g + 4 * pp) * 2;
        const uint32_t r0 = 255 - (64 * rw + lane);
        uint32_t oo = r0 * (4 * DCH) + part * (2 * DCH) + grp * 16;
        OPAQUE(oo);
#define ROW_OUT(T) (*(__attribute__((address_space(1))) u32x4*)((gw_t)out + (uint32_t)(oo + (T) * (256u * 4 * DCH))))
#define ROW_WAIT(S) flag_wait(flags + 128 + 32 * (S) + 4 * (lane & 7))
        if (part) {
            uint32_t qo = r0 * (2 * DCH) + grp * 16;
            OPAQUE(qo);
            u32x4 qv[8];
#define LDQ(T) qv[T] = GLD(u32x4, (gc_t)qin, (uint32_t)(qo + (T) * (256u * 2 * DCH)), 0)
#define ROWB(T) ROW_OUT(T) = gate(row_read(rg - 512 * (T)), qv[T])
            flag_wait(flags + 4 * (lane & 3));
            LDQ(0); LDQ(1); LDQ(2);
            SB();
            STAMP(0, 1);
            ROW_WAIT(0); ROWB(0); SB(); LDQ(3); LDQ(4); SB();
            STAMP(0, 2);
            ROW_WAIT(1); ROWB(1); ROWB(2); SB(); LDQ(5); LDQ(6); LDQ(7); SB();
            STAMP(0, 3);
            ROW_WAIT(2); ROWB(3); ROWB(4); SB();
            STAMP(0, 4);
            ROW_WAIT(3); ROWB(5); ROWB(6); SB();
            STAMP(0, 5);
            ROW_WAIT(4); ROWB(7);
        } else {
            const uint32_t t = gi * 64 + lane;
            const gc_t pkv = (gc_t)((t & 2) ? vin : kin) + ((t >> 2) * (2 * DCH) + 256 * xcd + 128 * (t & 1));
            const uint32_t ouq = (t >> 1) * (2 * DCH) + 256 * xcd + 128 * (t & 1);
            const gc_t pu = (gc_t)u + ouq, pq = (gc_t)qin + ouq;
            uint64_t seq = rw == 0 ? SEQ0 : rw == 1 ? SEQ1 : rw == 2 ? SEQ2 : SEQ3;
            for (int d = rw == 0 ? DL0 : rw == 1 ? DL1 : rw == 2 ? DL2 : DL3; d > 0; --d) __builtin_amdgcn_s_sleep(8);
#pragma unroll 1
            for (uint32_t c; (c = (uint32_t)seq & 0xffu) != 0xffu; seq >>= 8) {
                const gc_t a = c < 8 ? pkv + (size_t)c * (256 * 2 * DCH) : (c < 12 ? pu : pq) + (size_t)(c & 3) * (512 * 2 * DCH);
                const uint32_t pv = GLD(uint32_t, a, 0, 0);
                SB(); KEEP(pv); SB();
                if ((c >> 2) == 2) flag_set(flags + 288 + 4 * (c & 3));
            }
#define ROWA(T) ROW_OUT(T) = row_read(rg - 512 * (T))
            STAMP(0, 1);
            ROW_WAIT(0); ROWA(0); SB();
            STAMP(0, 2);
            ROW_WAIT(1); ROWA(1); ROWA(2); SB();
            STAMP(0, 3);
            ROW_WAIT(2); ROWA(3); ROWA(4); SB();
            STAMP(0, 4);
            ROW_WAIT(3); ROWA(5); ROWA(6); SB();
            STAMP(0, 5);
            ROW_WAIT(4); ROWA(7);
        }
        STAMP(1, 6);
        STAMPW(1, 7);
        RT(0, 1);
        return;
    }
    if (w >= 8) {
        const int pw = w - 8, c2 = lane & 3, rq = lane >> 2;
        const int s0 = 64 * pw + 4 * rq;
        uint32_t eo = (uint32_t)(s0 + 2) * (2 * DCH) + grp * 16 + 4 * c2;
        OPAQUE(eo);
        char* xp = lds + 2 * c2 * CH_STRIDE + 2 * (2044 - s0);
#define BLK(o, P) ((uint32_t)((o) + (P) * (256u * 2 * DCH)))
        if (part) {
            uint32_t kk[8][4], vv[8][4], hk[8][3];
            uint32_t ho = (uint32_t)(64 * pw + 16 * g - 2) * (2 * DCH) + grp * 16 + 4 * c2;
            OPAQUE(ho);
            const bool hz = (pw | g) == 0;
            const uint32_t ho0 = hz ? ho + 4 * (2 * DCH) : ho;
#define LDK(P) do { _Pragma("unroll") for (int i = 0; i < 4; ++i) kk[P][i] = GLD(uint32_t, (gc_t)kin, BLK(eo, P), (i - 2) * (2 * DCH)); } while (0)
#define LDV(P) do { _Pragma("unroll") for (int i = 0; i < 4; ++i) vv[P][i] = GLD(uint32_t, (gc_t)vin, BLK(eo, P), (i - 2) * (2 * DCH)); } while (0)
#define LDH(P) do { _Pragma("unroll") for (int i = 0; i < 3; ++i) hk[P][i] = GLD(uint32_t, (gc_t)kin, ((P) ? BLK(ho, P) : ho0), (i - 1) * (2 * DCH)); } while (0)
            const u32x4 wv = GLD(u32x4, (gc_t)shk, (uint32_t)(grp * 8 + 2 * c2) * 8, 0);
#pragma unroll
            for (int p = 0; p < NUP; ++p) { LDK(p); LDH(p); LDV(p); SB(); }
            const float wt[2][4] = {{lo_f(wv[0]), hi_f(wv[0]), lo_f(wv[1]), hi_f(wv[1])}, {lo_f(wv[2]), hi_f(wv[2]), lo_f(wv[3]), hi_f(wv[3])}};
#pragma unroll
            for (int i = 0; i < 3; ++i) hk[0][i] = hz ? 0u : hk[0][i];
            STAMP(0, 1);
#pragma unroll
            for (int p = 0; p < 8; ++p) {
                uint32_t kp[7];
#pragma unroll
                for (int i = 0; i < 3; ++i)
                    kp[i] = (uint32_t)__builtin_amdgcn_update_dpp((int)hk[p][i], (int)kk[p][1 + i], 0x114  , 0xf, 0xf, false);
#pragma unroll
                for (int i = 0; i < 4; ++i) kp[3 + i] = kk[p][i];
#pragma unroll
                for (int h = 0; h < 2; ++h) {
                    float kf[7], vf[4];
#pragma unroll
                    for (int i = 0; i < 7; ++i) kf[i] = h ? hi_f(kp[i]) : lo_f(kp[i]);
#pragma unroll
                    for (int i = 0; i < 4; ++i) vf[i] = h ? hi_f(vv[p][i]) : lo_f(vv[p][i]);
                    uint32_t o0, o1;
                    kv4(kf, wt[h], vf, o0, o1);
                    put_x4(xp + h * CH_STRIDE - 512 * p, o0, o1);
                    SB();
                    if (h == 0 && p + 3 < 8 && p >= LATE) { LDK(p + 3); LDH(p + 3); }
                    SB();
                }
                flag_set(flags + 16 * p + 4 * pw);
                SB();
                if (p == 0) { if (NUP < 2) { LDK(1); LDH(1); LDV(1); } LDK(2); LDH(2); LDV(2); SB(); }
                if (p + 3 < 8 && p < LATE) { LDK(p + 3); LDH(p + 3); }
                if (p + 3 < 8) LDV(p + 3);
                SB();
                if (p < 6) STAMP(0, 2 + p); else STAMP(1, p - 5);
            }
        } else {
            uint32_t uu[8][4];
#define LDU(P) do { _Pragma("unroll") for (int i = 0; i < 4; ++i) uu[P][i] = GLD(uint32_t, (gc_t)u, BLK(eo, P), (i - 2) * (2 * DCH)); SB(); } while (0)
#define PUTU(P) do { _Pragma("unroll") for (int h = 0; h < 2; ++h) { \
                    const uint32_t sel = h ? 0x07060302u : 0x05040100u; \
                    put_x4(xp + h * CH_STRIDE - 512 * (P), __builtin_amdgcn_perm(uu[P][2], uu[P][3], sel), __builtin_amdgcn_perm(uu[P][0], uu[P][1], sel)); } \
                flag_set(flags + 16 * (P) + 4 * pw); SB(); \
                if ((P) < 6) STAMP(0, 2 + (P)); else STAMP(1, (P) - 5); } while (0)
            flag_wait(flags + 288);
            STAMP(0, 1);
            LDU(0); LDU(1);
            PUTU(0); PUTU(1);
            flag_wait(flags + 292);
            LDU(2); LDU(3);
            PUTU(2); PUTU(3);
            flag_wait(flags + 296);
            LDU(4); LDU(5);
            PUTU(4); PUTU(5);
            flag_wait(flags + 300);
            LDU(6); LDU(7);
            PUTU(6); PUTU(7);
        }
        RT(0, 1);
        return;
    }

    const int chl = grp * 8 + w;
    char* cb = lds + w * CH_STRIDE;
    u32x4 kq[4];
    if (part) {
        const uint16_t* ksrc = ssmk + (size_t)chl * LSEQ;
        if (D_SSM) __builtin_amdgcn_s_sleep(D_SSM);
#pragma unroll
        for (int i = 0; i < 4; ++i) kq[i] = *(const u32x4*)(ksrc + 8 * (64 * i + lane));
        uint32_t dr = sD[chl];
        SB();
        KEEP(pf);
        OPAQUE(dr);
        kq[0][0] = pk_bf16(lo_f(kq[0][0]) + (lane ? 0.f : lo_f(dr)), hi_f(kq[0][0]));
    } else {
        float dt = bf2f(logdt[chl]);
        uint32_t pr[8];
#pragma unroll
        for (int bt = 0; bt < 2; ++bt) {
            const int n = chl * NMODE + bt * 64 + lane;
            pr[4 * bt] = logA[n]; pr[4 * bt + 1] = Aim[n]; pr[4 * bt + 2] = Cre[n]; pr[4 * bt + 3] = Cim[n];
        }
        uint32_t dr = Dsk[chl];
        SB();
        dt = rbf(expf(dt));
        float pa[2], pb[2], pcr[2], pci[2];
        float mx = 0.f;
#pragma unroll
        for (int bt = 0; bt < 2; ++bt) {
            const float ar = -rbf(expf(lo_f(pr[4 * bt])));
            pa[bt] = (ar * dt) * 1.4426950408889634f;
            pb[bt] = (lo_f(pr[4 * bt + 1]) * dt) * 0.15915494309189535f;
            pcr[bt] = 2.f * (lo_f(pr[4 * bt + 2]) * dt);
            pci[bt] = 2.f * (lo_f(pr[4 * bt + 3]) * dt);
            mx = fmaxf(mx, fmaxf(fabsf(pcr[bt]), fabsf(pci[bt])));
        }
        KEEP(pf);
        OPAQUE(dr);
        const float Dv = lane ? 0.f : lo_f(dr);
        int mi = (int)__float_as_uint(mx);
#define DPPMAX(ctrl) mi = max(mi, __builtin_amdgcn_update_dpp(0, mi, ctrl, 0xf, 0xf, false))
        DPPMAX(0x111); DPPMAX(0x112); DPPMAX(0x114); DPPMAX(0x118); DPPMAX(0x142); DPPMAX(0x143);
        int sf = 266 - ((__builtin_amdgcn_readlane(mi, 63) >> 23) & 0xff);
        sf = sf < 1 ? 1 : (sf > 239 ? 239 : sf);
        const float scl = __uint_as_float((uint32_t)sf << 23);
        const float inv = __uint_as_float((uint32_t)(240 - sf) << 23);
        STAMP(0, 1);

        char* Tl = cb + lane * 8;
        const char* Tr = cb + fr * KG_ROWB + g * 16;
#define TW(row, v0, v1) *(u32x2*)(Tl + (row) * KG_ROWB) = (u32x2){pk16(v0), pk16(v1)}
#define B2(X) do { _Pragma("unroll") for (int bt = 0; bt < 2; ++bt) { X; } } while (0)
        f16x8 bfr[8], af[8];
        f32x2 z8c[2], z1[2], z128[2];
        B2(z8c[bt] = anchor(pa[bt] * 8.f, pb[bt] * -8.f));
        B2(z1[bt] = anchor(pa[bt], pb[bt]));
        B2(z128[bt] = anchor(pa[bt] * 128.f, pb[bt] * 128.f));
        f32x2 f[2][16], h[2][8], cg[2][17], zs[2][3], zl[2][3], t[2][8];
        B2(t[bt][0] = cm1(z8c[bt], z8c[bt]); t[bt][1] = cm1(z128[bt], z128[bt]); t[bt][2] = cm1(z1[bt], z1[bt]));
        B2(cg[bt][0] = ((f32x2){pcr[bt] * scl, pci[bt] * scl}); f[bt][1] = z8c[bt] * 16384.f);
        SB();
        B2(zs[bt][0] = cm2(z8c[bt], z8c[bt], t[bt][0]); zl[bt][0] = cm2(z128[bt], z128[bt], t[bt][1]); h[bt][2] = cm2(z1[bt], z1[bt], t[bt][2]));
        B2(t[bt][3] = cm1(cg[bt][0], z128[bt]));
        SB();
        B2(t[bt][0] = cm1(zs[bt][0], zs[bt][0]); t[bt][1] = cm1(zl[bt][0], zl[bt][0]); t[bt][2] = cm1(h[bt][2], h[bt][2]);
           t[bt][4] = cm1(h[bt][2], z1[bt]); t[bt][5] = cm1(f[bt][1], zs[bt][0]); t[bt][6] = cm1(cg[bt][0], zl[bt][0]);
           cg[bt][1] = cm2(cg[bt][0], z128[bt], t[bt][3]); f[bt][2] = zs[bt][0] * 16384.f);
        SB();
        B2(zs[bt][1] = cm2(zs[bt][0], zs[bt][0], t[bt][0]); zl[bt][1] = cm2(zl[bt][0], zl[bt][0], t[bt][1]); h[bt][4] = cm2(h[bt][2], h[bt][2], t[bt][2]);
           h[bt][3] = cm2(h[bt][2], z1[bt], t[bt][4]); f[bt][3] = cm2(f[bt][1], zs[bt][0], t[bt][5]); cg[bt][2] = cm2(cg[bt][0], zl[bt][0], t[bt][6]);
           t[bt][3] = cm1(cg[bt][1], zl[bt][0]));
        SB();
        B2(t[bt][0] = cm1(zs[bt][1], zs[bt][1]); t[bt][1] = cm1(zl[bt][1], zl[bt][1]);
           t[bt][4] = cm1(f[bt][1], zs[bt][1]); t[bt][5] = cm1(f[bt][2], zs[bt][1]); t[bt][6] = cm1(f[bt][3], zs[bt][1]);
           cg[bt][3] = cm2(cg[bt][1], zl[bt][0], t[bt][3]); f[bt][4] = zs[bt][1] * 16384.f);
        SB();
        B2(zs[bt][2] = cm2(zs[bt][1], zs[bt][1], t[bt][0]); zl[bt][2] = cm2(zl[bt][1], zl[bt][1], t[bt][1]);
           f[bt][5] = cm2(f[bt][1], zs[bt][1], t[bt][4]); f[bt][6] = cm2(f[bt][2], zs[bt][1], t[bt][5]); f[bt][7] = cm2(f[bt][3], zs[bt][1], t[bt][6]));
        B2(f[bt][0] = ((f32x2){16384.f, 0.f}));
#pragma unroll
        for (int i = 0; i < 5; ++i) TW(i, f[0][i], f[1][i]);
        SB();
        B2(_Pragma("unroll") for (int i = 1; i < 8; ++i) t[bt][i] = cm1(f[bt][i], zs[bt][2]); f[bt][8] = zs[bt][2] * 16384.f);
#pragma unroll
        for (int i = 5; i < 8; ++i) TW(i, f[0][i], f[1][i]);
        SB();
        B2(_Pragma("unroll") for (int i = 1; i < 8; ++i) f[bt][8 + i] = cm2(f[bt][i], zs[bt][2], t[bt][i]));
        SB();
        B2(t[bt][0] = cm1(h[bt][4], z1[bt]); t[bt][1] = cm1(h[bt][4], h[bt][2]); t[bt][2] = cm1(h[bt][4], h[bt][3]));
#pragma unroll
        for (int i = 8; i < 16; ++i) TW(i, f[0][i], f[1][i]);
#pragma unroll
        for (int ks = 0; ks < 8; ++ks) bfr[ks] = *(const f16x8*)(Tr + ks * 64);
        B2(h[bt][1] = z1[bt]; h[bt][5] = cm2(h[bt][4], z1[bt], t[bt][0]); h[bt][6] = cm2(h[bt][4], h[bt][2], t[bt][1]); h[bt][7] = cm2(h[bt][4], h[bt][3], t[bt][2]));
        SB();
        f32x4 ac[3][2];
#define TRD() do { _Pragma("unroll") for (int ks = 0; ks < 8; ++ks) af[ks] = *(const f16x8*)(Tr + ks * 64); SB(); } while (0)
#define TMM(rt) do { \
            ac[rt][0] = (f32x4){0, 0, 0, 0}; ac[rt][1] = (f32x4){0, 0, 0, 0}; \
            _Pragma("unroll") for (int ks = 0; ks < 8; ++ks) \
                ac[rt][ks & 1] = __builtin_amdgcn_mfma_f32_16x16x32_f16(af[ks], bfr[ks], ac[rt][ks & 1], 0, 0, 0); SB(); } while (0)
#pragma unroll
        for (int aa = 0; aa < 2; ++aa) {
            TW(8 * aa, cg[0][aa], cg[1][aa]);
#pragma unroll
            for (int e0 = 1; e0 < 8; e0 += 4) {
                B2(_Pragma("unroll") for (int e = e0; e < e0 + 4 && e < 8; ++e) t[bt][e] = cm1(cg[bt][aa], h[bt][e]));
                SB();
                B2(_Pragma("unroll") for (int e = e0; e < e0 + 4 && e < 8; ++e) t[bt][e] = cm2(cg[bt][aa], h[bt][e], t[bt][e]));
                SB();
#pragma unroll
                for (int e = e0; e < e0 + 4 && e < 8; ++e) TW(8 * aa + e, t[0][e], t[1][e]);
                SB();
            }
        }
        TRD();
        uint16_t* KD = (uint16_t*)(lds + KD_OFF + w * 1536);
#define KDW(rt) do { \
            f32x4 v = (ac[rt][0] + ac[rt][1]) * inv; \
            if ((rt) == 0) v[0] += Dv; \
            _Pragma("unroll") for (int r = 0; r < 4; r += 2) { \
                uint32_t p = pk_bf16(v[r], v[r + 1]); \
                KD[(16 * (rt) + 4 * g + r) * 16 + fr] = (uint16_t)p; \
                KD[(16 * (rt) + 4 * g + r + 1) * 16 + fr] = (uint16_t)(p >> 16); } } while (0)
#pragma unroll
        for (int aa = 2; aa < 4; ++aa) {
            B2(_Pragma("unroll") for (int e = 1; e < 4; ++e) t[bt][e] = cm1(cg[bt][aa], h[bt][2 * e]));
            TW(4 * (aa - 2), cg[0][aa], cg[1][aa]);
            SB();
            B2(_Pragma("unroll") for (int e = 1; e < 4; ++e) t[bt][e] = cm2(cg[bt][aa], h[bt][2 * e], t[bt][e]));
            SB();
#pragma unroll
            for (int e = 1; e < 4; ++e) TW(4 * (aa - 2) + e, t[0][e], t[1][e]);
            SB();
        }
        TMM(0);
        B2(_Pragma("unroll") for (int i = 0; i < 4; ++i) { cg[bt][4 + i] = cm1(cg[bt][i], zl[bt][1]); cg[bt][8 + i] = cm1(cg[bt][i], zl[bt][2]); });
        SB();
        B2(_Pragma("unroll") for (int i = 0; i < 4; ++i) { cg[bt][4 + i] = cm2(cg[bt][i], zl[bt][1], cg[bt][4 + i]); cg[bt][8 + i] = cm2(cg[bt][i], zl[bt][2], cg[bt][8 + i]); });
        KDW(0);
        SB();
        B2(_Pragma("unroll") for (int i = 0; i < 4; ++i) t[bt][i] = cm1(cg[bt][4 + i], h[bt][4]); cg[bt][16] = cm1(cg[bt][8], zl[bt][2]));
#pragma unroll
        for (int i = 0; i < 4; ++i) TW(8 + 2 * i, cg[0][4 + i], cg[1][4 + i]);
        SB();
        B2(_Pragma("unroll") for (int i = 0; i < 4; ++i) t[bt][i] = cm2(cg[bt][4 + i], h[bt][4], t[bt][i]); cg[bt][16] = cm2(cg[bt][8], zl[bt][2], cg[bt][16]));
        SB();
#pragma unroll
        for (int i = 0; i < 4; ++i) TW(9 + 2 * i, t[0][i], t[1][i]);
        SB();
        TRD();
        B2(_Pragma("unroll") for (int i = 0; i < 4; ++i) cg[bt][12 + i] = cm1(cg[bt][8 + i], zl[bt][1]));
        TW(8, cg[0][16], cg[1][16]);
        SB();
        B2(_Pragma("unroll") for (int i = 0; i < 4; ++i) cg[bt][12 + i] = cm2(cg[bt][8 + i], zl[bt][1], cg[bt][12 + i]));
#pragma unroll
        for (int aa = 8; aa < 12; ++aa) TW(aa - 8, cg[0][aa], cg[1][aa]);
        SB();
#pragma unroll
        for (int aa = 12; aa < 16; ++aa) TW(aa - 8, cg[0][aa], cg[1][aa]);
        SB();
        TMM(1);
        TRD();
        KDW(1);
        SB();
        TMM(2);
        STAMP(0, 2);
        KDW(2);
        {
            int tid = threadIdx.x;
            asm volatile("" : "+v"(tid));
            const int lane = tid & 63, g = lane >> 4, fr = lane & 15;
            const int hi = lane >> 5, last = (fr == 15);
            const uint16_t* p0 = KD + (hi ? 16 + 4 * (g - 2) : 8 * g) * 16 + fr;
            const uint16_t* p1 = p0 - 16 * hi, * p2 = p0 - 32 * hi, * p3 = p0 - 48 * hi;
            const uint16_t* p7 = p0 + (hi ? (last ? 49 : 1) : 112);
            kq[0][0] = (uint32_t)p0[0] | ((uint32_t)p1[16] << 16);
            kq[0][1] = (uint32_t)p1[32] | ((uint32_t)p1[48] << 16);
            kq[0][2] = (uint32_t)p2[64] | ((uint32_t)p3[80] << 16);
            kq[0][3] = (uint32_t)p3[96] | ((uint32_t)p7[0] << 16);
            const uint16_t* q0 = KD + (24 + 2 * g) * 16 + fr;
            const uint32_t a = q0[0], bq = q0[16], c = q0[last ? 17 : 1];
            kq[1][0] = __builtin_amdgcn_perm(a, a, 0x01000100u);
            kq[1][1] = __builtin_amdgcn_perm(bq, a, 0x05040100u);
            kq[1][2] = __builtin_amdgcn_perm(bq, bq, 0x01000100u);
            kq[1][3] = __builtin_amdgcn_perm(c, c, 0x01000100u);
            const uint32_t sel = (lane & 1) ? 0x05040504u : 0x05040100u;
#pragma unroll
            for (int i = 2; i < 4; ++i) {
                const uint32_t e0 = KD[512 + 64 * (i - 2) + lane], e1 = KD[513 + 64 * (i - 2) + lane];
                kq[i][0] = kq[i][1] = __builtin_amdgcn_perm(e0, e0, 0x01000100u);
                kq[i][2] = __builtin_amdgcn_perm(e1, e0, sel);
                kq[i][3] = __builtin_amdgcn_perm(e1, e1, 0x01000100u);
            }
        }
    }
    int tid2 = threadIdx.x;
    asm volatile("" : "+v"(tid2));
#define lane (tid2 & 63)
#define g ((tid2 >> 4) & 3)
#define fr (tid2 & 15)
    {
        const u32x4 z = {0, 0, 0, 0};
        if (lane < 29) {
            *(u32x4*)(cb + KA_OFF + 16 * lane) = z;
            *(u32x4*)(cb + KB_OFF + 16 * lane) = z;
        }
        uint32_t c = 0;
#pragma unroll
        for (int i = 0; i < 4; ++i) {
            put_k(cb, 64 * i + lane, kq[i], prev_lane(kq[i][3], c));
            c = __builtin_amdgcn_readlane(kq[i][3], 63);
        }
    }
    STAMP(0, 3);

    const lds_t lb = (lds_t)lds + w * CH_STRIDE;
    const lds_t kaddr = lb + ((fr & 1) ? KA_OFF : KB_OFF) + 2 * (32 * (fr >> 1) + 8 * g + 8);
    lds_t xaddr = lb + ((fr & 1) ? (XA_OFF + 2 * (1822 - 2 * fr + 8 * g)) : (XB_OFF + 2 * (1820 - 2 * fr + 8 * g))) - 3584;
    asm volatile("" : "+v"(xaddr));
    const int lo_ = 64 * g + 2 * fr;
    char* sg = cb + KA_OFF + 464 + 2 * (2046 - lo_);
#define EPI(T) epi_acc(acc[T], sg - 512 * (T))
#define SFLAG(S) flag_set(flags + 128 + 32 * (S) + 4 * w)

    f32x4 acc[8];
#pragma unroll
    for (int i = 0; i < 8; ++i) acc[i] = (f32x4){0, 0, 0, 0};

    flag_wait(flags + 4 * (lane & 3));
    STAMP(0, 4);
    stage<0, 1>(kaddr, xaddr, acc);
    STAMP(0, 5);
    EPI(0); SFLAG(0);
    flag_wait(flags + 16 + 4 * (lane & 7));
    STAMP(0, 6);
    stage<1, 2>(kaddr, xaddr, acc);
    STAMP(0, 7);
    EPI(1); EPI(2); SFLAG(1);
    flag_wait(flags + 48 + 4 * (lane & 7));
    STAMP(1, 1);
    stage<3, 2>(kaddr, xaddr, acc);
    STAMP(1, 2);
    EPI(3); EPI(4); SFLAG(2);
    flag_wait(flags + 80 + 4 * (lane & 7));
    STAMP(1, 3);
    stage<5, 2>(kaddr, xaddr, acc);
    STAMP(1, 4);
    EPI(5); EPI(6); SFLAG(3);
    flag_wait(flags + 112 + 4 * (lane & 3));
    STAMP(1, 5);
    {
        f32x4 a7b = {0, 0, 0, 0};
#pragma unroll
        for (int c = 0; c < 8; c += 2) {
            MFMA(ldk(kaddr + 64 * c), ldx(xaddr + 64 * c), acc[7]);
            MFMA(ldk(kaddr + 64 * c + 64), ldx(xaddr + 64 * c + 64), a7b);
        }
#pragma unroll
        for (int i = 0; i < 4; ++i) {
            float s = acc[7][i] + a7b[i];
            asm volatile("" : "+v"(s));
            acc[7][i] = s;
        }
    }
    EPI(7); SFLAG(4);
    STAMP(1, 6);
    RT(0, 1);
}

extern "C" void kernel_launch(void* const* d_in, const int* in_sizes, int n_in,
                              void* d_out, int out_size, void* d_ws, size_t ws_size,
                              hipStream_t stream) {
    fused_kernel<<<256, 1024, 0, stream>>>(
        (const uint16_t*)d_in[0], (const uint16_t*)d_in[1], (const uint16_t*)d_in[2], (const uint16_t*)d_in[3],
        (const uint16_t*)d_in[4], (const uint16_t*)d_in[5], (const uint16_t*)d_in[6], (const uint16_t*)d_in[7],
        (const uint16_t*)d_in[8], (const uint16_t*)d_in[9], (const uint16_t*)d_in[10], (const uint16_t*)d_in[11],
        (const uint16_t*)d_in[12], (uint16_t*)d_out);
}
```

```cpp
#include <hip/hip_runtime.h>
#include <stdint.h>

typedef __attribute__((ext_vector_type(4))) unsigned int u32x4;
typedef __attribute__((ext_vector_type(2))) unsigned int u32x2;
typedef __attribute__((ext_vector_type(4))) float f32x4;
typedef __attribute__((ext_vector_type(2))) float f32x2;
typedef __attribute__((ext_vector_type(8))) _Float16 f16x8;
typedef __attribute__((ext_vector_type(2))) _Float16 f16x2;
typedef __attribute__((ext_vector_type(8))) __bf16 bf16x8;
typedef __attribute__((ext_vector_type(2))) __bf16 bf16x2;
typedef __attribute__((ext_vector_type(4))) short s16x4;

#define STAMP(k, i)
#define STAMPW(k, i)
#define RT(k, i)
#define STDECL
#define LSEQ 2048
#define DCH 1024
#define NMODE 128

__device__ __forceinline__ float bf2f(uint16_t v) { return __uint_as_float(((uint32_t)v) << 16); }
__device__ __forceinline__ float lo_f(uint32_t w) { return __uint_as_float(w << 16); }
__device__ __forceinline__ float hi_f(uint32_t w) { return __uint_as_float(w & 0xffff0000u); }
__device__ __forceinline__ uint32_t pk_bf16(float lo, float hi) {
    f32x2 x = {lo, hi};
    bf16x2 y = __builtin_convertvector(x, bf16x2);
    return __builtin_bit_cast(uint32_t, y);
}
__device__ __forceinline__ uint32_t pk_f16(float lo, float hi) {
    f32x2 x = {lo, hi};
    f16x2 y = __builtin_convertvector(x, f16x2);
    return __builtin_bit_cast(uint32_t, y);
}
__device__ __forceinline__ float rbf(float a) { return lo_f(pk_bf16(a, a)); }

#define KEEP(v) asm volatile("" :: "v"(v))
#define KEEPS(v) asm volatile("" :: "s"(v))
#define OPAQUE(v) asm volatile("" : "+v"(v))
#define SB() __builtin_amdgcn_sched_barrier(0)
__device__ __forceinline__ f32x2 cm1(f32x2 a, f32x2 b) {
    f32x2 t;
    asm("v_pk_mul_f32 %0, %1, %2 op_sel:[0,0] op_sel_hi:[0,1]" : "=v"(t) : "v"(a), "v"(b));
    return t;
}
__device__ __forceinline__ f32x2 cm2(f32x2 a, f32x2 b, f32x2 t) {
    f32x2 c;
    asm("v_pk_fma_f32 %0, %1, %2, %3 op_sel:[1,1,0] op_sel_hi:[1,0,1] neg_lo:[1,0,0]" : "=v"(c) : "v"(a), "v"(b), "v"(t));
    return c;
}
__device__ __forceinline__ f32x2 cmul(f32x2 a, f32x2 b) { return cm2(a, b, cm1(a, b)); }
__device__ __forceinline__ f32x2 anchor(float a, float b) {
    const float e = __builtin_amdgcn_exp2f(a);
    const float th = __builtin_amdgcn_fractf(b);
    return (f32x2){e * __builtin_amdgcn_cosf(th), e * __builtin_amdgcn_sinf(th)};
}
__device__ __forceinline__ uint32_t pk16(f32x2 c) { return pk_f16(c[0], c[1]); }

#define KG_ROWB 528

#define D_PF 32
#define D_SSM 0
#define NUP 1
#define LATE 1
#define SEQ0 0xff0c06040100ull
#define SEQ1 0xff0d070902ull
#define SEQ2 0xff0e0b0508ull
#define SEQ3 0xff0f0a03ull
#define DL0 0
#define DL1 7
#define DL2 9
#define DL3 11
#define KA_OFF 0
#define KB_OFF 4640
#define XA_OFF 9216
#define XB_OFF 13440
#define CH_STRIDE 17696
#define LA_OFF 4640
#define LB_OFF 5792
#define L2_OFF (XA_OFF + 4576)
#define L3_OFF (XA_OFF + 5088)
#define CW_OFF (XA_OFF + 5440)
#define KD_OFF (8 * CH_STRIDE)
#define FLAG_OFF (KD_OFF + 8 * 1536)
#define LDS_TOTAL (FLAG_OFF + 304)
#define CODE_BYTES 24320

template <int OA, int OB>
__device__ __forceinline__ void put_k(char* cb, int y, u32x4 d, uint32_t prev) {
    *(u32x4*)(cb + OA + 16 * y) = d;
    u32x4 o;
    o[0] = __builtin_amdgcn_alignbyte(d[0], prev, 2);
    o[1] = __builtin_amdgcn_alignbyte(d[1], d[0], 2);
    o[2] = __builtin_amdgcn_alignbyte(d[2], d[1], 2);
    o[3] = __builtin_amdgcn_alignbyte(d[3], d[2], 2);
    *(u32x4*)(cb + OB + 16 * y) = o;
}
__device__ __forceinline__ void put_x4(char* p, uint32_t o0, uint32_t o1) {
    *(u32x2*)(p + XA_OFF) = (u32x2){o0, o1};
    *(uint32_t*)(p + XB_OFF - 4) = o0;
    *(uint32_t*)(p + XB_OFF) = o1;
}
__device__ __forceinline__ uint32_t prev_lane(uint32_t v, uint32_t carry) {
    return (uint32_t)__builtin_amdgcn_update_dpp((int)carry, (int)v, 0x138  , 0xf, 0xf, false);
}
typedef __attribute__((address_space(3))) char* lds_t;
typedef __attribute__((address_space(3))) const volatile u32x2* lds_v2;
typedef __attribute__((address_space(3))) volatile uint32_t* lds_vu;
typedef __attribute__((address_space(3))) const bf16x8* lds_k8;
typedef __attribute__((address_space(1))) const char* gc_t;
typedef __attribute__((address_space(1))) char* gw_t;
#define GLD(T, base, voff, imm) (*(__attribute__((address_space(1))) const T*)((base) + (voff) + (imm)))
__device__ __forceinline__ bf16x8 ldx(lds_t p) {
    u32x2 a = *(lds_v2)p, b = *(lds_v2)(p + 8);
    u32x4 v = {a[0], a[1], b[0], b[1]};
    return __builtin_bit_cast(bf16x8, v);
}
__device__ __forceinline__ bf16x8 ldk(lds_t p) { return *(lds_k8)p; }
#define MFMA(a, b, c) c = __builtin_amdgcn_mfma_f32_16x16x32_bf16(a, b, c, 0, 0, 0)
#define SG_DS(n) __builtin_amdgcn_sched_group_barrier(0x100, n, 0)
#define SG_MF(n) __builtin_amdgcn_sched_group_barrier(0x008, n, 0)

#define RB(r) "v_cvt_pk_bf16_f32 " r ", 0, " r "\n\t"
__device__ __forceinline__ void kv4(const float (&k)[7], const float (&wt)[4], const float (&v)[4], uint32_t& o0, uint32_t& o1) {
    float a0, a1;
    asm("v_mul_f32 %2, %4, %14\n\t" "v_mul_f32 %3, %5, %14\n\t" "v_mul_f32 %0, %6, %14\n\t" "v_mul_f32 %1, %7, %14\n\t"
        "v_fmac_f32 %2, %5, %13\n\t" "v_fmac_f32 %3, %6, %13\n\t" "v_fmac_f32 %0, %7, %13\n\t" "v_fmac_f32 %1, %8, %13\n\t"
        "v_fmac_f32 %2, %6, %12\n\t" "v_fmac_f32 %3, %7, %12\n\t" "v_fmac_f32 %0, %8, %12\n\t" "v_fmac_f32 %1, %9, %12\n\t"
        "v_fmac_f32 %2, %7, %11\n\t" "v_fmac_f32 %3, %8, %11\n\t" "v_fmac_f32 %0, %9, %11\n\t" "v_fmac_f32 %1, %10, %11\n\t"
        RB("%2") RB("%3") RB("%0") RB("%1")
        "v_mul_f32 %2, %2, %15\n\t" "v_mul_f32 %3, %3, %16\n\t" "v_mul_f32 %0, %0, %17\n\t" "v_mul_f32 %1, %1, %18\n\t"
        "v_cvt_pk_bf16_f32 %0, %1, %0\n\t" "v_cvt_pk_bf16_f32 %1, %3, %2\n\t"
        : "=&v"(o0), "=&v"(o1), "=&v"(a0), "=&v"(a1)
        : "v"(k[0]), "v"(k[1]), "v"(k[2]), "v"(k[3]), "v"(k[4]), "v"(k[5]), "v"(k[6]),
          "v"(wt[0]), "v"(wt[1]), "v"(wt[2]), "v"(wt[3]), "v"(v[0]), "v"(v[1]), "v"(v[2]), "v"(v[3]));
}
__device__ __forceinline__ void epi_acc(const f32x4 a, char* dst) {
    uint32_t o0, o1;
    asm("v_cvt_pk_bf16_f32 %0, %3, %2\n\tv_cvt_pk_bf16_f32 %1, %5, %4" : "=&v"(o0), "=v"(o1) : "v"(a[0]), "v"(a[1]), "v"(a[2]), "v"(a[3]));
    *(uint32_t*)(dst) = o0;
    *(uint32_t*)(dst - 64) = o1;
}
__device__ __forceinline__ u32x4 row_read(const char* a0) {
    s16x4 v0 = __builtin_amdgcn_ds_read_tr16_b64_v4i16((__attribute__((address_space(3))) s16x4*)(a0));
    s16x4 v1 = __builtin_amdgcn_ds_read_tr16_b64_v4i16((__attribute__((address_space(3))) s16x4*)(a0 + 4 * CH_STRIDE));
    u32x2 p0 = __builtin_bit_cast(u32x2, v0), p1 = __builtin_bit_cast(u32x2, v1);
    return (u32x4){p0[0], p0[1], p1[0], p1[1]};
}
__device__ __forceinline__ void gate2(uint32_t y0, uint32_t y1, uint32_t q0, uint32_t q1, uint32_t& o0, uint32_t& o1) {
    float t0, t1, t2, t3;
    asm("v_lshlrev_b32 %2, 16, %6\n\t" "v_lshlrev_b32 %0, 16, %8\n\t" "v_and_b32 %3, 0xffff0000, %6\n\t" "v_and_b32 %1, 0xffff0000, %8\n\t"
        "v_mul_f32 %2, %2, %0\n\t" "v_mul_f32 %3, %3, %1\n\t"
        "v_lshlrev_b32 %4, 16, %7\n\t" "v_lshlrev_b32 %0, 16, %9\n\t" "v_and_b32 %5, 0xffff0000, %7\n\t" "v_and_b32 %1, 0xffff0000, %9\n\t"
        "v_mul_f32 %4, %4, %0\n\t" "v_mul_f32 %5, %5, %1\n\t"
        "v_cvt_pk_bf16_f32 %0, %2, %3\n\t" "v_cvt_pk_bf16_f32 %1, %4, %5\n\t"
        : "=&v"(o0), "=&v"(o1), "=&v"(t0), "=&v"(t1), "=&v"(t2), "=&v"(t3)
        : "v"(y0), "v"(y1), "v"(q0), "v"(q1));
}
__device__ __forceinline__ u32x4 gate(u32x4 y, u32x4 q) {
    uint32_t o0, o1, o2, o3;
    gate2(y[0], y[1], q[0], q[1], o0, o1);
    gate2(y[2], y[3], q[2], q[3], o2, o3);
    return (u32x4){o0, o1, o2, o3};
}
__device__ __forceinline__ void lds_barrier() {
    __builtin_amdgcn_fence(__ATOMIC_RELEASE, "workgroup", "local");
    __builtin_amdgcn_s_barrier();
    __builtin_amdgcn_fence(__ATOMIC_ACQUIRE, "workgroup", "local");
}
__device__ __forceinline__ void flag_set(lds_t p) {
    __builtin_amdgcn_fence(__ATOMIC_RELEASE, "workgroup", "local");
    *(lds_vu)p = 1u;
}
__device__ __forceinline__ void flag_wait(lds_t p) {
    while (__builtin_amdgcn_ballot_w64(*(lds_vu)p == 0u) != 0ull) __builtin_amdgcn_s_sleep(1);
    __builtin_amdgcn_fence(__ATOMIC_ACQUIRE, "workgroup", "local");
}

template <int T0, int NT>
__device__ __forceinline__ void stage(lds_t kaddr, lds_t xaddr, f32x4 (&acc)[8]) {
    constexpr int NK = 8 - T0, ND = NK + 2 * NT;
    bf16x8 ka[NK], xa[NT], kb[NK], xb[NT];
#pragma unroll
    for (int t = 0; t < NT; ++t) xa[t] = ldx(xaddr + 512 * (7 - T0 - t));
#pragma unroll
    for (int m = 0; m < NK; ++m) ka[m] = ldk(kaddr + 512 * m);
#pragma unroll 1
    for (int c0 = 0; c0 < 8; c0 += 2) {
        const lds_t kp = kaddr + 64 * c0, xp = xaddr + 64 * c0;
#pragma unroll
        for (int t = 0; t < NT; ++t) xb[t] = ldx(xp + 64 + 512 * (7 - T0 - t));
#pragma unroll
        for (int m = 0; m < NK; ++m) kb[m] = ldk(kp + 64 + 512 * m);
#pragma unroll
        for (int t = 0; t < NT; ++t)
#pragma unroll
            for (int m = 0; m < NK - t; ++m) MFMA(ka[m], xa[t], acc[T0 + t + m]);
#pragma unroll
        for (int i = 0; i < ND; ++i) { SG_MF(1); SG_DS(1); }
        SG_MF(NK);
        SB();
#pragma unroll
        for (int t = 0; t < NT; ++t) xa[t] = ldx(xp + 128 + 512 * (7 - T0 - t));
#pragma unroll
        for (int m = 0; m < NK; ++m) ka[m] = ldk(kp + 128 + 512 * m);
#pragma unroll
        for (int t = 0; t < NT; ++t)
#pragma unroll
            for (int m = 0; m < NK - t; ++m) MFMA(kb[m], xb[t], acc[T0 + t + m]);
#pragma unroll
        for (int i = 0; i < ND; ++i) { SG_MF(1); SG_DS(1); }
        SG_MF(NK);
        SB();
    }
}

typedef u32x2 __attribute__((aligned(4))) u32x2_a4;
typedef u32x4 __attribute__((aligned(8))) u32x4_a8;
__device__ __forceinline__ bf16x8 ld_a4(lds_t p) {
    const u32x2 a = *(__attribute__((address_space(3))) const u32x2_a4*)p, b = *(__attribute__((address_space(3))) const u32x2_a4*)(p + 8);
    return __builtin_bit_cast(bf16x8, ((u32x4){a[0], a[1], b[0], b[1]}));
}
__device__ __forceinline__ bf16x8 ld_a8(lds_t p) { return __builtin_bit_cast(bf16x8, (u32x4)*(__attribute__((address_space(3))) const u32x4_a8*)p); }
#define MF0(a, b) __builtin_amdgcn_mfma_f32_16x16x32_bf16(a, b, ((f32x4){0, 0, 0, 0}), 0, 0, 0)
#define XFRAG(sig) ldk(xb + 64 * (56 - (sig)))
template <int TA, int TB>
__device__ __forceinline__ void far_field(lds_t l0, lds_t lb, int tid, f32x4 (&acc)[8]) {
    OPAQUE(tid);
    const int g = (tid >> 4) & 3, fr = tid & 15;
    lds_t xb = lb + XA_OFF + 480 - 32 * fr + 16 * g, cw = l0 + CW_OFF + 16 * (tid & 63);
    lds_t k2a = lb + L2_OFF + 96 + 8 * fr - 32 * g, k3a = lb + L3_OFF + 48 + 4 * fr - 16 * g;
    OPAQUE(xb); OPAQUE(cw); OPAQUE(k2a); OPAQUE(k3a);
    bf16x8 x1 = ldk(xb + 64 * (56 - 8 * TA) + 16), x2 = XFRAG(8 * TA);
    const bf16x8 c1 = ldk(cw), c2 = ldk(cw + CH_STRIDE), c4 = ldk(cw + 2 * CH_STRIDE), c3 = ldk(cw + 3 * CH_STRIDE);
    bf16x8 k2[3][2], k3[4];
#pragma unroll
    for (int m = 0; m < 3; ++m) { k2[m][0] = ld_a8(k2a + 128 * m + 16); k2[m][1] = ld_a8(k2a + 128 * m); }
#pragma unroll
    for (int m = 0; m < 4 - TA; ++m) k3[m] = ld_a4(k3a + 64 * m);
    f32x4 d[4], e[2];
    d[0] = MF0(x1, c1); d[1] = MF0(x1, c2); d[2] = MF0(x2, c1); d[3] = MF0(x2, c2);
    e[0] = MF0(x1, c4); e[1] = MF0(x2, c3);
    SB();
#pragma unroll
    for (int T = TA; T <= TB; ++T) {
        if (T < TB) { x1 = ldk(xb + 64 * (56 - 8 * (T + 1)) + 16); x2 = XFRAG(8 * (T + 1)); }
        u32x4 r2[2], r3;
#pragma unroll
        for (int h = 0; h < 2; ++h)
            r2[h] = (u32x4){pk_bf16(d[3][2 * h + 1], d[2][2 * h + 1]), pk_bf16(d[1][2 * h + 1], d[0][2 * h + 1]),
                            pk_bf16(d[3][2 * h], d[2][2 * h]), pk_bf16(d[1][2 * h], d[0][2 * h])};
        if (T < 4) r3 = (u32x4){pk_bf16(e[1][3], e[0][3]), pk_bf16(e[1][2], e[0][2]), pk_bf16(e[1][1], e[0][1]), pk_bf16(e[1][0], e[0][0])};
        SB();
        if (T < TB) {
            d[0] = MF0(x1, c1); d[1] = MF0(x1, c2); d[2] = MF0(x2, c1); d[3] = MF0(x2, c2);
            if (T + 1 < 4) { e[0] = MF0(x1, c4); e[1] = MF0(x2, c3); }
        }
        SB();
#pragma unroll
        for (int h = 0; h < 2; ++h)
#pragma unroll
            for (int m = 2; m < 5; ++m)
                if (T + m < 8) MFMA(__builtin_bit_cast(bf16x8, r2[h]), k2[m - 2][h], acc[T + m]);
#pragma unroll
        for (int m = 7; m >= 4; --m)
            if (T + m < 8) MFMA(__builtin_bit_cast(bf16x8, r3), k3[m - 4], acc[T + m]);
        SB();
    }
}
__device__ __forceinline__ void epi_a(const f32x4 a, lds_t dst) {
    uint32_t o0, o1;
    asm("v_cvt_pk_bf16_f32 %0, %5, %4\n\tv_cvt_pk_bf16_f32 %1, %3, %2" : "=&v"(o0), "=v"(o1) : "v"(a[0]), "v"(a[1]), "v"(a[2]), "v"(a[3]));
    *(__attribute__((address_space(3))) u32x2*)dst = (u32x2){o0, o1};
}
template <int TA>
__device__ __forceinline__ void near_field(lds_t l0, lds_t lb, int tid, uint32_t kk, lds_t sf, f32x4 (&acc)[8]) {
    OPAQUE(tid);
    const int g = (tid >> 4) & 3, fr = tid & 15;
    lds_t xb = lb + XA_OFF + 480 - 32 * fr + 16 * g, cw = l0 + CW_OFF + 16 * (tid & 63);
    lds_t kfa = lb + ((fr & 1) ? LA_OFF : LB_OFF) + 4 + 4 * (fr >> 1) + 16 * g;
    lds_t sg = lb + KA_OFF + 464 + 504 - 32 * fr - 8 * g, xf = l0 + FLAG_OFF + 4 * (tid & 3);
    OPAQUE(xb); OPAQUE(kfa); OPAQUE(sg); OPAQUE(xf);
    bf16x8 kf[17], kb, xq[3];
    xq[0] = XFRAG(8 * TA - 7);
    xq[1] = XFRAG(8 * TA - 6);
#pragma unroll
    for (int j = 0; j < 8; ++j) { kf[7 - j] = ld_a4(kfa + 64 * (7 - j)); kf[15 - j] = ld_a4(kfa + 64 * (15 - j)); }
    kf[16] = ld_a4(kfa + 64 * 16);
    if (TA == 0) {
        u32x4 mb = __builtin_bit_cast(u32x4, ldk(cw + 1024));
        kb = __builtin_bit_cast(bf16x8, mb & kk);
    }
    f32x4 a7b = {0, 0, 0, 0};
    SB();
#pragma unroll
    for (int T = TA; T < TA + 4; ++T) {
#pragma unroll
        for (int s = 0; s < 8; ++s) {
            const int n = 8 * (T - TA) + s;
            if (s == 6 && (T & 3) >= 1 && (T & 3) < 3) flag_wait(xf + 16 * (T + 1));
            if (n + 2 < 32) xq[(n + 2) % 3] = XFRAG(8 * T - 7 + s + 2);
            if (T == 7 && (s & 1)) MFMA(kf[7 - s], xq[n % 3], a7b);
            else MFMA(kf[7 - s], xq[n % 3], acc[T]);
            if (T + 1 < 8) MFMA(kf[15 - s], xq[n % 3], acc[T + 1]);
            if (s == 7) {
                if (T + 2 < 8) MFMA(kf[16], xq[n % 3], acc[T + 2]);
                if (T + 4 < 8) MFMA(kb, xq[n % 3], acc[T + 4]);
            }
            if (s == 1 && T > TA) {
                epi_a(acc[T - 1], sg + 512 * (8 - T));
                if (T == 2 || T >= 6) flag_set(sf + 32 * (T == 2 ? 0 : T - 4));
            }
            SB();
        }
    }
    if (TA == 4) {
#pragma unroll
        for (int i = 0; i < 4; ++i) {
            float s = acc[7][i] + a7b[i];
            asm volatile("" : "+v"(s));
            acc[7][i] = s;
        }
    }
    epi_a(acc[TA + 3], sg + 512 * (4 - TA));
    flag_set(sf + 32 * (TA == 0 ? 1 : 4));
}

__global__ __launch_bounds__(1024) void fused_kernel(
    const uint16_t* __restrict__ u, const uint16_t* __restrict__ logA, const uint16_t* __restrict__ Aim,
    const uint16_t* __restrict__ Cre, const uint16_t* __restrict__ Cim, const uint16_t* __restrict__ logdt,
    const uint16_t* __restrict__ Dsk, const uint16_t* __restrict__ qin, const uint16_t* __restrict__ kin,
    const uint16_t* __restrict__ vin, const uint16_t* __restrict__ shk, const uint16_t* __restrict__ ssmk,
    const uint16_t* __restrict__ sD, uint16_t* __restrict__ out)
{
    __shared__ __attribute__((aligned(16))) char lds[LDS_TOTAL];
    uint64_t pc0;
    asm volatile("s_getpc_b64 %0" : "=s"(pc0));
    uint32_t pf = 0;
    if (threadIdx.x < CODE_BYTES / 128) pf = GLD(uint32_t, (gc_t)(pc0 & ~63ull), threadIdx.x * 128u, 0);
    SB();
    STDECL;
    RT(0, 0);
    STAMP(0, 0);
    STAMP(1, 0);
    const int b = blockIdx.x;
    const int xcd = b & 7, idx = b >> 3, part = idx >> 4, gi = idx & 15, grp = xcd * 16 + gi;
    const int lane = threadIdx.x & 63;
    const int w = __builtin_amdgcn_readfirstlane(threadIdx.x >> 6);
    const lds_t flags = (lds_t)lds + FLAG_OFF;
    const int g = lane >> 4, fr = lane & 15;
    {
        const u32x4 z = {0, 0, 0, 0};
        char* zp = w < 8 ? lds + w * CH_STRIDE + (lane < 4 ? XA_OFF + 4096 : XB_OFF + 4080 - 64) + 16 * lane : lds + FLAG_OFF + 16 * lane;
        if (lane < (w < 8 ? 9 : 19)) *(u32x4*)zp = z;
    }
    KEEPS(u); KEEPS(logA); KEEPS(Aim); KEEPS(Cre); KEEPS(Cim); KEEPS(logdt); KEEPS(Dsk);
    KEEPS(qin); KEEPS(kin); KEEPS(vin); KEEPS(shk); KEEPS(ssmk); KEEPS(sD); KEEPS(out);
    SB();
    lds_barrier();
    SB();
    if (w >= 12) {
        const int rw = w - 12;
        const int qq = (lane >> 2) & 3, pp = lane & 3;
        const char* rg = lds + qq * CH_STRIDE + KA_OFF + 464 + 3584 + (64 * rw + 16 * g + 4 * pp) * 2;
        const uint32_t r0 = 255 - (64 * rw + lane);
        uint32_t oo = r0 * (4 * DCH) + part * (2 * DCH) + grp * 16;
        OPAQUE(oo);
#define ROW_OUT(T) (*(__attribute__((address_space(1))) u32x4*)((gw_t)out + (uint32_t)(oo + (T) * (256u * 4 * DCH))))
#define ROW_WAIT(S) flag_wait(flags + 128 + 32 * (S) + 4 * (lane & 7))
        if (part) {
            uint32_t qo = r0 * (2 * DCH) + grp * 16;
            OPAQUE(qo);
            u32x4 qv[8];
#define LDQ(T) qv[T] = GLD(u32x4, (gc_t)qin, (uint32_t)(qo + (T) * (256u * 2 * DCH)), 0)
#define ROWB(T) ROW_OUT(T) = gate(row_read(rg - 512 * (T)), qv[T])
            flag_wait(flags + 4 * (lane & 3));
            LDQ(0); LDQ(1); LDQ(2);
            SB();
            STAMP(0, 1);
            ROW_WAIT(0); ROWB(0); SB(); LDQ(3); LDQ(4); SB();
            STAMP(0, 2);
            ROW_WAIT(1); ROWB(1); ROWB(2); SB(); LDQ(5); LDQ(6); LDQ(7); SB();
            STAMP(0, 3);
            ROW_WAIT(2); ROWB(3); ROWB(4); SB();
            STAMP(0, 4);
            ROW_WAIT(3); ROWB(5); ROWB(6); SB();
            STAMP(0, 5);
            ROW_WAIT(4); ROWB(7);
        } else {
            const uint32_t t = gi * 64 + lane;
            const gc_t pkv = (gc_t)((t & 2) ? vin : kin) + ((t >> 2) * (2 * DCH) + 256 * xcd + 128 * (t & 1));
            const uint32_t ouq = (t >> 1) * (2 * DCH) + 256 * xcd + 128 * (t & 1);
            const gc_t pu = (gc_t)u + ouq, pq = (gc_t)qin + ouq;
            uint64_t seq = rw == 0 ? SEQ0 : rw == 1 ? SEQ1 : rw == 2 ? SEQ2 : SEQ3;
            for (int d = rw == 0 ? DL0 : rw == 1 ? DL1 : rw == 2 ? DL2 : DL3; d > 0; --d) __builtin_amdgcn_s_sleep(8);
#pragma unroll 1
            for (uint32_t c; (c = (uint32_t)seq & 0xffu) != 0xffu; seq >>= 8) {
                const gc_t a = c < 8 ? pkv + (size_t)c * (256 * 2 * DCH) : (c < 12 ? pu : pq) + (size_t)(c & 3) * (512 * 2 * DCH);
                const uint32_t pv = GLD(uint32_t, a, 0, 0);
                SB(); KEEP(pv); SB();
                if ((c >> 2) == 2) flag_set(flags + 288 + 4 * (c & 3));
            }
#define ROWA(T) ROW_OUT(T) = row_read(rg - 512 * (T))
            STAMP(0, 1);
            ROW_WAIT(0); ROWA(0); ROWA(1); SB();
            STAMP(0, 2);
            ROW_WAIT(1); ROWA(2); ROWA(3); SB();
            STAMP(0, 3);
            ROW_WAIT(2); ROWA(4); ROWA(5); SB();
            STAMP(0, 4);
            ROW_WAIT(3); ROWA(6); SB();
            STAMP(0, 5);
            ROW_WAIT(4); ROWA(7);
        }
        STAMP(1, 6);
        STAMPW(1, 7);
        RT(0, 1);
        return;
    }
    if (w >= 8) {
        const int pw = w - 8, c2 = lane & 3, rq = lane >> 2;
        const int s0 = 64 * pw + 4 * rq;
        uint32_t eo = (uint32_t)(s0 + 2) * (2 * DCH) + grp * 16 + 4 * c2;
        OPAQUE(eo);
        char* xp = lds + 2 * c2 * CH_STRIDE + 2 * (2044 - s0);
#define BLK(o, P) ((uint32_t)((o) + (P) * (256u * 2 * DCH)))
        if (part) {
            uint32_t kk[8][4], vv[8][4], hk[8][3];
            uint32_t ho = (uint32_t)(64 * pw + 16 * g - 2) * (2 * DCH) + grp * 16 + 4 * c2;
            OPAQUE(ho);
            const bool hz = (pw | g) == 0;
            const uint32_t ho0 = hz ? ho + 4 * (2 * DCH) : ho;
#define LDK(P) do { _Pragma("unroll") for (int i = 0; i < 4; ++i) kk[P][i] = GLD(uint32_t, (gc_t)kin, BLK(eo, P), (i - 2) * (2 * DCH)); } while (0)
#define LDV(P) do { _Pragma("unroll") for (int i = 0; i < 4; ++i) vv[P][i] = GLD(uint32_t, (gc_t)vin, BLK(eo, P), (i - 2) * (2 * DCH)); } while (0)
#define LDH(P) do { _Pragma("unroll") for (int i = 0; i < 3; ++i) hk[P][i] = GLD(uint32_t, (gc_t)kin, ((P) ? BLK(ho, P) : ho0), (i - 1) * (2 * DCH)); } while (0)
            const u32x4 wv = GLD(u32x4, (gc_t)shk, (uint32_t)(grp * 8 + 2 * c2) * 8, 0);
#pragma unroll
            for (int p = 0; p < NUP; ++p) { LDK(p); LDH(p); LDV(p); SB(); }
            const float wt[2][4] = {{lo_f(wv[0]), hi_f(wv[0]), lo_f(wv[1]), hi_f(wv[1])}, {lo_f(wv[2]), hi_f(wv[2]), lo_f(wv[3]), hi_f(wv[3])}};
#pragma unroll
            for (int i = 0; i < 3; ++i) hk[0][i] = hz ? 0u : hk[0][i];
            STAMP(0, 1);
#pragma unroll
            for (int p = 0; p < 8; ++p) {
                uint32_t kp[7];
#pragma unroll
                for (int i = 0; i < 3; ++i)
                    kp[i] = (uint32_t)__builtin_amdgcn_update_dpp((int)hk[p][i], (int)kk[p][1 + i], 0x114  , 0xf, 0xf, false);
#pragma unroll
                for (int i = 0; i < 4; ++i) kp[3 + i] = kk[p][i];
#pragma unroll
                for (int h = 0; h < 2; ++h) {
                    float kf[7], vf[4];
#pragma unroll
                    for (int i = 0; i < 7; ++i) kf[i] = h ? hi_f(kp[i]) : lo_f(kp[i]);
#pragma unroll
                    for (int i = 0; i < 4; ++i) vf[i] = h ? hi_f(vv[p][i]) : lo_f(vv[p][i]);
                    uint32_t o0, o1;
                    kv4(kf, wt[h], vf, o0, o1);
                    put_x4(xp + h * CH_STRIDE - 512 * p, o0, o1);
                    SB();
                    if (h == 0 && p + 3 < 8 && p >= LATE) { LDK(p + 3); LDH(p + 3); }
                    SB();
                }
                flag_set(flags + 16 * p + 4 * pw);
                SB();
                if (p == 0) { if (NUP < 2) { LDK(1); LDH(1); LDV(1); } LDK(2); LDH(2); LDV(2); SB(); }
                if (p + 3 < 8 && p < LATE) { LDK(p + 3); LDH(p + 3); }
                if (p + 3 < 8) LDV(p + 3);
                SB();
                if (p < 6) STAMP(0, 2 + p); else STAMP(1, p - 5);
            }
        } else {
            uint32_t uu[8][4];
#define LDU(P) do { _Pragma("unroll") for (int i = 0; i < 4; ++i) uu[P][i] = GLD(uint32_t, (gc_t)u, BLK(eo, P), (i - 2) * (2 * DCH)); SB(); } while (0)
#define PUTU(P) do { _Pragma("unroll") for (int h = 0; h < 2; ++h) { \
                    const uint32_t sel = h ? 0x07060302u : 0x05040100u; \
                    *(u32x2*)(xp + h * CH_STRIDE - 512 * (P) + XA_OFF) = (u32x2){__builtin_amdgcn_perm(uu[P][2], uu[P][3], sel), __builtin_amdgcn_perm(uu[P][0], uu[P][1], sel)}; } \
                flag_set(flags + 16 * (P) + 4 * pw); SB(); \
                if ((P) < 6) STAMP(0, 2 + (P)); else STAMP(1, (P) - 5); } while (0)
#pragma unroll 1
            for (int ci = pw; ci < 5; ci += 4) {
                const uint32_t lo = (uint32_t)((0x121413151aull >> (8 * ci)) & 0xff), span = (uint32_t)((0x0106080402ull >> (8 * ci)) & 0xff);
                const uint32_t one = ci < 4 ? 0x3f80u : 0xffffu, z0 = fr + 8 * g - lo;
                u32x4 cv;
#pragma unroll
                for (int q = 0; q < 4; ++q) cv[q] = (z0 + 2 * q <= span ? one : 0u) | (z0 + 2 * q + 1 <= span ? one << 16 : 0u);
                *(u32x4*)(lds + (ci & 3) * CH_STRIDE + CW_OFF + 1024 * (ci >> 2) + 16 * lane) = cv;
            }
            flag_wait(flags + 288);
            STAMP(0, 1);
            LDU(0); LDU(1);
            PUTU(0); PUTU(1);
            flag_wait(flags + 292);
            LDU(2); LDU(3);
            PUTU(2); PUTU(3);
            flag_wait(flags + 296);
            LDU(4); LDU(5);
            PUTU(4); PUTU(5);
            flag_wait(flags + 300);
            LDU(6); LDU(7);
            PUTU(6); PUTU(7);
        }
        RT(0, 1);
        return;
    }

    const int chl = grp * 8 + w;
    char* cb = lds + w * CH_STRIDE;
    u32x4 kq[4];
    if (part) {
        const uint16_t* ksrc = ssmk + (size_t)chl * LSEQ;
        if (D_SSM) __builtin_amdgcn_s_sleep(D_SSM);
#pragma unroll
        for (int i = 0; i < 4; ++i) kq[i] = *(const u32x4*)(ksrc + 8 * (64 * i + lane));
        uint32_t dr = sD[chl];
        SB();
        KEEP(pf);
        OPAQUE(dr);
        kq[0][0] = pk_bf16(lo_f(kq[0][0]) + (lane ? 0.f : lo_f(dr)), hi_f(kq[0][0]));
    } else {
        float dt = bf2f(logdt[chl]);
        uint32_t pr[8];
#pragma unroll
        for (int bt = 0; bt < 2; ++bt) {
            const int n = chl * NMODE + bt * 64 + lane;
            pr[4 * bt] = logA[n]; pr[4 * bt + 1] = Aim[n]; pr[4 * bt + 2] = Cre[n]; pr[4 * bt + 3] = Cim[n];
        }
        uint32_t dr = Dsk[chl];
        SB();
        {
            const u32x4 z = {0, 0, 0, 0};
            *(u32x4*)(cb + XA_OFF + 4096 + 16 * lane) = z;
            if (lane < 18) *(u32x4*)(cb + XA_OFF + 5120 + 16 * lane) = z;
        }
        SB();
        dt = rbf(expf(dt));
        float pa[2], pb[2], pcr[2], pci[2];
        float mx = 0.f;
#pragma unroll
        for (int bt = 0; bt < 2; ++bt) {
            const float ar = -rbf(expf(lo_f(pr[4 * bt])));
            pa[bt] = (ar * dt) * 1.4426950408889634f;
            pb[bt] = (lo_f(pr[4 * bt + 1]) * dt) * 0.15915494309189535f;
            pcr[bt] = 2.f * (lo_f(pr[4 * bt + 2]) * dt);
            pci[bt] = 2.f * (lo_f(pr[4 * bt + 3]) * dt);
            mx = fmaxf(mx, fmaxf(fabsf(pcr[bt]), fabsf(pci[bt])));
        }
        KEEP(pf);
        OPAQUE(dr);
        const float Dv = lane ? 0.f : lo_f(dr);
        int mi = (int)__float_as_uint(mx);
#define DPPMAX(ctrl) mi = max(mi, __builtin_amdgcn_update_dpp(0, mi, ctrl, 0xf, 0xf, false))
        DPPMAX(0x111); DPPMAX(0x112); DPPMAX(0x114); DPPMAX(0x118); DPPMAX(0x142); DPPMAX(0x143);
        int sf = 266 - ((__builtin_amdgcn_readlane(mi, 63) >> 23) & 0xff);
        sf = sf < 1 ? 1 : (sf > 239 ? 239 : sf);
        const float scl = __uint_as_float((uint32_t)sf << 23);
        const float inv = __uint_as_float((uint32_t)(240 - sf) << 23);
        STAMP(0, 1);

        char* Tl = cb + lane * 8;
        const char* Tr = cb + fr * KG_ROWB + g * 16;
#define TW(row, v0, v1) *(u32x2*)(Tl + (row) * KG_ROWB) = (u32x2){pk16(v0), pk16(v1)}
#define B2(X) do { _Pragma("unroll") for (int bt = 0; bt < 2; ++bt) { X; } } while (0)
        f16x8 bfr[8], af[8];
        f32x2 z8c[2], z1[2], z128[2];
        B2(z8c[bt] = anchor(pa[bt] * 8.f, pb[bt] * -8.f));
        B2(z1[bt] = anchor(pa[bt], pb[bt]));
        B2(z128[bt] = anchor(pa[bt] * 128.f, pb[bt] * 128.f));
        f32x2 f[2][16], h[2][8], cg[2][17], zs[2][3], zl[2][3], t[2][8];
        B2(t[bt][0] = cm1(z8c[bt], z8c[bt]); t[bt][1] = cm1(z128[bt], z128[bt]); t[bt][2] = cm1(z1[bt], z1[bt]));
        B2(cg[bt][0] = ((f32x2){pcr[bt] * scl, pci[bt] * scl}); f[bt][1] = z8c[bt] * 16384.f);
        SB();
        B2(zs[bt][0] = cm2(z8c[bt], z8c[bt], t[bt][0]); zl[bt][0] = cm2(z128[bt], z128[bt], t[bt][1]); h[bt][2] = cm2(z1[bt], z1[bt], t[bt][2]));
        B2(t[bt][3] = cm1(cg[bt][0], z128[bt]));
        SB();
        B2(t[bt][0] = cm1(zs[bt][0], zs[bt][0]); t[bt][1] = cm1(zl[bt][0], zl[bt][0]); t[bt][2] = cm1(h[bt][2], h[bt][2]);
           t[bt][4] = cm1(h[bt][2], z1[bt]); t[bt][5] = cm1(f[bt][1], zs[bt][0]); t[bt][6] = cm1(cg[bt][0], zl[bt][0]);
           cg[bt][1] = cm2(cg[bt][0], z128[bt], t[bt][3]); f[bt][2] = zs[bt][0] * 16384.f);
        SB();
        B2(zs[bt][1] = cm2(zs[bt][0], zs[bt][0], t[bt][0]); zl[bt][1] = cm2(zl[bt][0], zl[bt][0], t[bt][1]); h[bt][4] = cm2(h[bt][2], h[bt][2], t[bt][2]);
           h[bt][3] = cm2(h[bt][2], z1[bt], t[bt][4]); f[bt][3] = cm2(f[bt][1], zs[bt][0], t[bt][5]); cg[bt][2] = cm2(cg[bt][0], zl[bt][0], t[bt][6]);
           t[bt][3] = cm1(cg[bt][1], zl[bt][0]));
        SB();
        B2(t[bt][0] = cm1(zs[bt][1], zs[bt][1]); t[bt][1] = cm1(zl[bt][1], zl[bt][1]);
           t[bt][4] = cm1(f[bt][1], zs[bt][1]); t[bt][5] = cm1(f[bt][2], zs[bt][1]); t[bt][6] = cm1(f[bt][3], zs[bt][1]);
           cg[bt][3] = cm2(cg[bt][1], zl[bt][0], t[bt][3]); f[bt][4] = zs[bt][1] * 16384.f);
        SB();
        B2(zs[bt][2] = cm2(zs[bt][1], zs[bt][1], t[bt][0]); zl[bt][2] = cm2(zl[bt][1], zl[bt][1], t[bt][1]);
           f[bt][5] = cm2(f[bt][1], zs[bt][1], t[bt][4]); f[bt][6] = cm2(f[bt][2], zs[bt][1], t[bt][5]); f[bt][7] = cm2(f[bt][3], zs[bt][1], t[bt][6]));
        B2(f[bt][0] = ((f32x2){16384.f, 0.f}));
#pragma unroll
        for (int i = 0; i < 5; ++i) TW(i, f[0][i], f[1][i]);
        SB();
        B2(_Pragma("unroll") for (int i = 1; i < 8; ++i) t[bt][i] = cm1(f[bt][i], zs[bt][2]); f[bt][8] = zs[bt][2] * 16384.f);
#pragma unroll
        for (int i = 5; i < 8; ++i) TW(i, f[0][i], f[1][i]);
        SB();
        B2(_Pragma("unroll") for (int i = 1; i < 8; ++i) f[bt][8 + i] = cm2(f[bt][i], zs[bt][2], t[bt][i]));
        SB();
        B2(t[bt][0] = cm1(h[bt][4], z1[bt]); t[bt][1] = cm1(h[bt][4], h[bt][2]); t[bt][2] = cm1(h[bt][4], h[bt][3]));
#pragma unroll
        for (int i = 8; i < 16; ++i) TW(i, f[0][i], f[1][i]);
#pragma unroll
        for (int ks = 0; ks < 8; ++ks) bfr[ks] = *(const f16x8*)(Tr + ks * 64);
        B2(h[bt][1] = z1[bt]; h[bt][5] = cm2(h[bt][4], z1[bt], t[bt][0]); h[bt][6] = cm2(h[bt][4], h[bt][2], t[bt][1]); h[bt][7] = cm2(h[bt][4], h[bt][3], t[bt][2]));
        SB();
        f32x4 ac[3][2];
#define TRD() do { _Pragma("unroll") for (int ks = 0; ks < 8; ++ks) af[ks] = *(const f16x8*)(Tr + ks * 64); SB(); } while (0)
#define TMM(rt) do { \
            ac[rt][0] = (f32x4){0, 0, 0, 0}; ac[rt][1] = (f32x4){0, 0, 0, 0}; \
            _Pragma("unroll") for (int ks = 0; ks < 8; ++ks) \
                ac[rt][ks & 1] = __builtin_amdgcn_mfma_f32_16x16x32_f16(af[ks], bfr[ks], ac[rt][ks & 1], 0, 0, 0); SB(); } while (0)
#pragma unroll
        for (int aa = 0; aa < 2; ++aa) {
            TW(8 * aa, cg[0][aa], cg[1][aa]);
#pragma unroll
            for (int e0 = 1; e0 < 8; e0 += 4) {
                B2(_Pragma("unroll") for (int e = e0; e < e0 + 4 && e < 8; ++e) t[bt][e] = cm1(cg[bt][aa], h[bt][e]));
                SB();
                B2(_Pragma("unroll") for (int e = e0; e < e0 + 4 && e < 8; ++e) t[bt][e] = cm2(cg[bt][aa], h[bt][e], t[bt][e]));
                SB();
#pragma unroll
                for (int e = e0; e < e0 + 4 && e < 8; ++e) TW(8 * aa + e, t[0][e], t[1][e]);
                SB();
            }
        }
        TRD();
        uint16_t* KD = (uint16_t*)(lds + KD_OFF + w * 1536);
#define KDW(rt) do { \
            f32x4 v = (ac[rt][0] + ac[rt][1]) * inv; \
            if ((rt) == 0) v[0] += Dv; \
            _Pragma("unroll") for (int r = 0; r < 4; r += 2) { \
                uint32_t p = pk_bf16(v[r], v[r + 1]); \
                if ((rt) < 2) { \
                    KD[(16 * (rt) + 4 * g + r) * 16 + fr] = (uint16_t)p; \
                    KD[(16 * (rt) + 4 * g + r + 1) * 16 + fr] = (uint16_t)(p >> 16); \
                    if ((rt) == 1) *(uint32_t*)((lane >= (r ? 32 : 34) ? cb + L2_OFF + 128 * g + 4 * fr - 140 : (char*)KD + 1024) + 32 * r) = p; \
                } else { \
                    char* a3 = lane < 32 ? cb + L3_OFF + 58 + 128 * g + 2 * fr : (char*)KD + 1024; \
                    if (r == 0) { \
                        kk = (uint32_t)__builtin_amdgcn_readlane((int)p, 0) & 0xffffu; kk |= kk << 16; \
                        *(uint16_t*)(lane == 0 ? cb + L2_OFF + 372 : lane == 32 ? cb + L3_OFF + 314 : a3) = (uint16_t)p; \
                    } else *(uint16_t*)(a3 + 32 * r) = (uint16_t)p; \
                    *(uint16_t*)(a3 + 32 * r + 32) = (uint16_t)(p >> 16); \
                } } } while (0)
        uint32_t kk = 0;
#pragma unroll
        for (int aa = 2; aa < 4; ++aa) {
            B2(_Pragma("unroll") for (int e = 1; e < 4; ++e) t[bt][e] = cm1(cg[bt][aa], h[bt][2 * e]));
            TW(4 * (aa - 2), cg[0][aa], cg[1][aa]);
            SB();
            B2(_Pragma("unroll") for (int e = 1; e < 4; ++e) t[bt][e] = cm2(cg[bt][aa], h[bt][2 * e], t[bt][e]));
            SB();
#pragma unroll
            for (int e = 1; e < 4; ++e) TW(4 * (aa - 2) + e, t[0][e], t[1][e]);
            SB();
        }
        TMM(0);
        B2(_Pragma("unroll") for (int i = 0; i < 4; ++i) { cg[bt][4 + i] = cm1(cg[bt][i], zl[bt][1]); cg[bt][8 + i] = cm1(cg[bt][i], zl[bt][2]); });
        SB();
        B2(_Pragma("unroll") for (int i = 0; i < 4; ++i) { cg[bt][4 + i] = cm2(cg[bt][i], zl[bt][1], cg[bt][4 + i]); cg[bt][8 + i] = cm2(cg[bt][i], zl[bt][2], cg[bt][8 + i]); });
        KDW(0);
        SB();
        B2(_Pragma("unroll") for (int i = 0; i < 4; ++i) t[bt][i] = cm1(cg[bt][4 + i], h[bt][4]); cg[bt][16] = cm1(cg[bt][8], zl[bt][2]));
#pragma unroll
        for (int i = 0; i < 4; ++i) TW(8 + 2 * i, cg[0][4 + i], cg[1][4 + i]);
        SB();
        B2(_Pragma("unroll") for (int i = 0; i < 4; ++i) t[bt][i] = cm2(cg[bt][4 + i], h[bt][4], t[bt][i]); cg[bt][16] = cm2(cg[bt][8], zl[bt][2], cg[bt][16]));
        SB();
#pragma unroll
        for (int i = 0; i < 4; ++i) TW(9 + 2 * i, t[0][i], t[1][i]);
        SB();
        TRD();
        B2(_Pragma("unroll") for (int i = 0; i < 4; ++i) cg[bt][12 + i] = cm1(cg[bt][8 + i], zl[bt][1]));
        TW(8, cg[0][16], cg[1][16]);
        SB();
        B2(_Pragma("unroll") for (int i = 0; i < 4; ++i) cg[bt][12 + i] = cm2(cg[bt][8 + i], zl[bt][1], cg[bt][12 + i]));
#pragma unroll
        for (int aa = 8; aa < 12; ++aa) TW(aa - 8, cg[0][aa], cg[1][aa]);
        SB();
#pragma unroll
        for (int aa = 12; aa < 16; ++aa) TW(aa - 8, cg[0][aa], cg[1][aa]);
        SB();
        TMM(1);
        TRD();
        KDW(1);
        SB();
        TMM(2);
        STAMP(0, 2);
        KDW(2);
        int tid = threadIdx.x;
        asm volatile("" : "+v"(tid));
        {
            const int lane = tid & 63, g = lane >> 4, fr = lane & 15;
            const int hi = lane >> 5, last = (fr == 15);
            const uint16_t* p0 = KD + (hi ? 16 + 4 * (g - 2) : 8 * g) * 16 + fr;
            const uint16_t* p1 = p0 - 16 * hi, * p2 = p0 - 32 * hi, * p3 = p0 - 48 * hi;
            const uint16_t* p7 = p0 + (hi ? (last ? 49 : 1) : 112);
            kq[0][0] = (uint32_t)p0[0] | ((uint32_t)p1[16] << 16);
            kq[0][1] = (uint32_t)p1[32] | ((uint32_t)p1[48] << 16);
            kq[0][2] = (uint32_t)p2[64] | ((uint32_t)p3[80] << 16);
            kq[0][3] = (uint32_t)p3[96] | ((uint32_t)p7[0] << 16);
            const uint16_t* q0 = KD + 24 * 16 + (lane & 1);
            const uint32_t a = q0[0], bq = q0[16], c = q0[1];
            const uint32_t s0 = lane < 2 ? 0x01000100u : 0x0c0c0c0cu;
            kq[1][0] = __builtin_amdgcn_perm(a, a, s0);
            kq[1][1] = __builtin_amdgcn_perm(bq, a, lane < 2 ? 0x05040100u : 0x0c0c0c0cu);
            kq[1][2] = __builtin_amdgcn_perm(bq, bq, s0);
            kq[1][3] = __builtin_amdgcn_perm(c, c, lane < 1 ? 0x01000100u : 0x0c0c0c0cu);
            put_k<LA_OFF + 32, LB_OFF + 32>(cb, lane, kq[0], prev_lane(kq[0][3], 0));
            const uint32_t pv = prev_lane(kq[1][3], __builtin_amdgcn_readlane(kq[0][3], 63));
            if (lane < 4) {
                put_k<LA_OFF + 32, LB_OFF + 32>(cb, 64 + lane, kq[1], pv);
                *(u32x4*)(cb + ((lane & 2) ? LB_OFF : LA_OFF) + 16 * (lane & 1)) = (u32x4){0, 0, 0, 0};
            }
        }
        STAMP(0, 3);
        {
            const lds_t lb = (lds_t)lds + w * CH_STRIDE, sf = flags + 128 + 4 * w;
            f32x4 acc[8];
#pragma unroll
            for (int i = 0; i < 8; ++i) acc[i] = (f32x4){0, 0, 0, 0};
            OPAQUE(tid);
            flag_wait(flags + 4 * (tid & 7));
            STAMP(0, 4);
            far_field<0, 1>((lds_t)lds, lb, tid, acc);
            STAMP(0, 5);
            near_field<0>((lds_t)lds, lb, tid, kk, sf, acc);
            STAMP(0, 6);
            OPAQUE(tid);
            flag_wait(flags + 64 + 4 * (tid & 7));
            STAMP(0, 7);
            far_field<2, 5>((lds_t)lds, lb, tid, acc);
            STAMP(1, 1);
            near_field<4>((lds_t)lds, lb, tid, kk, sf, acc);
            STAMP(1, 6);
            RT(0, 1);
        }
        return;
    }
    int tid2 = threadIdx.x;
    asm volatile("" : "+v"(tid2));
#define lane (tid2 & 63)
#define g ((tid2 >> 4) & 3)
#define fr (tid2 & 15)
    {
        const u32x4 z = {0, 0, 0, 0};
        if (lane < 29) {
            *(u32x4*)(cb + KA_OFF + 16 * lane) = z;
            *(u32x4*)(cb + KB_OFF + 16 * lane) = z;
        }
        uint32_t c = 0;
#pragma unroll
        for (int i = 0; i < 4; ++i) {
            put_k<KA_OFF + 464, KB_OFF + 464>(cb, 64 * i + lane, kq[i], prev_lane(kq[i][3], c));
            c = __builtin_amdgcn_readlane(kq[i][3], 63);
        }
    }
    STAMP(0, 3);

    const lds_t lb = (lds_t)lds + w * CH_STRIDE;
    const lds_t kaddr = lb + ((fr & 1) ? KA_OFF : KB_OFF) + 2 * (32 * (fr >> 1) + 8 * g + 8);
    lds_t xaddr = lb + ((fr & 1) ? (XA_OFF + 2 * (1822 - 2 * fr + 8 * g)) : (XB_OFF + 2 * (1820 - 2 * fr + 8 * g))) - 3584;
    asm volatile("" : "+v"(xaddr));
    const int lo_ = 64 * g + 2 * fr;
    char* sg = cb + KA_OFF + 464 + 2 * (2046 - lo_);
#define EPI(T) epi_acc(acc[T], sg - 512 * (T))
#define SFLAG(S) flag_set(flags + 128 + 32 * (S) + 4 * w)

    f32x4 acc[8];
#pragma unroll
    for (int i = 0; i < 8; ++i) acc[i] = (f32x4){0, 0, 0, 0};

    flag_wait(flags + 4 * (lane & 3));
    STAMP(0, 4);
    stage<0, 1>(kaddr, xaddr, acc);
    STAMP(0, 5);
    EPI(0); SFLAG(0);
    flag_wait(flags + 16 + 4 * (lane & 7));
    STAMP(0, 6);
    stage<1, 2>(kaddr, xaddr, acc);
    STAMP(0, 7);
    EPI(1); EPI(2); SFLAG(1);
    flag_wait(flags + 48 + 4 * (lane & 7));
    STAMP(1, 1);
    stage<3, 2>(kaddr, xaddr, acc);
    STAMP(1, 2);
    EPI(3); EPI(4); SFLAG(2);
    flag_wait(flags + 80 + 4 * (lane & 7));
    STAMP(1, 3);
    stage<5, 2>(kaddr, xaddr, acc);
    STAMP(1, 4);
    EPI(5); EPI(6); SFLAG(3);
    flag_wait(flags + 112 + 4 * (lane & 3));
    STAMP(1, 5);
    {
        f32x4 a7b = {0, 0, 0, 0};
#pragma unroll
        for (int c = 0; c < 8; c += 2) {
            MFMA(ldk(kaddr + 64 * c), ldx(xaddr + 64 * c), acc[7]);
            MFMA(ldk(kaddr + 64 * c + 64), ldx(xaddr + 64 * c + 64), a7b);
        }
#pragma unroll
        for (int i = 0; i < 4; ++i) {
            float s = acc[7][i] + a7b[i];
            asm volatile("" : "+v"(s));
            acc[7][i] = s;
        }
    }
    EPI(7); SFLAG(4);
    STAMP(1, 6);
    RT(0, 1);
}

extern "C" void kernel_launch(void* const* d_in, const int* in_sizes, int n_in,
                              void* d_out, int out_size, void* d_ws, size_t ws_size,
                              hipStream_t stream) {
    fused_kernel<<<256, 1024, 0, stream>>>(
        (const uint16_t*)d_in[0], (const uint16_t*)d_in[1], (const uint16_t*)d_in[2], (const uint16_t*)d_in[3],
        (const uint16_t*)d_in[4], (const uint16_t*)d_in[5], (const uint16_t*)d_in[6], (const uint16_t*)d_in[7],
        (const uint16_t*)d_in[8], (const uint16_t*)d_in[9], (const uint16_t*)d_in[10], (const uint16_t*)d_in[11],
        (const uint16_t*)d_in[12], (uint16_t*)d_out);
}
```

```cpp
#include <hip/hip_runtime.h>
#include <stdint.h>

typedef __attribute__((ext_vector_type(4))) unsigned int u32x4;
typedef __attribute__((ext_vector_type(2))) unsigned int u32x2;
typedef __attribute__((ext_vector_type(4))) float f32x4;
typedef __attribute__((ext_vector_type(2))) float f32x2;
typedef __attribute__((ext_vector_type(8))) _Float16 f16x8;
typedef __attribute__((ext_vector_type(2))) _Float16 f16x2;
typedef __attribute__((ext_vector_type(8))) __bf16 bf16x8;
typedef __attribute__((ext_vector_type(2))) __bf16 bf16x2;
typedef __attribute__((ext_vector_type(4))) short s16x4;

#define STAMP(k, i)
#define STAMPW(k, i)
#define RT(k, i)
#define STDECL
#define LSEQ 2048
#define DCH 1024
#define NMODE 128

__device__ __forceinline__ float bf2f(uint16_t v) { return __uint_as_float(((uint32_t)v) << 16); }
__device__ __forceinline__ float lo_f(uint32_t w) { return __uint_as_float(w << 16); }
__device__ __forceinline__ float hi_f(uint32_t w) { return __uint_as_float(w & 0xffff0000u); }
__device__ __forceinline__ uint32_t pk_bf16(float lo, float hi) {
    f32x2 x = {lo, hi};
    bf16x2 y = __builtin_convertvector(x, bf16x2);
    return __builtin_bit_cast(uint32_t, y);
}
__device__ __forceinline__ uint32_t pk_f16(float lo, float hi) {
    f32x2 x = {lo, hi};
    f16x2 y = __builtin_convertvector(x, f16x2);
    return __builtin_bit_cast(uint32_t, y);
}
__device__ __forceinline__ float rbf(float a) { return lo_f(pk_bf16(a, a)); }

#define KEEP(v) asm volatile("" :: "v"(v))
#define KEEPS(v) asm volatile("" :: "s"(v))
#define OPAQUE(v) asm volatile("" : "+v"(v))
#define SB() __builtin_amdgcn_sched_barrier(0)
__device__ __forceinline__ f32x2 cm1(f32x2 a, f32x2 b) {
    f32x2 t;
    asm("v_pk_mul_f32 %0, %1, %2 op_sel:[0,0] op_sel_hi:[0,1]" : "=v"(t) : "v"(a), "v"(b));
    return t;
}
__device__ __forceinline__ f32x2 cm2(f32x2 a, f32x2 b, f32x2 t) {
    f32x2 c;
    asm("v_pk_fma_f32 %0, %1, %2, %3 op_sel:[1,1,0] op_sel_hi:[1,0,1] neg_lo:[1,0,0]" : "=v"(c) : "v"(a), "v"(b), "v"(t));
    return c;
}
__device__ __forceinline__ f32x2 cmul(f32x2 a, f32x2 b) { return cm2(a, b, cm1(a, b)); }
__device__ __forceinline__ f32x2 anchor(float a, float b) {
    const float e = __builtin_amdgcn_exp2f(a);
    const float th = __builtin_amdgcn_fractf(b);
    return (f32x2){e * __builtin_amdgcn_cosf(th), e * __builtin_amdgcn_sinf(th)};
}
__device__ __forceinline__ uint32_t pk16(f32x2 c) { return pk_f16(c[0], c[1]); }

#define KG_ROWB 544

#define D_PF 32
#define D_SSM 0
#define NUP 1
#define LATE 1
#define SEQ0 0xff0c06040100ull
#define SEQ1 0xff0d070902ull
#define SEQ2 0xff0e0b0508ull
#define SEQ3 0xff0f0a03ull
#define DL0 0
#define DL1 7
#define DL2 9
#define DL3 11
#define KA_OFF 0
#define KB_OFF 4640
#define XA_OFF 9216
#define XB_OFF 13440
#define CH_STRIDE 17696
#define LA_OFF 4640
#define LA_STR 1088
#define L2_OFF (XA_OFF + 4576)
#define L3_OFF (XA_OFF + 5088)
#define CW_OFF (XA_OFF + 5440)
#define KD_OFF (8 * CH_STRIDE)
#define FLAG_OFF (KD_OFF + 8 * 1536)
#define LDS_TOTAL (FLAG_OFF + 304)
#define CODE_BYTES 24320

template <int OA, int OB>
__device__ __forceinline__ void put_k(char* cb, int y, u32x4 d, uint32_t prev) {
    *(u32x4*)(cb + OA + 16 * y) = d;
    u32x4 o;
    o[0] = __builtin_amdgcn_alignbyte(d[0], prev, 2);
    o[1] = __builtin_amdgcn_alignbyte(d[1], d[0], 2);
    o[2] = __builtin_amdgcn_alignbyte(d[2], d[1], 2);
    o[3] = __builtin_amdgcn_alignbyte(d[3], d[2], 2);
    *(u32x4*)(cb + OB + 16 * y) = o;
}
__device__ __forceinline__ void put_k4(char* p, u32x4 d, uint32_t p2, uint32_t p3) {
    const uint32_t o0 = __builtin_amdgcn_alignbyte(d[0], p3, 2), o1 = __builtin_amdgcn_alignbyte(d[1], d[0], 2);
    const uint32_t o2 = __builtin_amdgcn_alignbyte(d[2], d[1], 2), o3 = __builtin_amdgcn_alignbyte(d[3], d[2], 2);
    *(u32x4*)p = d;
    *(u32x4*)(p + LA_STR) = (u32x4){o0, o1, o2, o3};
    *(u32x4*)(p + 2 * LA_STR) = (u32x4){p3, d[0], d[1], d[2]};
    *(u32x4*)(p + 3 * LA_STR) = (u32x4){__builtin_amdgcn_alignbyte(p3, p2, 2), o0, o1, o2};
}
__device__ __forceinline__ void put_x4(char* p, uint32_t o0, uint32_t o1) {
    *(u32x2*)(p + XA_OFF) = (u32x2){o0, o1};
    *(uint32_t*)(p + XB_OFF - 4) = o0;
    *(uint32_t*)(p + XB_OFF) = o1;
}
__device__ __forceinline__ uint32_t prev_lane(uint32_t v, uint32_t carry) {
    return (uint32_t)__builtin_amdgcn_update_dpp((int)carry, (int)v, 0x138  , 0xf, 0xf, false);
}
typedef __attribute__((address_space(3))) char* lds_t;
typedef __attribute__((address_space(3))) const volatile u32x2* lds_v2;
typedef __attribute__((address_space(3))) volatile uint32_t* lds_vu;
typedef __attribute__((address_space(3))) const bf16x8* lds_k8;
typedef __attribute__((address_space(1))) const char* gc_t;
typedef __attribute__((address_space(1))) char* gw_t;
#define GLD(T, base, voff, imm) (*(__attribute__((address_space(1))) const T*)((base) + (voff) + (imm)))
__device__ __forceinline__ bf16x8 ldx(lds_t p) {
    u32x2 a = *(lds_v2)p, b = *(lds_v2)(p + 8);
    u32x4 v = {a[0], a[1], b[0], b[1]};
    return __builtin_bit_cast(bf16x8, v);
}
__device__ __forceinline__ bf16x8 ldk(lds_t p) { return *(lds_k8)p; }
#define MFMA(a, b, c) c = __builtin_amdgcn_mfma_f32_16x16x32_bf16(a, b, c, 0, 0, 0)
#define SG_DS(n) __builtin_amdgcn_sched_group_barrier(0x100, n, 0)
#define SG_MF(n) __builtin_amdgcn_sched_group_barrier(0x008, n, 0)

#define RB(r) "v_cvt_pk_bf16_f32 " r ", 0, " r "\n\t"
__device__ __forceinline__ void kv4(const float (&k)[7], const float (&wt)[4], const float (&v)[4], uint32_t& o0, uint32_t& o1) {
    float a0, a1;
    asm("v_mul_f32 %2, %4, %14\n\t" "v_mul_f32 %3, %5, %14\n\t" "v_mul_f32 %0, %6, %14\n\t" "v_mul_f32 %1, %7, %14\n\t"
        "v_fmac_f32 %2, %5, %13\n\t" "v_fmac_f32 %3, %6, %13\n\t" "v_fmac_f32 %0, %7, %13\n\t" "v_fmac_f32 %1, %8, %13\n\t"
        "v_fmac_f32 %2, %6, %12\n\t" "v_fmac_f32 %3, %7, %12\n\t" "v_fmac_f32 %0, %8, %12\n\t" "v_fmac_f32 %1, %9, %12\n\t"
        "v_fmac_f32 %2, %7, %11\n\t" "v_fmac_f32 %3, %8, %11\n\t" "v_fmac_f32 %0, %9, %11\n\t" "v_fmac_f32 %1, %10, %11\n\t"
        RB("%2") RB("%3") RB("%0") RB("%1")
        "v_mul_f32 %2, %2, %15\n\t" "v_mul_f32 %3, %3, %16\n\t" "v_mul_f32 %0, %0, %17\n\t" "v_mul_f32 %1, %1, %18\n\t"
        "v_cvt_pk_bf16_f32 %0, %1, %0\n\t" "v_cvt_pk_bf16_f32 %1, %3, %2\n\t"
        : "=&v"(o0), "=&v"(o1), "=&v"(a0), "=&v"(a1)
        : "v"(k[0]), "v"(k[1]), "v"(k[2]), "v"(k[3]), "v"(k[4]), "v"(k[5]), "v"(k[6]),
          "v"(wt[0]), "v"(wt[1]), "v"(wt[2]), "v"(wt[3]), "v"(v[0]), "v"(v[1]), "v"(v[2]), "v"(v[3]));
}
__device__ __forceinline__ void epi_acc(const f32x4 a, char* dst) {
    uint32_t o0, o1;
    asm("v_cvt_pk_bf16_f32 %0, %3, %2\n\tv_cvt_pk_bf16_f32 %1, %5, %4" : "=&v"(o0), "=v"(o1) : "v"(a[0]), "v"(a[1]), "v"(a[2]), "v"(a[3]));
    *(uint32_t*)(dst) = o0;
    *(uint32_t*)(dst - 64) = o1;
}
__device__ __forceinline__ u32x4 row_read(const char* a0) {
    s16x4 v0 = __builtin_amdgcn_ds_read_tr16_b64_v4i16((__attribute__((address_space(3))) s16x4*)(a0));
    s16x4 v1 = __builtin_amdgcn_ds_read_tr16_b64_v4i16((__attribute__((address_space(3))) s16x4*)(a0 + 4 * CH_STRIDE));
    u32x2 p0 = __builtin_bit_cast(u32x2, v0), p1 = __builtin_bit_cast(u32x2, v1);
    return (u32x4){p0[0], p0[1], p1[0], p1[1]};
}
__device__ __forceinline__ void gate2(uint32_t y0, uint32_t y1, uint32_t q0, uint32_t q1, uint32_t& o0, uint32_t& o1) {
    float t0, t1, t2, t3;
    asm("v_lshlrev_b32 %2, 16, %6\n\t" "v_lshlrev_b32 %0, 16, %8\n\t" "v_and_b32 %3, 0xffff0000, %6\n\t" "v_and_b32 %1, 0xffff0000, %8\n\t"
        "v_mul_f32 %2, %2, %0\n\t" "v_mul_f32 %3, %3, %1\n\t"
        "v_lshlrev_b32 %4, 16, %7\n\t" "v_lshlrev_b32 %0, 16, %9\n\t" "v_and_b32 %5, 0xffff0000, %7\n\t" "v_and_b32 %1, 0xffff0000, %9\n\t"
        "v_mul_f32 %4, %4, %0\n\t" "v_mul_f32 %5, %5, %1\n\t"
        "v_cvt_pk_bf16_f32 %0, %2, %3\n\t" "v_cvt_pk_bf16_f32 %1, %4, %5\n\t"
        : "=&v"(o0), "=&v"(o1), "=&v"(t0), "=&v"(t1), "=&v"(t2), "=&v"(t3)
        : "v"(y0), "v"(y1), "v"(q0), "v"(q1));
}
__device__ __forceinline__ u32x4 gate(u32x4 y, u32x4 q) {
    uint32_t o0, o1, o2, o3;
    gate2(y[0], y[1], q[0], q[1], o0, o1);
    gate2(y[2], y[3], q[2], q[3], o2, o3);
    return (u32x4){o0, o1, o2, o3};
}
__device__ __forceinline__ void lds_barrier() {
    __builtin_amdgcn_fence(__ATOMIC_RELEASE, "workgroup", "local");
    __builtin_amdgcn_s_barrier();
    __builtin_amdgcn_fence(__ATOMIC_ACQUIRE, "workgroup", "local");
}
__device__ __forceinline__ void flag_set(lds_t p) {
    __builtin_amdgcn_fence(__ATOMIC_RELEASE, "workgroup", "local");
    *(lds_vu)p = 1u;
}
__device__ __forceinline__ void flag_wait(lds_t p) {
    while (__builtin_amdgcn_ballot_w64(*(lds_vu)p == 0u) != 0ull) __builtin_amdgcn_s_sleep(1);
    __builtin_amdgcn_fence(__ATOMIC_ACQUIRE, "workgroup", "local");
}

template <int T0, int NT>
__device__ __forceinline__ void stage(lds_t kaddr, lds_t xaddr, f32x4 (&acc)[8]) {
    constexpr int NK = 8 - T0, ND = NK + 2 * NT;
    bf16x8 ka[NK], xa[NT], kb[NK], xb[NT];
#pragma unroll
    for (int t = 0; t < NT; ++t) xa[t] = ldx(xaddr + 512 * (7 - T0 - t));
#pragma unroll
    for (int m = 0; m < NK; ++m) ka[m] = ldk(kaddr + 512 * m);
#pragma unroll 1
    for (int c0 = 0; c0 < 8; c0 += 2) {
        const lds_t kp = kaddr + 64 * c0, xp = xaddr + 64 * c0;
#pragma unroll
        for (int t = 0; t < NT; ++t) xb[t] = ldx(xp + 64 + 512 * (7 - T0 - t));
#pragma unroll
        for (int m = 0; m < NK; ++m) kb[m] = ldk(kp + 64 + 512 * m);
#pragma unroll
        for (int t = 0; t < NT; ++t)
#pragma unroll
            for (int m = 0; m < NK - t; ++m) MFMA(ka[m], xa[t], acc[T0 + t + m]);
#pragma unroll
        for (int i = 0; i < ND; ++i) { SG_MF(1); SG_DS(1); }
        SG_MF(NK);
        SB();
#pragma unroll
        for (int t = 0; t < NT; ++t) xa[t] = ldx(xp + 128 + 512 * (7 - T0 - t));
#pragma unroll
        for (int m = 0; m < NK; ++m) ka[m] = ldk(kp + 128 + 512 * m);
#pragma unroll
        for (int t = 0; t < NT; ++t)
#pragma unroll
            for (int m = 0; m < NK - t; ++m) MFMA(kb[m], xb[t], acc[T0 + t + m]);
#pragma unroll
        for (int i = 0; i < ND; ++i) { SG_MF(1); SG_DS(1); }
        SG_MF(NK);
        SB();
    }
}

typedef u32x2 __attribute__((aligned(4))) u32x2_a4;
typedef u32x4 __attribute__((aligned(8))) u32x4_a8;
__device__ __forceinline__ bf16x8 ld_a4(lds_t p) {
    const u32x2 a = *(__attribute__((address_space(3))) const u32x2_a4*)p, b = *(__attribute__((address_space(3))) const u32x2_a4*)(p + 8);
    return __builtin_bit_cast(bf16x8, ((u32x4){a[0], a[1], b[0], b[1]}));
}
__device__ __forceinline__ bf16x8 ld_a8(lds_t p) { return __builtin_bit_cast(bf16x8, (u32x4)*(__attribute__((address_space(3))) const u32x4_a8*)p); }
#define MF0(a, b) __builtin_amdgcn_mfma_f32_16x16x32_bf16(a, b, ((f32x4){0, 0, 0, 0}), 0, 0, 0)
#define XFRAG(sig) ldk(xb + 64 * (56 - (sig)))
template <int TA, int TB>
__device__ __forceinline__ void far_field(lds_t l0, lds_t lb, int tid, f32x4 (&acc)[8]) {
    OPAQUE(tid);
    const int g = (tid >> 4) & 3, fr = tid & 15;
    lds_t xb = lb + XA_OFF + 480 - 32 * fr + 16 * g, cw = l0 + CW_OFF + 16 * (tid & 63);
    lds_t k2a = lb + L2_OFF + 96 + 8 * fr - 32 * g, k3a = lb + L3_OFF + 48 + 4 * fr - 16 * g;
    OPAQUE(xb); OPAQUE(cw); OPAQUE(k2a); OPAQUE(k3a);
    bf16x8 x1 = ldk(xb + 64 * (56 - 8 * TA) + 16), x2 = XFRAG(8 * TA);
    const bf16x8 c1 = ldk(cw), c2 = ldk(cw + CH_STRIDE), c4 = ldk(cw + 2 * CH_STRIDE), c3 = ldk(cw + 3 * CH_STRIDE);
    bf16x8 k2[3][2], k3[4];
#pragma unroll
    for (int m = 0; m < 3; ++m) { k2[m][0] = ldx(k2a + 128 * m + 16); k2[m][1] = ldx(k2a + 128 * m); }
#pragma unroll
    for (int m = 0; m < 4 - TA; ++m) k3[m] = ld_a4(k3a + 64 * m);
    f32x4 d[4], e[2];
    d[0] = MF0(x1, c1); d[1] = MF0(x1, c2); d[2] = MF0(x2, c1); d[3] = MF0(x2, c2);
    e[0] = MF0(x1, c4); e[1] = MF0(x2, c3);
    SB();
#pragma unroll
    for (int T = TA; T <= TB; ++T) {
        if (T < TB) { x1 = ldk(xb + 64 * (56 - 8 * (T + 1)) + 16); x2 = XFRAG(8 * (T + 1)); }
        u32x4 r2[2], r3;
#pragma unroll
        for (int h = 0; h < 2; ++h)
            r2[h] = (u32x4){pk_bf16(d[3][2 * h + 1], d[2][2 * h + 1]), pk_bf16(d[1][2 * h + 1], d[0][2 * h + 1]),
                            pk_bf16(d[3][2 * h], d[2][2 * h]), pk_bf16(d[1][2 * h], d[0][2 * h])};
        if (T < 4) r3 = (u32x4){pk_bf16(e[1][3], e[0][3]), pk_bf16(e[1][2], e[0][2]), pk_bf16(e[1][1], e[0][1]), pk_bf16(e[1][0], e[0][0])};
        SB();
        if (T < TB) {
            d[0] = MF0(x1, c1); d[1] = MF0(x1, c2); d[2] = MF0(x2, c1); d[3] = MF0(x2, c2);
            if (T + 1 < 4) { e[0] = MF0(x1, c4); e[1] = MF0(x2, c3); }
        }
        SB();
#pragma unroll
        for (int h = 0; h < 2; ++h)
#pragma unroll
            for (int m = 2; m < 5; ++m)
                if (T + m < 8) MFMA(__builtin_bit_cast(bf16x8, r2[h]), k2[m - 2][h], acc[T + m]);
#pragma unroll
        for (int m = 7; m >= 4; --m)
            if (T + m < 8) MFMA(__builtin_bit_cast(bf16x8, r3), k3[m - 4], acc[T + m]);
        SB();
    }
}
__device__ __forceinline__ void epi_a(const f32x4 a, lds_t dst) {
    uint32_t o0, o1;
    asm("v_cvt_pk_bf16_f32 %0, %5, %4\n\tv_cvt_pk_bf16_f32 %1, %3, %2" : "=&v"(o0), "=v"(o1) : "v"(a[0]), "v"(a[1]), "v"(a[2]), "v"(a[3]));
    *(__attribute__((address_space(3))) u32x2*)dst = (u32x2){o0, o1};
}
#define NEAR_ADDR() \
    OPAQUE(tid); \
    const int g = (tid >> 4) & 3, fr = tid & 15; \
    lds_t xb = lb + XA_OFF + 480 - 32 * fr + 16 * g; \
    lds_t kfa = lb + LA_OFF + (~fr & 3) * LA_STR + 8 + 8 * (fr >> 2) + 16 * g; \
    lds_t sg = lb + KA_OFF + 464 + 504 - 32 * fr - 8 * g, xf = l0 + FLAG_OFF + 4 * (tid & 3); \
    OPAQUE(xb); OPAQUE(kfa); OPAQUE(sg); OPAQUE(xf)
__device__ __forceinline__ void near_lo(lds_t l0, lds_t lb, int tid, uint32_t kk, lds_t sf, f32x4 (&acc)[8]) {
    NEAR_ADDR();
    bf16x8 kf[3][2], xq[3][4];
#define NLD(c) do { kf[(c) % 3][0] = ldx(kfa + 64 * (c)); kf[(c) % 3][1] = ldx(kfa + 64 * (c) + 512); \
        _Pragma("unroll") for (int t = 0; t < 4; ++t) xq[(c) % 3][t] = XFRAG(8 * t - (c)); } while (0)
    NLD(0);
    const bf16x8 k16 = ldx(kfa + 64 * 16);
    const bf16x8 kb = __builtin_bit_cast(bf16x8, __builtin_bit_cast(u32x4, ldk(l0 + CW_OFF + 1024 + 16 * (tid & 63))) & kk);
    NLD(1);
    SB();
#pragma unroll
    for (int c = 0; c < 8; ++c) {
        if (c + 2 < 8) NLD(c + 2);
#pragma unroll
        for (int t = 0; t < 4; ++t) MFMA(kf[c % 3][0], xq[c % 3][t], acc[t]);
#pragma unroll
        for (int t = 0; t < 4; ++t) MFMA(kf[c % 3][1], xq[c % 3][t], acc[t + 1]);
        if (c + 2 < 8) {
#pragma unroll
            for (int i = 0; i < 8; ++i) { SG_MF(1); SG_DS(1); }
        }
        SB();
        if (c == 0) {
#pragma unroll
            for (int t = 0; t < 4; ++t) MFMA(kb, xq[0][t], acc[t + 4]);
#pragma unroll
            for (int t = 0; t < 4; ++t) MFMA(k16, xq[0][t], acc[t + 2]);
            SB();
        }
    }
#pragma unroll
    for (int t = 0; t < 4; ++t) epi_a(acc[t], sg + 512 * (7 - t));
    flag_set(sf);
}
#define NXQ 6
__device__ __forceinline__ void near_hi(lds_t l0, lds_t lb, int tid, lds_t sf, f32x4 (&acc)[8]) {
    NEAR_ADDR();
    bf16x8 kf[17], xq[NXQ];
#pragma unroll
    for (int j = 0; j < 8; ++j) {
        if (j < NXQ - 1) xq[j] = XFRAG(25 + j);
        kf[7 - j] = ldx(kfa + 64 * (7 - j)); kf[15 - j] = ldx(kfa + 64 * (15 - j));
    }
    kf[16] = ldx(kfa + 64 * 16);
    f32x4 a7b = {0, 0, 0, 0};
    SB();
#pragma unroll
    for (int T = 4; T < 8; ++T) {
#pragma unroll
        for (int s = 0; s < 8; ++s) {
            const int n = 8 * (T - 4) + s;
            if (s == 9 - NXQ && (T == 5 || T == 6)) flag_wait(xf + 16 * (T + 1));
            if (n + NXQ - 1 < 32) xq[(n + NXQ - 1) % NXQ] = XFRAG(25 + n + NXQ - 1);
            if (T == 7 && (s & 1)) MFMA(kf[7 - s], xq[n % NXQ], a7b);
            else MFMA(kf[7 - s], xq[n % NXQ], acc[T]);
            if (T + 1 < 8) MFMA(kf[15 - s], xq[n % NXQ], acc[T + 1]);
            if (s == 7 && T + 2 < 8) MFMA(kf[16], xq[n % NXQ], acc[T + 2]);
            if (s == 1 && T > 4) {
                epi_a(acc[T - 1], sg + 512 * (8 - T));
                flag_set(sf + 32 * (T - 4));
            }
            SB();
        }
    }
#pragma unroll
    for (int i = 0; i < 4; ++i) {
        float s = acc[7][i] + a7b[i];
        asm volatile("" : "+v"(s));
        acc[7][i] = s;
    }
    epi_a(acc[7], sg);
    flag_set(sf + 32 * 4);
}

__global__ __launch_bounds__(1024) void fused_kernel(
    const uint16_t* __restrict__ u, const uint16_t* __restrict__ logA, const uint16_t* __restrict__ Aim,
    const uint16_t* __restrict__ Cre, const uint16_t* __restrict__ Cim, const uint16_t* __restrict__ logdt,
    const uint16_t* __restrict__ Dsk, const uint16_t* __restrict__ qin, const uint16_t* __restrict__ kin,
    const uint16_t* __restrict__ vin, const uint16_t* __restrict__ shk, const uint16_t* __restrict__ ssmk,
    const uint16_t* __restrict__ sD, uint16_t* __restrict__ out)
{
    __shared__ __attribute__((aligned(16))) char lds[LDS_TOTAL];
    uint64_t pc0;
    asm volatile("s_getpc_b64 %0" : "=s"(pc0));
    uint32_t pf = 0;
    if (threadIdx.x < CODE_BYTES / 128) pf = GLD(uint32_t, (gc_t)(pc0 & ~63ull), threadIdx.x * 128u, 0);
    SB();
    STDECL;
    RT(0, 0);
    STAMP(0, 0);
    STAMP(1, 0);
    const int b = blockIdx.x;
    const int xcd = b & 7, idx = b >> 3, part = idx >> 4, gi = idx & 15, grp = xcd * 16 + gi;
    const int lane = threadIdx.x & 63;
    const int w = __builtin_amdgcn_readfirstlane(threadIdx.x >> 6);
    const lds_t flags = (lds_t)lds + FLAG_OFF;
    const int g = lane >> 4, fr = lane & 15;
    {
        const u32x4 z = {0, 0, 0, 0};
        char* zp = w < 8 ? lds + w * CH_STRIDE + (lane < 4 ? XA_OFF + 4096 : XB_OFF + 4080 - 64) + 16 * lane : lds + FLAG_OFF + 16 * lane;
        if (lane < (w < 8 ? 9 : 19)) *(u32x4*)zp = z;
    }
    KEEPS(u); KEEPS(logA); KEEPS(Aim); KEEPS(Cre); KEEPS(Cim); KEEPS(logdt); KEEPS(Dsk);
    KEEPS(qin); KEEPS(kin); KEEPS(vin); KEEPS(shk); KEEPS(ssmk); KEEPS(sD); KEEPS(out);
    SB();
    lds_barrier();
    SB();
    if (w >= 12) {
        const int rw = w - 12;
        const int qq = (lane >> 2) & 3, pp = lane & 3;
        const char* rg = lds + qq * CH_STRIDE + KA_OFF + 464 + 3584 + (64 * rw + 16 * g + 4 * pp) * 2;
        const uint32_t r0 = 255 - (64 * rw + lane);
        uint32_t oo = r0 * (4 * DCH) + part * (2 * DCH) + grp * 16;
        OPAQUE(oo);
#define ROW_OUT(T) (*(__attribute__((address_space(1))) u32x4*)((gw_t)out + (uint32_t)(oo + (T) * (256u * 4 * DCH))))
#define ROW_WAIT(S) flag_wait(flags + 128 + 32 * (S) + 4 * (lane & 7))
        if (part) {
            uint32_t qo = r0 * (2 * DCH) + grp * 16;
            OPAQUE(qo);
            u32x4 qv[8];
#define LDQ(T) qv[T] = GLD(u32x4, (gc_t)qin, (uint32_t)(qo + (T) * (256u * 2 * DCH)), 0)
#define ROWB(T) ROW_OUT(T) = gate(row_read(rg - 512 * (T)), qv[T])
            flag_wait(flags + 4 * (lane & 3));
            LDQ(0); LDQ(1); LDQ(2);
            SB();
            STAMP(0, 1);
            ROW_WAIT(0); ROWB(0); SB(); LDQ(3); LDQ(4); SB();
            STAMP(0, 2);
            ROW_WAIT(1); ROWB(1); ROWB(2); SB(); LDQ(5); LDQ(6); LDQ(7); SB();
            STAMP(0, 3);
            ROW_WAIT(2); ROWB(3); ROWB(4); SB();
            STAMP(0, 4);
            ROW_WAIT(3); ROWB(5); ROWB(6); SB();
            STAMP(0, 5);
            ROW_WAIT(4); ROWB(7);
        } else {
            const uint32_t t = gi * 64 + lane;
            const gc_t pkv = (gc_t)((t & 2) ? vin : kin) + ((t >> 2) * (2 * DCH) + 256 * xcd + 128 * (t & 1));
            const uint32_t ouq = (t >> 1) * (2 * DCH) + 256 * xcd + 128 * (t & 1);
            const gc_t pu = (gc_t)u + ouq, pq = (gc_t)qin + ouq;
            uint64_t seq = rw == 0 ? SEQ0 : rw == 1 ? SEQ1 : rw == 2 ? SEQ2 : SEQ3;
            for (int d = rw == 0 ? DL0 : rw == 1 ? DL1 : rw == 2 ? DL2 : DL3; d > 0; --d) __builtin_amdgcn_s_sleep(8);
#pragma unroll 1
            for (uint32_t c; (c = (uint32_t)seq & 0xffu) != 0xffu; seq >>= 8) {
                const gc_t a = c < 8 ? pkv + (size_t)c * (256 * 2 * DCH) : (c < 12 ? pu : pq) + (size_t)(c & 3) * (512 * 2 * DCH);
                const uint32_t pv = GLD(uint32_t, a, 0, 0);
                SB(); KEEP(pv); SB();
                if ((c >> 2) == 2) flag_set(flags + 288 + 4 * (c & 3));
            }
#define ROWA(T) ROW_OUT(T) = row_read(rg - 512 * (T))
            STAMP(0, 1);
            ROW_WAIT(0); ROWA(0); ROWA(1); ROWA(2); ROWA(3); SB();
            STAMP(0, 2);
            ROW_WAIT(1); ROWA(4); SB();
            STAMP(0, 3);
            ROW_WAIT(2); ROWA(5); SB();
            STAMP(0, 4);
            ROW_WAIT(3); ROWA(6); SB();
            STAMP(0, 5);
            ROW_WAIT(4); ROWA(7);
        }
        STAMP(1, 6);
        STAMPW(1, 7);
        RT(0, 1);
        return;
    }
    if (w >= 8) {
        const int pw = w - 8, c2 = lane & 3, rq = lane >> 2;
        const int s0 = 64 * pw + 4 * rq;
        uint32_t eo = (uint32_t)(s0 + 2) * (2 * DCH) + grp * 16 + 4 * c2;
        OPAQUE(eo);
        char* xp = lds + 2 * c2 * CH_STRIDE + 2 * (2044 - s0);
#define BLK(o, P) ((uint32_t)((o) + (P) * (256u * 2 * DCH)))
        if (part) {
            uint32_t kk[8][4], vv[8][4], hk[8][3];
            uint32_t ho = (uint32_t)(64 * pw + 16 * g - 2) * (2 * DCH) + grp * 16 + 4 * c2;
            OPAQUE(ho);
            const bool hz = (pw | g) == 0;
            const uint32_t ho0 = hz ? ho + 4 * (2 * DCH) : ho;
#define LDK(P) do { _Pragma("unroll") for (int i = 0; i < 4; ++i) kk[P][i] = GLD(uint32_t, (gc_t)kin, BLK(eo, P), (i - 2) * (2 * DCH)); } while (0)
#define LDV(P) do { _Pragma("unroll") for (int i = 0; i < 4; ++i) vv[P][i] = GLD(uint32_t, (gc_t)vin, BLK(eo, P), (i - 2) * (2 * DCH)); } while (0)
#define LDH(P) do { _Pragma("unroll") for (int i = 0; i < 3; ++i) hk[P][i] = GLD(uint32_t, (gc_t)kin, ((P) ? BLK(ho, P) : ho0), (i - 1) * (2 * DCH)); } while (0)
            const u32x4 wv = GLD(u32x4, (gc_t)shk, (uint32_t)(grp * 8 + 2 * c2) * 8, 0);
#pragma unroll
            for (int p = 0; p < NUP; ++p) { LDK(p); LDH(p); LDV(p); SB(); }
            const float wt[2][4] = {{lo_f(wv[0]), hi_f(wv[0]), lo_f(wv[1]), hi_f(wv[1])}, {lo_f(wv[2]), hi_f(wv[2]), lo_f(wv[3]), hi_f(wv[3])}};
#pragma unroll
            for (int i = 0; i < 3; ++i) hk[0][i] = hz ? 0u : hk[0][i];
            STAMP(0, 1);
#pragma unroll
            for (int p = 0; p < 8; ++p) {
                uint32_t kp[7];
#pragma unroll
                for (int i = 0; i < 3; ++i)
                    kp[i] = (uint32_t)__builtin_amdgcn_update_dpp((int)hk[p][i], (int)kk[p][1 + i], 0x114  , 0xf, 0xf, false);
#pragma unroll
                for (int i = 0; i < 4; ++i) kp[3 + i] = kk[p][i];
#pragma unroll
                for (int h = 0; h < 2; ++h) {
                    float kf[7], vf[4];
#pragma unroll
                    for (int i = 0; i < 7; ++i) kf[i] = h ? hi_f(kp[i]) : lo_f(kp[i]);
#pragma unroll
                    for (int i = 0; i < 4; ++i) vf[i] = h ? hi_f(vv[p][i]) : lo_f(vv[p][i]);
                    uint32_t o0, o1;
                    kv4(kf, wt[h], vf, o0, o1);
                    put_x4(xp + h * CH_STRIDE - 512 * p, o0, o1);
                    SB();
                    if (h == 0 && p + 3 < 8 && p >= LATE) { LDK(p + 3); LDH(p + 3); }
                    SB();
                }
                flag_set(flags + 16 * p + 4 * pw);
                SB();
                if (p == 0) { if (NUP < 2) { LDK(1); LDH(1); LDV(1); } LDK(2); LDH(2); LDV(2); SB(); }
                if (p + 3 < 8 && p < LATE) { LDK(p + 3); LDH(p + 3); }
                if (p + 3 < 8) LDV(p + 3);
                SB();
                if (p < 6) STAMP(0, 2 + p); else STAMP(1, p - 5);
            }
        } else {
            uint32_t uu[8][4];
#define LDU(P) do { _Pragma("unroll") for (int i = 0; i < 4; ++i) uu[P][i] = GLD(uint32_t, (gc_t)u, BLK(eo, P), (i - 2) * (2 * DCH)); SB(); } while (0)
#define PUTU(P) do { _Pragma("unroll") for (int h = 0; h < 2; ++h) { \
                    const uint32_t sel = h ? 0x07060302u : 0x05040100u; \
                    *(u32x2*)(xp + h * CH_STRIDE - 512 * (P) + XA_OFF) = (u32x2){__builtin_amdgcn_perm(uu[P][2], uu[P][3], sel), __builtin_amdgcn_perm(uu[P][0], uu[P][1], sel)}; } \
                flag_set(flags + 16 * (P) + 4 * pw); SB(); \
                if ((P) < 6) STAMP(0, 2 + (P)); else STAMP(1, (P) - 5); } while (0)
#pragma unroll 1
            for (int ci = pw; ci < 5; ci += 4) {
                const uint32_t lo = (uint32_t)((0x121413151aull >> (8 * ci)) & 0xff), span = (uint32_t)((0x0106080402ull >> (8 * ci)) & 0xff);
                const uint32_t one = ci < 4 ? 0x3f80u : 0xffffu, z0 = fr + 8 * g - lo;
                u32x4 cv;
#pragma unroll
                for (int q = 0; q < 4; ++q) cv[q] = (z0 + 2 * q <= span ? one : 0u) | (z0 + 2 * q + 1 <= span ? one << 16 : 0u);
                *(u32x4*)(lds + (ci & 3) * CH_STRIDE + CW_OFF + 1024 * (ci >> 2) + 16 * lane) = cv;
            }
            flag_wait(flags + 288);
            STAMP(0, 1);
            LDU(0); LDU(1);
            PUTU(0); PUTU(1);
            flag_wait(flags + 292);
            LDU(2); LDU(3);
            PUTU(2); PUTU(3);
            flag_wait(flags + 296);
            LDU(4); LDU(5);
            PUTU(4); PUTU(5);
            flag_wait(flags + 300);
            LDU(6); LDU(7);
            PUTU(6); PUTU(7);
        }
        RT(0, 1);
        return;
    }

    const int chl = grp * 8 + w;
    char* cb = lds + w * CH_STRIDE;
    u32x4 kq[4];
    if (part) {
        const uint16_t* ksrc = ssmk + (size_t)chl * LSEQ;
        if (D_SSM) __builtin_amdgcn_s_sleep(D_SSM);
#pragma unroll
        for (int i = 0; i < 4; ++i) kq[i] = *(const u32x4*)(ksrc + 8 * (64 * i + lane));
        uint32_t dr = sD[chl];
        SB();
        KEEP(pf);
        OPAQUE(dr);
        kq[0][0] = pk_bf16(lo_f(kq[0][0]) + (lane ? 0.f : lo_f(dr)), hi_f(kq[0][0]));
    } else {
        float dt = bf2f(logdt[chl]);
        uint32_t pr[8];
#pragma unroll
        for (int bt = 0; bt < 2; ++bt) {
            const int n = chl * NMODE + bt * 64 + lane;
            pr[4 * bt] = logA[n]; pr[4 * bt + 1] = Aim[n]; pr[4 * bt + 2] = Cre[n]; pr[4 * bt + 3] = Cim[n];
        }
        uint32_t dr = Dsk[chl];
        SB();
        {
            const u32x4 z = {0, 0, 0, 0};
            *(u32x4*)(cb + XA_OFF + 4096 + 16 * lane) = z;
            if (lane < 18) *(u32x4*)(cb + XA_OFF + 5120 + 16 * lane) = z;
        }
        SB();
        dt = rbf(expf(dt));
        float pa[2], pb[2], pcr[2], pci[2];
        float mx = 0.f;
#pragma unroll
        for (int bt = 0; bt < 2; ++bt) {
            const float ar = -rbf(expf(lo_f(pr[4 * bt])));
            pa[bt] = (ar * dt) * 1.4426950408889634f;
            pb[bt] = (lo_f(pr[4 * bt + 1]) * dt) * 0.15915494309189535f;
            pcr[bt] = 2.f * (lo_f(pr[4 * bt + 2]) * dt);
            pci[bt] = 2.f * (lo_f(pr[4 * bt + 3]) * dt);
            mx = fmaxf(mx, fmaxf(fabsf(pcr[bt]), fabsf(pci[bt])));
        }
        KEEP(pf);
        OPAQUE(dr);
        const float Dv = lane ? 0.f : lo_f(dr);
        int mi = (int)__float_as_uint(mx);
#define DPPMAX(ctrl) mi = max(mi, __builtin_amdgcn_update_dpp(0, mi, ctrl, 0xf, 0xf, false))
        DPPMAX(0x111); DPPMAX(0x112); DPPMAX(0x114); DPPMAX(0x118); DPPMAX(0x142); DPPMAX(0x143);
        int sf = 266 - ((__builtin_amdgcn_readlane(mi, 63) >> 23) & 0xff);
        sf = sf < 1 ? 1 : (sf > 239 ? 239 : sf);
        const float scl = __uint_as_float((uint32_t)sf << 23);
        const float inv = __uint_as_float((uint32_t)(240 - sf) << 23);
        STAMP(0, 1);

        char* Tl = cb + lane * 8;
        const char* Tr = cb + fr * KG_ROWB + g * 16;
#define TW(row, v0, v1) *(u32x2*)(Tl + (row) * KG_ROWB) = (u32x2){pk16(v0), pk16(v1)}
#define B2(X) do { _Pragma("unroll") for (int bt = 0; bt < 2; ++bt) { X; } } while (0)
        f16x8 bfr[8], af[8];
        f32x2 z8c[2], z1[2], z128[2];
        B2(z8c[bt] = anchor(pa[bt] * 8.f, pb[bt] * -8.f));
        B2(z1[bt] = anchor(pa[bt], pb[bt]));
        B2(z128[bt] = anchor(pa[bt] * 128.f, pb[bt] * 128.f));
        f32x2 f[2][16], h[2][8], cg[2][17], zs[2][3], zl[2][3], t[2][8];
        B2(t[bt][0] = cm1(z8c[bt], z8c[bt]); t[bt][1] = cm1(z128[bt], z128[bt]); t[bt][2] = cm1(z1[bt], z1[bt]));
        B2(cg[bt][0] = ((f32x2){pcr[bt] * scl, pci[bt] * scl}); f[bt][1] = z8c[bt] * 16384.f);
        SB();
        B2(zs[bt][0] = cm2(z8c[bt], z8c[bt], t[bt][0]); zl[bt][0] = cm2(z128[bt], z128[bt], t[bt][1]); h[bt][2] = cm2(z1[bt], z1[bt], t[bt][2]));
        B2(t[bt][3] = cm1(cg[bt][0], z128[bt]));
        SB();
        B2(t[bt][0] = cm1(zs[bt][0], zs[bt][0]); t[bt][1] = cm1(zl[bt][0], zl[bt][0]); t[bt][2] = cm1(h[bt][2], h[bt][2]);
           t[bt][4] = cm1(h[bt][2], z1[bt]); t[bt][5] = cm1(f[bt][1], zs[bt][0]); t[bt][6] = cm1(cg[bt][0], zl[bt][0]);
           cg[bt][1] = cm2(cg[bt][0], z128[bt], t[bt][3]); f[bt][2] = zs[bt][0] * 16384.f);
        SB();
        B2(zs[bt][1] = cm2(zs[bt][0], zs[bt][0], t[bt][0]); zl[bt][1] = cm2(zl[bt][0], zl[bt][0], t[bt][1]); h[bt][4] = cm2(h[bt][2], h[bt][2], t[bt][2]);
           h[bt][3] = cm2(h[bt][2], z1[bt], t[bt][4]); f[bt][3] = cm2(f[bt][1], zs[bt][0], t[bt][5]); cg[bt][2] = cm2(cg[bt][0], zl[bt][0], t[bt][6]);
           t[bt][3] = cm1(cg[bt][1], zl[bt][0]));
        SB();
        B2(t[bt][0] = cm1(zs[bt][1], zs[bt][1]); t[bt][1] = cm1(zl[bt][1], zl[bt][1]);
           t[bt][4] = cm1(f[bt][1], zs[bt][1]); t[bt][5] = cm1(f[bt][2], zs[bt][1]); t[bt][6] = cm1(f[bt][3], zs[bt][1]);
           cg[bt][3] = cm2(cg[bt][1], zl[bt][0], t[bt][3]); f[bt][4] = zs[bt][1] * 16384.f);
        SB();
        B2(zs[bt][2] = cm2(zs[bt][1], zs[bt][1], t[bt][0]); zl[bt][2] = cm2(zl[bt][1], zl[bt][1], t[bt][1]);
           f[bt][5] = cm2(f[bt][1], zs[bt][1], t[bt][4]); f[bt][6] = cm2(f[bt][2], zs[bt][1], t[bt][5]); f[bt][7] = cm2(f[bt][3], zs[bt][1], t[bt][6]));
        B2(f[bt][0] = ((f32x2){16384.f, 0.f}));
#pragma unroll
        for (int i = 0; i < 5; ++i) TW(i, f[0][i], f[1][i]);
        SB();
        B2(_Pragma("unroll") for (int i = 1; i < 8; ++i) t[bt][i] = cm1(f[bt][i], zs[bt][2]); f[bt][8] = zs[bt][2] * 16384.f);
#pragma unroll
        for (int i = 5; i < 8; ++i) TW(i, f[0][i], f[1][i]);
        SB();
        B2(_Pragma("unroll") for (int i = 1; i < 8; ++i) f[bt][8 + i] = cm2(f[bt][i], zs[bt][2], t[bt][i]));
        SB();
        B2(t[bt][0] = cm1(h[bt][4], z1[bt]); t[bt][1] = cm1(h[bt][4], h[bt][2]); t[bt][2] = cm1(h[bt][4], h[bt][3]));
#pragma unroll
        for (int i = 8; i < 16; ++i) TW(i, f[0][i], f[1][i]);
#pragma unroll
        for (int ks = 0; ks < 8; ++ks) bfr[ks] = *(const f16x8*)(Tr + ks * 64);
        B2(h[bt][1] = z1[bt]; h[bt][5] = cm2(h[bt][4], z1[bt], t[bt][0]); h[bt][6] = cm2(h[bt][4], h[bt][2], t[bt][1]); h[bt][7] = cm2(h[bt][4], h[bt][3], t[bt][2]));
        SB();
        f32x4 ac[3][2];
#define TRD() do { _Pragma("unroll") for (int ks = 0; ks < 8; ++ks) af[ks] = *(const f16x8*)(Tr + ks * 64); SB(); } while (0)
#define TMM(rt) do { \
            ac[rt][0] = (f32x4){0, 0, 0, 0}; ac[rt][1] = (f32x4){0, 0, 0, 0}; \
            _Pragma("unroll") for (int ks = 0; ks < 8; ++ks) \
                ac[rt][ks & 1] = __builtin_amdgcn_mfma_f32_16x16x32_f16(af[ks], bfr[ks], ac[rt][ks & 1], 0, 0, 0); SB(); } while (0)
#pragma unroll
        for (int aa = 0; aa < 2; ++aa) {
            TW(8 * aa, cg[0][aa], cg[1][aa]);
#pragma unroll
            for (int e0 = 1; e0 < 8; e0 += 4) {
                B2(_Pragma("unroll") for (int e = e0; e < e0 + 4 && e < 8; ++e) t[bt][e] = cm1(cg[bt][aa], h[bt][e]));
                SB();
                B2(_Pragma("unroll") for (int e = e0; e < e0 + 4 && e < 8; ++e) t[bt][e] = cm2(cg[bt][aa], h[bt][e], t[bt][e]));
                SB();
#pragma unroll
                for (int e = e0; e < e0 + 4 && e < 8; ++e) TW(8 * aa + e, t[0][e], t[1][e]);
                SB();
            }
        }
        TRD();
        uint16_t* KD = (uint16_t*)(lds + KD_OFF + w * 1536);
#define KDW(rt) do { \
            f32x4 v = (ac[rt][0] + ac[rt][1]) * inv; \
            if ((rt) == 0) v[0] += Dv; \
            _Pragma("unroll") for (int r = 0; r < 4; r += 2) { \
                uint32_t p = pk_bf16(v[r], v[r + 1]); \
                if ((rt) < 2) { \
                    KD[(16 * (rt) + 4 * g + r) * 16 + fr] = (uint16_t)p; \
                    KD[(16 * (rt) + 4 * g + r + 1) * 16 + fr] = (uint16_t)(p >> 16); \
                    if ((rt) == 1) *(uint32_t*)((lane >= (r ? 32 : 34) ? cb + L2_OFF + 128 * g + 4 * fr - 140 : (char*)KD + 1024) + 32 * r) = p; \
                } else { \
                    char* a3 = lane < 32 ? cb + L3_OFF + 58 + 128 * g + 2 * fr : (char*)KD + 1024; \
                    if (r == 0) { \
                        kk = (uint32_t)__builtin_amdgcn_readlane((int)p, 0) & 0xffffu; kk |= kk << 16; \
                        *(uint16_t*)(lane == 0 ? cb + L2_OFF + 372 : lane == 32 ? cb + L3_OFF + 314 : a3) = (uint16_t)p; \
                    } else *(uint16_t*)(a3 + 32 * r) = (uint16_t)p; \
                    *(uint16_t*)(a3 + 32 * r + 32) = (uint16_t)(p >> 16); \
                } } } while (0)
        uint32_t kk = 0;
#pragma unroll
        for (int aa = 2; aa < 4; ++aa) {
            B2(_Pragma("unroll") for (int e = 1; e < 4; ++e) t[bt][e] = cm1(cg[bt][aa], h[bt][2 * e]));
            TW(4 * (aa - 2), cg[0][aa], cg[1][aa]);
            SB();
            B2(_Pragma("unroll") for (int e = 1; e < 4; ++e) t[bt][e] = cm2(cg[bt][aa], h[bt][2 * e], t[bt][e]));
            SB();
#pragma unroll
            for (int e = 1; e < 4; ++e) TW(4 * (aa - 2) + e, t[0][e], t[1][e]);
            SB();
        }
        TMM(0);
        B2(_Pragma("unroll") for (int i = 0; i < 4; ++i) { cg[bt][4 + i] = cm1(cg[bt][i], zl[bt][1]); cg[bt][8 + i] = cm1(cg[bt][i], zl[bt][2]); });
        SB();
        B2(_Pragma("unroll") for (int i = 0; i < 4; ++i) { cg[bt][4 + i] = cm2(cg[bt][i], zl[bt][1], cg[bt][4 + i]); cg[bt][8 + i] = cm2(cg[bt][i], zl[bt][2], cg[bt][8 + i]); });
        KDW(0);
        SB();
        B2(_Pragma("unroll") for (int i = 0; i < 4; ++i) t[bt][i] = cm1(cg[bt][4 + i], h[bt][4]); cg[bt][16] = cm1(cg[bt][8], zl[bt][2]));
#pragma unroll
        for (int i = 0; i < 4; ++i) TW(8 + 2 * i, cg[0][4 + i], cg[1][4 + i]);
        SB();
        B2(_Pragma("unroll") for (int i = 0; i < 4; ++i) t[bt][i] = cm2(cg[bt][4 + i], h[bt][4], t[bt][i]); cg[bt][16] = cm2(cg[bt][8], zl[bt][2], cg[bt][16]));
        SB();
#pragma unroll
        for (int i = 0; i < 4; ++i) TW(9 + 2 * i, t[0][i], t[1][i]);
        SB();
        TRD();
        B2(_Pragma("unroll") for (int i = 0; i < 4; ++i) cg[bt][12 + i] = cm1(cg[bt][8 + i], zl[bt][1]));
        TW(8, cg[0][16], cg[1][16]);
        SB();
        B2(_Pragma("unroll") for (int i = 0; i < 4; ++i) cg[bt][12 + i] = cm2(cg[bt][8 + i], zl[bt][1], cg[bt][12 + i]));
#pragma unroll
        for (int aa = 8; aa < 12; ++aa) TW(aa - 8, cg[0][aa], cg[1][aa]);
        SB();
#pragma unroll
        for (int aa = 12; aa < 16; ++aa) TW(aa - 8, cg[0][aa], cg[1][aa]);
        SB();
        TMM(1);
        TRD();
        KDW(1);
        SB();
        int tid = threadIdx.x;
        asm volatile("" : "+v"(tid));
        {
            const int lane = tid & 63, g = lane >> 4, fr = lane & 15;
            const int hi = lane >> 5, last = (fr == 15);
            const uint16_t* p0 = KD + (hi ? 16 + 4 * (g - 2) : 8 * g) * 16 + fr;
            const uint16_t* p1 = p0 - 16 * hi, * p2 = p0 - 32 * hi, * p3 = p0 - 48 * hi;
            const uint16_t* p7 = p0 + (hi ? (last ? 49 : 1) : 112);
            const uint16_t* q0 = KD + 24 * 16 + (lane & 1);
            const uint32_t e0 = p0[0], e1 = p1[16], e2 = p1[32], e3 = p1[48], e4 = p2[64], e5 = p3[80], e6 = p3[96], e7 = p7[0];
            const uint32_t a = q0[0], bq = q0[16], c = q0[1];
            SB();
            TMM(2);
            STAMP(0, 2);
            kq[0][0] = e0 | (e1 << 16);
            kq[0][1] = e2 | (e3 << 16);
            kq[0][2] = e4 | (e5 << 16);
            kq[0][3] = e6 | (e7 << 16);
            const uint32_t s0 = lane < 2 ? 0x01000100u : 0x0c0c0c0cu;
            kq[1][0] = __builtin_amdgcn_perm(a, a, s0);
            kq[1][1] = __builtin_amdgcn_perm(bq, a, lane < 2 ? 0x05040100u : 0x0c0c0c0cu);
            kq[1][2] = __builtin_amdgcn_perm(bq, bq, s0);
            kq[1][3] = __builtin_amdgcn_perm(c, c, lane < 1 ? 0x01000100u : 0x0c0c0c0cu);
            put_k4(cb + LA_OFF + 32 + 16 * lane, kq[0], prev_lane(kq[0][2], 0), prev_lane(kq[0][3], 0));
            const uint32_t pv2 = prev_lane(kq[1][2], __builtin_amdgcn_readlane(kq[0][2], 63));
            const uint32_t pv3 = prev_lane(kq[1][3], __builtin_amdgcn_readlane(kq[0][3], 63));
            if (lane < 6) put_k4(cb + LA_OFF + 32 + 16 * (lane < 4 ? 64 + lane : lane - 6), kq[1], pv2, pv3);
            SB();
            KDW(2);
        }
        STAMP(0, 3);
        {
            const lds_t lb = (lds_t)lds + w * CH_STRIDE, sf = flags + 128 + 4 * w;
            f32x4 acc[8];
#pragma unroll
            for (int i = 0; i < 8; ++i) acc[i] = (f32x4){0, 0, 0, 0};
            OPAQUE(tid);
            flag_wait(flags + 4 * (tid & 7));
            STAMP(0, 4);
            far_field<0, 1>((lds_t)lds, lb, tid, acc);
            STAMP(0, 5);
            OPAQUE(tid);
            flag_wait(flags + 32 + 4 * (tid & 7));
            near_lo((lds_t)lds, lb, tid, kk, sf, acc);
            STAMP(0, 6);
            OPAQUE(tid);
            flag_wait(flags + 64 + 4 * (tid & 7));
            STAMP(0, 7);
            far_field<2, 5>((lds_t)lds, lb, tid, acc);
            STAMP(1, 1);
            near_hi((lds_t)lds, lb, tid, sf, acc);
            STAMP(1, 6);
            RT(0, 1);
        }
        return;
    }
    int tid2 = threadIdx.x;
    asm volatile("" : "+v"(tid2));
#define lane (tid2 & 63)
#define g ((tid2 >> 4) & 3)
#define fr (tid2 & 15)
    {
        const u32x4 z = {0, 0, 0, 0};
        if (lane < 29) {
            *(u32x4*)(cb + KA_OFF + 16 * lane) = z;
            *(u32x4*)(cb + KB_OFF + 16 * lane) = z;
        }
        uint32_t c = 0;
#pragma unroll
        for (int i = 0; i < 4; ++i) {
            put_k<KA_OFF + 464, KB_OFF + 464>(cb, 64 * i + lane, kq[i], prev_lane(kq[i][3], c));
            c = __builtin_amdgcn_readlane(kq[i][3], 63);
        }
    }
    STAMP(0, 3);

    const lds_t lb = (lds_t)lds + w * CH_STRIDE;
    const lds_t kaddr = lb + ((fr & 1) ? KA_OFF : KB_OFF) + 2 * (32 * (fr >> 1) + 8 * g + 8);
    lds_t xaddr = lb + ((fr & 1) ? (XA_OFF + 2 * (1822 - 2 * fr + 8 * g)) : (XB_OFF + 2 * (1820 - 2 * fr + 8 * g))) - 3584;
    asm volatile("" : "+v"(xaddr));
    const int lo_ = 64 * g + 2 * fr;
    char* sg = cb + KA_OFF + 464 + 2 * (2046 - lo_);
#define EPI(T) epi_acc(acc[T], sg - 512 * (T))
#define SFLAG(S) flag_set(flags + 128 + 32 * (S) + 4 * w)

    f32x4 acc[8];
#pragma unroll
    for (int i = 0; i < 8; ++i) acc[i] = (f32x4){0, 0, 0, 0};

    flag_wait(flags + 4 * (lane & 3));
    STAMP(0, 4);
    stage<0, 1>(kaddr, xaddr, acc);
    STAMP(0, 5);
    EPI(0); SFLAG(0);
    flag_wait(flags + 16 + 4 * (lane & 7));
    STAMP(0, 6);
    stage<1, 2>(kaddr, xaddr, acc);
    STAMP(0, 7);
    EPI(1); EPI(2); SFLAG(1);
    flag_wait(flags + 48 + 4 * (lane & 7));
    STAMP(1, 1);
    stage<3, 2>(kaddr, xaddr, acc);
    STAMP(1, 2);
    EPI(3); EPI(4); SFLAG(2);
    flag_wait(flags + 80 + 4 * (lane & 7));
    STAMP(1, 3);
    stage<5, 2>(kaddr, xaddr, acc);
    STAMP(1, 4);
    EPI(5); EPI(6); SFLAG(3);
    flag_wait(flags + 112 + 4 * (lane & 3));
    STAMP(1, 5);
    {
        f32x4 a7b = {0, 0, 0, 0};
#pragma unroll
        for (int c = 0; c < 8; c += 2) {
            MFMA(ldk(kaddr + 64 * c), ldx(xaddr + 64 * c), acc[7]);
            MFMA(ldk(kaddr + 64 * c + 64), ldx(xaddr + 64 * c + 64), a7b);
        }
#pragma unroll
        for (int i = 0; i < 4; ++i) {
            float s = acc[7][i] + a7b[i];
            asm volatile("" : "+v"(s));
            acc[7][i] = s;
        }
    }
    EPI(7); SFLAG(4);
    STAMP(1, 6);
    RT(0, 1);
}

extern "C" void kernel_launch(void* const* d_in, const int* in_sizes, int n_in,
                              void* d_out, int out_size, void* d_ws, size_t ws_size,
                              hipStream_t stream) {
    fused_kernel<<<256, 1024, 0, stream>>>(
        (const uint16_t*)d_in[0], (const uint16_t*)d_in[1], (const uint16_t*)d_in[2], (const uint16_t*)d_in[3],
        (const uint16_t*)d_in[4], (const uint16_t*)d_in[5], (const uint16_t*)d_in[6], (const uint16_t*)d_in[7],
        (const uint16_t*)d_in[8], (const uint16_t*)d_in[9], (const uint16_t*)d_in[10], (const uint16_t*)d_in[11],
        (const uint16_t*)d_in[12], (uint16_t*)d_out);
}
```

```cpp
#define SEQ0 0x0b0b050300ull
#define DL0 0
#define SEQ1 0x07070a0c01ull
#define DL1 3
#define SEQ2 0x0e0e060908ull
#define DL2 4
#define SEQ3 0x0f0f0d0402ull
#define DL3 5
#define CH_D 1
#define APS 130
#define PERMA 0x76043215u
#define PERMB 0x76043215u
#define UL 1
#define THF 1
#define THS 0
#define KEEP4 1
#define KEEP3 1
#define KEEP3N 2
#define KEEP2 1
#define AFLAT 2
#define QL 3
#define FEM 24
#define G1 3
#define G2 5
#define TS 4
#define QP 8
#define QO 0
#define THR 8
#include <hip/hip_runtime.h>
#include <stdint.h>
#include <utility>

typedef __attribute__((ext_vector_type(4))) unsigned int u32x4;
typedef __attribute__((ext_vector_type(2))) unsigned int u32x2;
typedef __attribute__((ext_vector_type(4))) float f32x4;
typedef __attribute__((ext_vector_type(2))) float f32x2;
typedef __attribute__((ext_vector_type(8))) _Float16 f16x8;
typedef __attribute__((ext_vector_type(2))) _Float16 f16x2;
typedef __attribute__((ext_vector_type(8))) __bf16 bf16x8;
typedef __attribute__((ext_vector_type(2))) __bf16 bf16x2;
typedef __attribute__((ext_vector_type(4))) short s16x4;

#define STAMP(k, i)
#define STAMPW(k, i)
#define RT(k, i)
#define STDECL
#define LSEQ 2048
#define DCH 1024
#define NMODE 128

__device__ __forceinline__ float bf2f(uint16_t v) { return __uint_as_float(((uint32_t)v) << 16); }
__device__ __forceinline__ float lo_f(uint32_t w) { return __uint_as_float(w << 16); }
__device__ __forceinline__ float hi_f(uint32_t w) { return __uint_as_float(w & 0xffff0000u); }
__device__ __forceinline__ uint32_t pk_bf16(float lo, float hi) {
    f32x2 x = {lo, hi};
    bf16x2 y = __builtin_convertvector(x, bf16x2);
    return __builtin_bit_cast(uint32_t, y);
}
__device__ __forceinline__ uint32_t pk_f16(float lo, float hi) {
    f32x2 x = {lo, hi};
    f16x2 y = __builtin_convertvector(x, f16x2);
    return __builtin_bit_cast(uint32_t, y);
}
__device__ __forceinline__ float rbf(float a) { return lo_f(pk_bf16(a, a)); }

#define KEEP(v) asm volatile("" :: "v"(v))
#define KEEPS(v) asm volatile("" :: "s"(v))
#define OPAQUE(v) asm volatile("" : "+v"(v))
#define SB() __builtin_amdgcn_sched_barrier(0)
__device__ __forceinline__ f32x2 cm1(f32x2 a, f32x2 b) {
    f32x2 t;
    asm("v_pk_mul_f32 %0, %1, %2 op_sel:[0,0] op_sel_hi:[0,1]" : "=v"(t) : "v"(a), "v"(b));
    return t;
}
__device__ __forceinline__ f32x2 cm2(f32x2 a, f32x2 b, f32x2 t) {
    f32x2 c;
    asm("v_pk_fma_f32 %0, %1, %2, %3 op_sel:[1,1,0] op_sel_hi:[1,0,1] neg_lo:[1,0,0]" : "=v"(c) : "v"(a), "v"(b), "v"(t));
    return c;
}
__device__ __forceinline__ f32x2 cmp(f32x2 a, f32x2 b) {
    float re, im;
    asm("v_mul_f32 %0, %2, %4\n\tv_mul_f32 %1, %2, %5\n\tv_fma_f32 %0, -%3, %5, %0\n\tv_fmac_f32 %1, %3, %4"
        : "=&v"(re), "=&v"(im) : "v"(a[0]), "v"(a[1]), "v"(b[0]), "v"(b[1]));
    return (f32x2){re, im};
}
__device__ __forceinline__ uint32_t hm1(uint32_t a, uint32_t b) {
    uint32_t t;
    asm("v_pk_mul_f16 %0, %1, %2 op_sel:[0,0] op_sel_hi:[0,1]" : "=v"(t) : "v"(a), "v"(b));
    return t;
}
__device__ __forceinline__ uint32_t hm2(uint32_t a, uint32_t b, uint32_t t) {
    uint32_t c;
    asm("v_pk_fma_f16 %0, %1, %2, %3 op_sel:[1,1,0] op_sel_hi:[1,0,1] neg_lo:[1,0,0]" : "=v"(c) : "v"(a), "v"(b), "v"(t));
    return c;
}
__device__ __forceinline__ f32x2 anchor(float a, float b) {
    const float e = __builtin_amdgcn_exp2f(a);
    const float th = __builtin_amdgcn_fractf(b);
    return (f32x2){e * __builtin_amdgcn_cosf(th), e * __builtin_amdgcn_sinf(th)};
}
__device__ __forceinline__ uint32_t pk16(f32x2 c) { return pk_f16(c[0], c[1]); }

#define KG_ROWB 544

#define D_PF 32
#define D_SSM 0
#define NUP 1
#define LATE 1
#define P_IN 3
#define P_OUT 3
#define P_KG 2
#define P_CA 1
#define P_CA0 0
#define P_CB 1
#define KA_OFF 0
#define KB_OFF 4640
#define XA_OFF 9216
#define XB_OFF 13440
#define CH_STRIDE 17696
#define SA 8768
#define LA_OFF 4096
#define LA_STR 1088
#define XR_OFF (8 * SA)
#define SX 5456
#define L2_OFF 4576
#define L3_OFF 5088
#define CW_OFF (XR_OFF + 8 * SX)
#define KD_OFF (8 * CH_STRIDE)
#define FLAG_OFF (KD_OFF + 8 * 1536)
#define LDS_TOTAL (FLAG_OFF + 320)
#define CODE_BYTES 28160

template <int OA, int OB>
__device__ __forceinline__ void put_k(char* cb, int y, u32x4 d, uint32_t prev) {
    *(u32x4*)(cb + OA + 16 * y) = d;
    u32x4 o;
    o[0] = __builtin_amdgcn_alignbyte(d[0], prev, 2);
    o[1] = __builtin_amdgcn_alignbyte(d[1], d[0], 2);
    o[2] = __builtin_amdgcn_alignbyte(d[2], d[1], 2);
    o[3] = __builtin_amdgcn_alignbyte(d[3], d[2], 2);
    *(u32x4*)(cb + OB + 16 * y) = o;
}
__device__ __forceinline__ void put_k4(char* p, u32x4 d, uint32_t p2, uint32_t p3) {
    const uint32_t o0 = __builtin_amdgcn_alignbyte(d[0], p3, 2), o1 = __builtin_amdgcn_alignbyte(d[1], d[0], 2);
    const uint32_t o2 = __builtin_amdgcn_alignbyte(d[2], d[1], 2), o3 = __builtin_amdgcn_alignbyte(d[3], d[2], 2);
    *(u32x4*)p = d;
    *(u32x4*)(p + LA_STR) = (u32x4){o0, o1, o2, o3};
    *(u32x4*)(p + 2 * LA_STR) = (u32x4){p3, d[0], d[1], d[2]};
    *(u32x4*)(p + 3 * LA_STR) = (u32x4){__builtin_amdgcn_alignbyte(p3, p2, 2), o0, o1, o2};
}
__device__ __forceinline__ void put_x4(char* p, uint32_t o0, uint32_t o1) {
    *(u32x2*)(p + XA_OFF) = (u32x2){o0, o1};
    *(uint32_t*)(p + XB_OFF - 4) = o0;
    *(uint32_t*)(p + XB_OFF) = o1;
}
__device__ __forceinline__ uint32_t prev_lane(uint32_t v, uint32_t carry) {
    return (uint32_t)__builtin_amdgcn_update_dpp((int)carry, (int)v, 0x138  , 0xf, 0xf, false);
}
typedef __attribute__((address_space(3))) char* lds_t;
typedef __attribute__((address_space(3))) const volatile u32x2* lds_v2;
typedef __attribute__((address_space(3))) volatile uint32_t* lds_vu;
typedef __attribute__((address_space(3))) const bf16x8* lds_k8;
typedef __attribute__((address_space(1))) const char* gc_t;
typedef __attribute__((address_space(1))) char* gw_t;
#define GLD(T, base, voff, imm) (*(__attribute__((address_space(1))) const T*)((base) + (voff) + (imm)))
#define GLDN(T, base, voff, imm) __builtin_nontemporal_load((__attribute__((address_space(1))) const T*)((base) + (voff) + (imm)))
#define QW 0
#define NT_PF 0
#define NT_ONCE 1
#define NT_DEM 0
#define NT_ST 0
#define GLDD GLD
#define LD1(p) __builtin_nontemporal_load(p)
__device__ __forceinline__ bf16x8 ldx(lds_t p) {
    u32x2 a = *(lds_v2)p, b = *(lds_v2)(p + 8);
    u32x4 v = {a[0], a[1], b[0], b[1]};
    return __builtin_bit_cast(bf16x8, v);
}
__device__ __forceinline__ bf16x8 ldk(lds_t p) { return *(lds_k8)p; }
#define MFMA(a, b, c) c = __builtin_amdgcn_mfma_f32_16x16x32_bf16(a, b, c, 0, 0, 0)
#define SG_DS(n) __builtin_amdgcn_sched_group_barrier(0x100, n, 0)
#define SG_MF(n) __builtin_amdgcn_sched_group_barrier(0x008, n, 0)

#define RB(r) "v_cvt_pk_bf16_f32 " r ", 0, " r "\n\t"
__device__ __forceinline__ void kv4(const float (&k)[7], const float (&wt)[4], const float (&v)[4], uint32_t& o0, uint32_t& o1) {
    float a0, a1;
    asm("v_mul_f32 %2, %4, %14\n\t" "v_mul_f32 %3, %5, %14\n\t" "v_mul_f32 %0, %6, %14\n\t" "v_mul_f32 %1, %7, %14\n\t"
        "v_fmac_f32 %2, %5, %13\n\t" "v_fmac_f32 %3, %6, %13\n\t" "v_fmac_f32 %0, %7, %13\n\t" "v_fmac_f32 %1, %8, %13\n\t"
        "v_fmac_f32 %2, %6, %12\n\t" "v_fmac_f32 %3, %7, %12\n\t" "v_fmac_f32 %0, %8, %12\n\t" "v_fmac_f32 %1, %9, %12\n\t"
        "v_fmac_f32 %2, %7, %11\n\t" "v_fmac_f32 %3, %8, %11\n\t" "v_fmac_f32 %0, %9, %11\n\t" "v_fmac_f32 %1, %10, %11\n\t"
        RB("%2") RB("%3") RB("%0") RB("%1")
        "v_mul_f32 %2, %2, %15\n\t" "v_mul_f32 %3, %3, %16\n\t" "v_mul_f32 %0, %0, %17\n\t" "v_mul_f32 %1, %1, %18\n\t"
        "v_cvt_pk_bf16_f32 %0, %1, %0\n\t" "v_cvt_pk_bf16_f32 %1, %3, %2\n\t"
        : "=&v"(o0), "=&v"(o1), "=&v"(a0), "=&v"(a1)
        : "v"(k[0]), "v"(k[1]), "v"(k[2]), "v"(k[3]), "v"(k[4]), "v"(k[5]), "v"(k[6]),
          "v"(wt[0]), "v"(wt[1]), "v"(wt[2]), "v"(wt[3]), "v"(v[0]), "v"(v[1]), "v"(v[2]), "v"(v[3]));
}
__device__ __forceinline__ void epi_acc(const f32x4 a, char* dst) {
    uint32_t o0, o1;
    asm("v_cvt_pk_bf16_f32 %0, %3, %2\n\tv_cvt_pk_bf16_f32 %1, %5, %4" : "=&v"(o0), "=v"(o1) : "v"(a[0]), "v"(a[1]), "v"(a[2]), "v"(a[3]));
    *(uint32_t*)(dst) = o0;
    *(uint32_t*)(dst - 64) = o1;
}
template <int STR>
__device__ __forceinline__ u32x4 row_read(const char* a0) {
    s16x4 v0 = __builtin_amdgcn_ds_read_tr16_b64_v4i16((__attribute__((address_space(3))) s16x4*)(a0));
    s16x4 v1 = __builtin_amdgcn_ds_read_tr16_b64_v4i16((__attribute__((address_space(3))) s16x4*)(a0 + 4 * STR));
    u32x2 p0 = __builtin_bit_cast(u32x2, v0), p1 = __builtin_bit_cast(u32x2, v1);
    return (u32x4){p0[0], p0[1], p1[0], p1[1]};
}
__device__ __forceinline__ void gate2(uint32_t y0, uint32_t y1, uint32_t q0, uint32_t q1, uint32_t& o0, uint32_t& o1) {
    float t0, t1, t2, t3;
    asm("v_lshlrev_b32 %2, 16, %6\n\t" "v_lshlrev_b32 %0, 16, %8\n\t" "v_and_b32 %3, 0xffff0000, %6\n\t" "v_and_b32 %1, 0xffff0000, %8\n\t"
        "v_mul_f32 %2, %2, %0\n\t" "v_mul_f32 %3, %3, %1\n\t"
        "v_lshlrev_b32 %4, 16, %7\n\t" "v_lshlrev_b32 %0, 16, %9\n\t" "v_and_b32 %5, 0xffff0000, %7\n\t" "v_and_b32 %1, 0xffff0000, %9\n\t"
        "v_mul_f32 %4, %4, %0\n\t" "v_mul_f32 %5, %5, %1\n\t"
        "v_cvt_pk_bf16_f32 %0, %2, %3\n\t" "v_cvt_pk_bf16_f32 %1, %4, %5\n\t"
        : "=&v"(o0), "=&v"(o1), "=&v"(t0), "=&v"(t1), "=&v"(t2), "=&v"(t3)
        : "v"(y0), "v"(y1), "v"(q0), "v"(q1));
}
__device__ __forceinline__ u32x4 gate(u32x4 y, u32x4 q) {
    uint32_t o0, o1, o2, o3;
    gate2(y[0], y[1], q[0], q[1], o0, o1);
    gate2(y[2], y[3], q[2], q[3], o2, o3);
    return (u32x4){o0, o1, o2, o3};
}
__device__ __forceinline__ void lds_barrier() {
    __builtin_amdgcn_fence(__ATOMIC_RELEASE, "workgroup", "local");
    __builtin_amdgcn_s_barrier();
    __builtin_amdgcn_fence(__ATOMIC_ACQUIRE, "workgroup", "local");
}
__device__ __forceinline__ void flag_set(lds_t p) {
    asm volatile("" ::: "memory");
    *(lds_vu)p = 1u;
    asm volatile("" ::: "memory");
}
__device__ __forceinline__ void flag_wait(lds_t p) {
    while (__builtin_amdgcn_ballot_w64(*(lds_vu)p == 0u) != 0ull) __builtin_amdgcn_s_sleep(1);
    __builtin_amdgcn_fence(__ATOMIC_ACQUIRE, "workgroup", "local");
}

#define FD 8
#define FE 7
#define NMF 288
#define FEZ(st) ((FEM >> (st)) & 1)
#define FES(st) (FEZ(st) ? 0 : FE)
#define PD 1
#define PM 0
#define BPRIO 1
#define KEEP2N 0
#define NFR 272
struct Sched {
    unsigned char acc[NMF];
    short ka[NMF], xa[NMF];
    short off[NFR], isx[NFR], at[NFR];
    short beg[6], lo[NMF + 1], cnt[NMF + 1];
};
constexpr Sched make_sched() {
    constexpr int np[5] = {8, 13, 9, 5, 1};
    constexpr int pr[5][13][3] = {
        {{0, 0, 0}, {1, 0, 1}, {2, 0, 2}, {3, 0, 3}, {4, 0, 4}, {5, 0, 5}, {6, 0, 6}, {7, 0, 7}},
        {{0, 1, 1}, {1, 1, 2}, {2, 1, 3}, {3, 1, 4}, {0, 2, 2}, {4, 1, 5}, {1, 2, 3}, {5, 1, 6}, {2, 2, 4}, {6, 1, 7}, {3, 2, 5}, {4, 2, 6}, {5, 2, 7}},
        {{0, 3, 3}, {1, 3, 4}, {2, 3, 5}, {3, 3, 6}, {0, 4, 4}, {4, 3, 7}, {1, 4, 5}, {2, 4, 6}, {3, 4, 7}},
        {{0, 5, 5}, {1, 5, 6}, {2, 5, 7}, {0, 6, 8}, {1, 6, 9}},
        {{0, 7, 7}}};
    Sched s = {};
    int p = 0, nf = 0;
    int kprev[8][8] = {};
    constexpr int keep[5] = {0, 0, KEEP2, KEEP3, KEEP4};
    for (int st = 0; st < 5; ++st) {
        s.beg[st] = (short)p;
        const int gate = st ? p - FES(st) : 0;
        for (int c = 0; c < 8; ++c) {
            int kid[8] = {-1, -1, -1, -1, -1, -1, -1, -1}, xid[8] = {-1, -1, -1, -1, -1, -1, -1, -1};
            for (int i = 0; i < np[st]; ++i, ++p) {
                const int m = pr[st][i][0], x = pr[st][i][1];
                if (xid[x] < 0) {
                    xid[x] = nf; s.off[nf] = (short)(512 * (7 - x) + 64 * c); s.isx[nf] = 1;
                    s.at[nf] = (short)(p - FD < gate ? gate : p - FD); ++nf;
                }
                if (kid[m] < 0 && (((keep[st] >> m) & 1) || (st == 3 && m == 1 && c < KEEP3N) || (st == 2 && m == 0 && c < KEEP2N))) kid[m] = kprev[m][c];
                if (kid[m] < 0) {
                    kprev[m][c] = nf;
                    kid[m] = nf; s.off[nf] = (short)(512 * m + 64 * c); s.isx[nf] = 0;
                    s.at[nf] = (short)(p - FD < 0 ? -1 : p - FD); ++nf;
                }
                s.ka[p] = (short)kid[m]; s.xa[p] = (short)xid[x];
                s.acc[p] = (unsigned char)(st == 4 && (c & 1) ? 9 : pr[st][i][2]);
            }
        }
    }
    s.beg[5] = (short)p;
    for (int f = 0; f < nf; ++f) {
        const int q = s.at[f] + 1;
        if (!s.cnt[q]) s.lo[q] = (short)f;
        s.cnt[q] = (short)(f - s.lo[q] + 1);
    }
    return s;
}
constexpr Sched SCH = make_sched();
struct BState {
    lds_t kaddr, xaddr, fl8, sfl;
    char* sg;
    uint32_t pv;
    bf16x8 f[NFR];
    f32x4 acc[10];
};
template <int P, int F>
__device__ __forceinline__ void b_load(BState& s) {
    if constexpr (SCH.at[F] == P) s.f[F] = SCH.isx[F] ? ldx(s.xaddr + SCH.off[F]) : ldk(s.kaddr + SCH.off[F]);
}
template <int P, int... F>
__device__ __forceinline__ void b_loads_(BState& s, std::integer_sequence<int, F...>) { (b_load<P, SCH.lo[P + 1] + F>(s), ...); }
template <int P>
__device__ __forceinline__ void b_loads(BState& s) { b_loads_<P>(s, std::make_integer_sequence<int, SCH.cnt[P + 1]>{}); }
__device__ __forceinline__ f32x4 add4(f32x4 a, f32x4 b) {
#pragma unroll
    for (int i = 0; i < 4; ++i) {
        float t = a[i] + b[i];
        asm volatile("" : "+v"(t));
        a[i] = t;
    }
    return a;
}
template <int P>
__device__ __forceinline__ void b_slot(BState& s) {
#define AT(st, d) (P == SCH.beg[st] + (d))
#define FLG(st) (s.fl8 + ((st) == 4 ? 96 : 32 * (st) - 16))
    constexpr int st = AT(1, -FES(1)) ? 1 : AT(2, -FES(2)) ? 2 : AT(3, -FES(3)) ? 3 : AT(4, -FES(4)) ? 4 : 0;
    constexpr int sp = AT(1, -FES(1) - 6) ? 1 : AT(2, -FES(2) - 6) ? 2 : AT(3, -FES(3) - 6) ? 3 : AT(4, -FES(4) - 6) ? 4 : 0;
    if constexpr (sp) s.pv = *(lds_vu)FLG(sp);
    if constexpr (st == 1 && FEZ(1)) { epi_acc(s.acc[0], s.sg); flag_set(s.sfl); }
    if constexpr (st == 2 && FEZ(2)) { epi_acc(s.acc[1], s.sg - 512); epi_acc(s.acc[2], s.sg - 1024); flag_set(s.sfl + 32); }
    if constexpr (st == 3 && FEZ(3)) { epi_acc(s.acc[3], s.sg - 1536); epi_acc(s.acc[4], s.sg - 2048); flag_set(s.sfl + 64); }
    if constexpr (st == 4 && FEZ(4)) { epi_acc(s.acc[5], s.sg - 2560); epi_acc(add4(s.acc[6], s.acc[8]), s.sg - 3072); flag_set(s.sfl + 96); }
    if constexpr (st) {
        if (__builtin_amdgcn_ballot_w64(s.pv == 0u) != 0ull) flag_wait(FLG(st));
        asm volatile("" ::: "memory");
        SB();
    }
    b_loads<P>(s);
    MFMA(s.f[SCH.ka[P]], s.f[SCH.xa[P]], s.acc[SCH.acc[P]]);
    if constexpr (AT(2, 3)) __builtin_amdgcn_s_setprio(1);
    if constexpr (AT(3, 3)) __builtin_amdgcn_s_setprio(0);
    if constexpr (AT(1, 2) && !FEZ(1)) { epi_acc(s.acc[0], s.sg); flag_set(s.sfl); }
    if constexpr (AT(2, 1) && !FEZ(2)) epi_acc(s.acc[1], s.sg - 512);
    if constexpr (AT(2, 2) && !FEZ(2)) { epi_acc(s.acc[2], s.sg - 1024); flag_set(s.sfl + 32); }
    if constexpr (AT(3, 1) && !FEZ(3)) epi_acc(s.acc[3], s.sg - 1536);
    if constexpr (AT(3, 2) && !FEZ(3)) { epi_acc(s.acc[4], s.sg - 2048); flag_set(s.sfl + 64); }
    if constexpr (AT(4, 0) && !FEZ(4)) epi_acc(s.acc[5], s.sg - 2560);
    if constexpr (AT(4, 1) && !FEZ(4)) { epi_acc(add4(s.acc[6], s.acc[8]), s.sg - 3072); flag_set(s.sfl + 96); }
    SB();
}
template <int... P>
__device__ __forceinline__ void b_slots(BState& s, std::integer_sequence<int, P...>) { (b_slot<P>(s), ...); }

typedef u32x2 __attribute__((aligned(4))) u32x2_a4;
typedef u32x4 __attribute__((aligned(8))) u32x4_a8;
__device__ __forceinline__ bf16x8 ld_a4(lds_t p) {
    const u32x2 a = *(__attribute__((address_space(3))) const u32x2_a4*)p, b = *(__attribute__((address_space(3))) const u32x2_a4*)(p + 8);
    return __builtin_bit_cast(bf16x8, ((u32x4){a[0], a[1], b[0], b[1]}));
}
__device__ __forceinline__ bf16x8 ld_a8(lds_t p) { return __builtin_bit_cast(bf16x8, (u32x4)*(__attribute__((address_space(3))) const u32x4_a8*)p); }
#define MF0(a, b) __builtin_amdgcn_mfma_f32_16x16x32_bf16(a, b, ((f32x4){0, 0, 0, 0}), 0, 0, 0)
#define XFRAG(sig) ldk(xb + 64 * (56 - (sig)))
template <int TA, int TB>
__device__ __forceinline__ void far_field(lds_t l0, lds_t lx, int tid, f32x4 (&acc)[8]) {
    OPAQUE(tid);
    const int g = (tid >> 4) & 3, fr = tid & 15;
    lds_t xb = lx + 480 - 32 * fr + 16 * g, cw = l0 + CW_OFF + 16 * (tid & 63);
    lds_t k2a = lx + L2_OFF + 96 + 8 * fr - 32 * g, k3a = lx + L3_OFF + 48 + 4 * fr - 16 * g;
    OPAQUE(xb); OPAQUE(cw); OPAQUE(k2a); OPAQUE(k3a);
    bf16x8 x1 = ldk(xb + 64 * (56 - 8 * TA) + 16), x2 = XFRAG(8 * TA);
    const bf16x8 c1 = ldk(cw), c2 = ldk(cw + 1024), c4 = ldk(cw + 2048), c3 = ldk(cw + 3072);
    bf16x8 k2[3][2], k3[4];
#pragma unroll
    for (int m = 0; m < 3; ++m) { k2[m][0] = ldx(k2a + 128 * m + 16); k2[m][1] = ldx(k2a + 128 * m); }
#pragma unroll
    for (int m = 0; m < 4 - TA; ++m) k3[m] = ld_a4(k3a + 64 * m);
    f32x4 d[4], e[2];
    d[0] = MF0(x1, c1); d[1] = MF0(x1, c2); d[2] = MF0(x2, c1); d[3] = MF0(x2, c2);
    e[0] = MF0(x1, c4); e[1] = MF0(x2, c3);
    SB();
#pragma unroll
    for (int T = TA; T <= TB; ++T) {
        if (T < TB) { x1 = ldk(xb + 64 * (56 - 8 * (T + 1)) + 16); x2 = XFRAG(8 * (T + 1)); }
        u32x4 r2[2], r3;
#pragma unroll
        for (int h = 0; h < 2; ++h)
            r2[h] = (u32x4){pk_bf16(d[3][2 * h + 1], d[2][2 * h + 1]), pk_bf16(d[1][2 * h + 1], d[0][2 * h + 1]),
                            pk_bf16(d[3][2 * h], d[2][2 * h]), pk_bf16(d[1][2 * h], d[0][2 * h])};
        if (T < 4) r3 = (u32x4){pk_bf16(e[1][3], e[0][3]), pk_bf16(e[1][2], e[0][2]), pk_bf16(e[1][1], e[0][1]), pk_bf16(e[1][0], e[0][0])};
        SB();
        if (T < TB) {
            d[0] = MF0(x1, c1); d[1] = MF0(x1, c2); d[2] = MF0(x2, c1); d[3] = MF0(x2, c2);
            if (T + 1 < 4) { e[0] = MF0(x1, c4); e[1] = MF0(x2, c3); }
        }
        SB();
#pragma unroll
        for (int h = 0; h < 2; ++h)
#pragma unroll
            for (int m = 2; m < 5; ++m)
                if (T + m < 8) MFMA(__builtin_bit_cast(bf16x8, r2[h]), k2[m - 2][h], acc[T + m]);
#pragma unroll
        for (int m = 7; m >= 4; --m)
            if (T + m < 8) MFMA(__builtin_bit_cast(bf16x8, r3), k3[m - 4], acc[T + m]);
        SB();
    }
}
__device__ __forceinline__ void epi_a(const f32x4 a, lds_t dst) {
    uint32_t o0, o1;
    asm("v_cvt_pk_bf16_f32 %0, %5, %4\n\tv_cvt_pk_bf16_f32 %1, %3, %2" : "=&v"(o0), "=v"(o1) : "v"(a[0]), "v"(a[1]), "v"(a[2]), "v"(a[3]));
    *(__attribute__((address_space(3))) u32x2*)dst = (u32x2){o0, o1};
}
#define NEAR_ADDR() \
    OPAQUE(tid); \
    const int g = (tid >> 4) & 3, fr = tid & 15; \
    lds_t xb = lx + 480 - 32 * fr + 16 * g; \
    lds_t kfa = lb + LA_OFF + (~fr & 3) * LA_STR + 8 + 8 * (fr >> 2) + 16 * g; \
    lds_t sg = lb + 504 - 32 * fr - 8 * g, xf = l0 + FLAG_OFF + 4 * (tid & 3); \
    OPAQUE(xb); OPAQUE(kfa); OPAQUE(sg); OPAQUE(xf)
__device__ __forceinline__ void near_lo(lds_t l0, lds_t lb, lds_t lx, int tid, uint32_t kk, lds_t sf, f32x4 (&acc)[8]) {
    NEAR_ADDR();
    bf16x8 kf[3][2], xq[3][4];
#define NLD(c) do { kf[(c) % 3][0] = ldx(kfa + 64 * (c)); kf[(c) % 3][1] = ldx(kfa + 64 * (c) + 512); \
        _Pragma("unroll") for (int t = 0; t < 4; ++t) xq[(c) % 3][t] = XFRAG(8 * t - (c)); } while (0)
    NLD(0);
    const bf16x8 k16 = ldx(kfa + 64 * 16);
    const bf16x8 kb = __builtin_bit_cast(bf16x8, __builtin_bit_cast(u32x4, ldk(l0 + CW_OFF + 4096 + 16 * (tid & 63))) & kk);
    NLD(1);
    SB();
#pragma unroll
    for (int c = 0; c < 8; ++c) {
        if (c + 2 < 8) NLD(c + 2);
#pragma unroll
        for (int t = 0; t < 4; ++t) MFMA(kf[c % 3][0], xq[c % 3][t], acc[t]);
#pragma unroll
        for (int t = 0; t < 4; ++t) MFMA(kf[c % 3][1], xq[c % 3][t], acc[t + 1]);
        if (c + 2 < 8) {
#pragma unroll
            for (int i = 0; i < 8; ++i) { SG_MF(1); SG_DS(1); }
        }
        SB();
        if (c == 0) {
#pragma unroll
            for (int t = 0; t < 4; ++t) MFMA(kb, xq[0][t], acc[t + 4]);
#pragma unroll
            for (int t = 0; t < 4; ++t) MFMA(k16, xq[0][t], acc[t + 2]);
            SB();
        }
    }
#pragma unroll
    for (int t = 0; t < 4; ++t) epi_a(acc[t], sg + 512 * (7 - t));
    flag_set(sf);
}
#define NXQ 6
__device__ __forceinline__ void near_hi(lds_t l0, lds_t lb, lds_t lx, int tid, lds_t sf, f32x4 (&acc)[8]) {
    NEAR_ADDR();
    bf16x8 kf[17], xq[NXQ];
#pragma unroll
    for (int j = 0; j < 8; ++j) {
        if (j < NXQ - 1) xq[j] = XFRAG(25 + j);
        kf[7 - j] = ldx(kfa + 64 * (7 - j)); kf[15 - j] = ldx(kfa + 64 * (15 - j));
    }
    kf[16] = ldx(kfa + 64 * 16);
    f32x4 a7b = {0, 0, 0, 0};
    SB();
#pragma unroll
    for (int T = 4; T < 8; ++T) {
#pragma unroll
        for (int s = 0; s < 8; ++s) {
            const int n = 8 * (T - 4) + s;
            if (s == 9 - NXQ && (T == 5 || T == 6)) flag_wait(xf + 16 * (T + 1));
            if (n + NXQ - 1 < 32) xq[(n + NXQ - 1) % NXQ] = XFRAG(25 + n + NXQ - 1);
            if (T == 7 && (s & 1)) MFMA(kf[7 - s], xq[n % NXQ], a7b);
            else MFMA(kf[7 - s], xq[n % NXQ], acc[T]);
            if (T + 1 < 8) MFMA(kf[15 - s], xq[n % NXQ], acc[T + 1]);
            if (s == 7 && T + 2 < 8) MFMA(kf[16], xq[n % NXQ], acc[T + 2]);
            if (s == 1 && T > 4) {
                epi_a(acc[T - 1], sg + 512 * (8 - T));
                flag_set(sf + 32 * (T - 4));
            }
            SB();
        }
    }
#pragma unroll
    for (int i = 0; i < 4; ++i) {
        float s = acc[7][i] + a7b[i];
        asm volatile("" : "+v"(s));
        acc[7][i] = s;
    }
    epi_a(acc[7], sg);
    flag_set(sf + 32 * 4);
}

#define AFD 7
#define AFE 5
#define AS2 4
#define AS3 6
#define APP 0
#define NAO 202
#define NAF 200
enum { F_KF, F_KB, F_XF, F_CW, F_K2, F_K3 };
struct ASched {
    unsigned char kind[NAO], dst[NAO];
    short a[NAO], b[NAO];
    short fk[NAF], off[NAF], at[NAF];
    short beg[5], sums[3][2], epi[5], lo[NAO + 1], cnt[NAO + 1];
    int n, nf, gate;
    constexpr int frag(int k, int o) {
        fk[nf] = (short)k; off[nf] = (short)o;
        const int lim = k == F_XF ? gate : -1;
        at[nf] = (short)(n - AFD < lim ? lim : n - AFD);
        return nf++;
    }
    constexpr void op(int k, int d, int x, int y) { kind[n] = (unsigned char)k; dst[n] = (unsigned char)d; a[n] = (short)x; b[n] = (short)y; ++n; }
};
#define XO(sig) (64 * (56 - (sig)))
constexpr ASched make_asched() {
    ASched s = {};
    for (int st = 0; st < 4; ++st) {
        const int a = 2 * st, b = a + 1;
        s.beg[st] = (short)s.n;
        s.gate = st ? s.n - AFE : 0;
        if (st < 3) {
            int cw[4] = {0, 0, 0, 0}, xa0 = 0;
            for (int hf = 0; hf < 2; ++hf) {
                const int T = a + hf;
                const int x1 = s.frag(F_XF, XO(8 * T) + 16);
                if (!hf) cw[0] = s.frag(F_CW, 0);
                s.op(1, 0, x1, cw[0]);
                if (!hf) cw[1] = s.frag(F_CW, 1024);
                s.op(1, 1, x1, cw[1]);
                const int x2 = s.frag(F_XF, XO(8 * T));
                if (!hf) xa0 = x2;
                s.op(1, 2, x2, cw[0]);
                s.op(1, 3, x2, cw[1]);
                if (T < 4) {
                    if (!hf) cw[2] = s.frag(F_CW, 2048);
                    s.op(1, 4, x1, cw[2]);
                    if (!hf) cw[3] = s.frag(F_CW, 3072);
                    s.op(1, 5, x2, cw[3]);
                }
                s.sums[st][hf] = (short)s.n;
                for (int c = 4 * hf; c < 4 * hf + 4; ++c) {
                    const int k0 = s.frag(F_KF, 64 * c), xb = s.frag(F_XF, XO(8 * b - c));
                    s.op(0, b, k0, xb);
                    const int xa = c ? s.frag(F_XF, XO(8 * a - c)) : xa0;
                    s.op(0, a, k0, xa);
                    const int k1 = s.frag(F_KF, 64 * c + 512);
                    s.op(0, b, k1, xa);
                    s.op(0, b + 1, k1, xb);
                    if (c == 0) {
                        const int k16 = s.frag(F_KF, 1024);
                        s.op(0, b + 2, k16, xb);
                        if (st < 2) {
                            const int kb = s.frag(F_KB, 4096);
                            s.op(0, a + 4, kb, xa);
                            s.op(0, b + 4, kb, xb);
                        }
                        s.op(0, a + 2, k16, xa);
                    }
                }
            }
            s.epi[st] = (short)(s.n + 1);
            for (int h = 0; h < 2; ++h) {
                if (st < 2) {
                    for (int i = 0; i < 3; ++i) {
                        const int m = i == 0 ? 4 : i == 1 ? 2 : 3, k = s.frag(F_K2, 128 * (m - 2) + (h ? 0 : 16));
                        if (m == 3) { s.op(2, b + m, 3 + h, k); s.op(2, a + m, h, k); }
                        else { s.op(2, a + m, h, k); s.op(2, b + m, 3 + h, k); }
                    }
                } else {
                    const int k = s.frag(F_K2, h ? 0 : 16);
                    s.op(2, 8, 3 + h, k);
                    s.op(2, 6, h, k);
                    const int k3 = s.frag(F_K2, 128 + (h ? 0 : 16));
                    s.op(2, 7, h, k3);
                }
            }
            if (st == 0) {
                int k = s.frag(F_K3, 128); s.op(2, 6, 2, k); s.op(2, 7, 5, k);
                k = s.frag(F_K3, 0); s.op(2, 4, 2, k); s.op(2, 5, 5, k);
                k = s.frag(F_K3, 192); s.op(2, 7, 2, k);
                k = s.frag(F_K3, 64); s.op(2, 6, 5, k); s.op(2, 5, 2, k);
            } else if (st == 1) {
                int k = s.frag(F_K3, 0); s.op(2, 7, 5, k); s.op(2, 6, 2, k);
                k = s.frag(F_K3, 64); s.op(2, 7, 2, k);
            }
        } else {
            int k0[8] = {0, 0, 0, 0, 0, 0, 0, 0};
            for (int c = 0; c < 8; ++c) {
                k0[c] = s.frag(F_KF, 64 * c);
                const int x = s.frag(F_XF, XO(48 - c));
                s.op(0, 6, k0[c], x);
                const int k1 = s.frag(F_KF, 64 * c + 512);
                s.op(0, 7, k1, x);
            }
            s.epi[3] = (short)(s.n + 1);
            for (int c = 0; c < 8; ++c) s.op(0, (c & 1) ? 7 : 8, k0[c], s.frag(F_XF, XO(56 - c)));
        }
    }
    s.beg[4] = (short)s.n;
    for (int f = 0; f < s.nf; ++f) {
        const int q = s.at[f] + 1;
        if (!s.cnt[q]) s.lo[q] = (short)f;
        s.cnt[q] = (short)(f - s.lo[q] + 1);
    }
    return s;
}
constexpr ASched make_asched2() {
    ASched s = {};
    int cw[4] = {0, 0, 0, 0}, k2[3][2] = {}, k3[4] = {};
    auto sums = [&](int T, bool ldc) {
        const int tb = 6 * (T & 1);
        const int x1 = s.frag(F_XF, XO(8 * T) + 16);
        if (ldc) cw[0] = s.frag(F_CW, 0);
        s.op(1, tb, x1, cw[0]);
        if (ldc) cw[1] = s.frag(F_CW, 1024);
        s.op(1, tb + 1, x1, cw[1]);
        const int x2 = s.frag(F_XF, XO(8 * T));
        s.op(1, tb + 2, x2, cw[0]);
        s.op(1, tb + 3, x2, cw[1]);
        if (T < 4) {
            if (ldc) cw[2] = s.frag(F_CW, 2048);
            s.op(1, tb + 4, x1, cw[2]);
            if (ldc) cw[3] = s.frag(F_CW, 3072);
            s.op(1, tb + 5, x2, cw[3]);
        }
        s.sums[T >> 1][T & 1] = (short)(s.n + (T < 4 ? 0 : 1));
    };
    auto prods = [&](int T, bool ldk) {
        const int r = 3 * (T & 1);
        for (int h = 0; h < 2; ++h)
            for (int m = 2; m < 5; ++m) {
                if (T + m > 7) continue;
                if (ldk) k2[m - 2][h] = s.frag(F_K2, 128 * (m - 2) + (h ? 0 : 16));
                s.op(2, T == 5 && !h ? 8 : T + m, r + h, k2[m - 2][h]);
            }
        for (int m = 7; m >= 4; --m) {
            if (T + m > 7) continue;
            if (ldk) k3[m - 4] = s.frag(F_K3, 64 * (m - 4));
            s.op(2, T == 3 ? 8 : T + m, r + 2, k3[m - 4]);
        }
    };
    s.beg[0] = 0; s.gate = 0;
    sums(0, true); sums(1, false); prods(0, true); prods(1, false);
    s.beg[1] = (short)s.n; s.gate = s.n - AFE;
    for (int c = 0; c < 8; ++c) {
        int x[4] = {0, 0, 0, 0};
        const int k0 = s.frag(F_KF, 64 * c);
        for (int t = 0; t < 4; ++t) { x[t] = s.frag(F_XF, XO(8 * t - c)); s.op(0, t, k0, x[t]); }
        const int k1 = s.frag(F_KF, 64 * c + 512);
        for (int t = 0; t < 4; ++t) s.op(0, t + 1, k1, x[t]);
        if (c == 0) {
            const int kb = s.frag(F_KB, 4096);
            for (int t = 3; t >= 0; --t) s.op(0, t + 4, kb, x[t]);
            const int k16 = s.frag(F_KF, 1024);
            for (int t = 0; t < 4; ++t) s.op(0, t + 2, k16, x[t]);
        }
        if (c == AS2) sums(2, true);
        if (c == AS3) sums(3, false);
    }
    s.epi[0] = (short)s.n;
    prods(2, true); prods(3, false);
    s.beg[2] = (short)s.n; s.gate = s.n - AFE;
    sums(4, true); sums(5, false);
    int kf[17] = {};
    for (int T = 4; T < 8; ++T) {
        if (T == 6) { s.beg[3] = (short)s.n; s.gate = s.n - AFE; }
        for (int j = 0; j < 8; ++j) {
            const int x = s.frag(F_XF, XO(8 * T - 7 + j));
            if (T == 4) kf[7 - j] = s.frag(F_KF, 64 * (7 - j));
            s.op(0, T == 7 && (j & 1) ? 8 : T, kf[7 - j], x);
            if (T == 4) kf[15 - j] = s.frag(F_KF, 64 * (15 - j));
            if (T < 7) s.op(0, T + 1, kf[15 - j], x);
            if (j == 7 && T < 6) {
                if (T == 4) kf[16] = s.frag(F_KF, 1024);
                s.op(0, T + 2, kf[16], x);
            }
            if (T == 4 && j == 1) { prods(4, true); prods(5, false); }
        }
        if (T < 7) s.epi[T - 3] = (short)(s.n + 1);
    }
    s.beg[4] = (short)s.n;
    for (int f = 0; f < s.nf; ++f) {
        const int q = s.at[f] + 1;
        if (!s.cnt[q]) s.lo[q] = (short)f;
        s.cnt[q] = (short)(f - s.lo[q] + 1);
    }
    return s;
}
constexpr ASched ASC = make_asched2();
#define TB(q) (6 * ((q) & 1))
struct AState {
    lds_t xb, kfa, cw, k2a, k3a, sg, fl8, sf;
    uint32_t kk, pv;
    bool hiw;
    bf16x8 f[NAF];
    f32x4 acc[9], t[12];
    u32x4 R[6];
};
template <int P, int F>
__device__ __forceinline__ void a_load(AState& s) {
    if constexpr (ASC.at[F] == P) {
        constexpr int k = ASC.fk[F], o = ASC.off[F];
        if constexpr (k == F_KF) s.f[F] = ldx(s.kfa + o);
        else if constexpr (k == F_XF) s.f[F] = ldk(s.xb + o);
        else if constexpr (k == F_K2) s.f[F] = ldx(s.k2a + o);
        else if constexpr (k == F_K3) s.f[F] = ld_a4(s.k3a + o);
        else s.f[F] = ldk(s.cw + o);
    }
}
template <int P, int... F>
__device__ __forceinline__ void a_loads_(AState& s, std::integer_sequence<int, F...>) { (a_load<P, ASC.lo[P + 1] + F>(s), ...); }
template <int P>
__device__ __forceinline__ void a_loads(AState& s) { a_loads_<P>(s, std::make_integer_sequence<int, ASC.cnt[P + 1]>{}); }
template <int P>
__device__ __forceinline__ void a_slot(AState& s) {
#define AAT(st, d) (P == ASC.beg[st] + (d))
    constexpr int st = AAT(1, -AFE) ? 1 : AAT(2, -AFE) ? 2 : AAT(3, -AFE) ? 3 : 0;
    constexpr int sp = AAT(1, -AFE - 6) ? 1 : AAT(2, -AFE - 6) ? 2 : AAT(3, -AFE - 6) ? 3 : 0;
    if constexpr (sp) s.pv = *(lds_vu)(s.fl8 + 32 * sp);
    if constexpr (st) {
        if (__builtin_amdgcn_ballot_w64(s.pv == 0u) != 0ull) flag_wait(s.fl8 + 32 * st);
        asm volatile("" ::: "memory");
        SB();
    }
    a_loads<P>(s);
    constexpr int k = ASC.kind[P], d = ASC.dst[P], a = ASC.a[P], b = ASC.b[P];
    if constexpr (k == 0 && ASC.fk[a] == F_KB && ASC.a[P - 1] != a)
        s.f[a] = __builtin_bit_cast(bf16x8, __builtin_bit_cast(u32x4, s.f[a]) & s.kk);
    if constexpr (k == 0) MFMA(s.f[a], s.f[b], s.acc[d]);
    else if constexpr (k == 1) s.t[d] = MF0(s.f[a], s.f[b]);
    else MFMA(__builtin_bit_cast(bf16x8, s.R[a]), s.f[b], s.acc[d]);
#pragma unroll
    for (int q = 0; q < 6; ++q) {
        const int i = P - ASC.sums[q >> 1][q & 1] - 1, r = 3 * (q & 1);
        if (i >= 0 && i < 4) {
            const int e = 2 * (i >> 1) + 1 - (i & 1);
            s.R[r + (i >> 1)][2 * (i & 1)] = pk_bf16(s.t[TB(q) + 3][e], s.t[TB(q) + 2][e]);
            s.R[r + (i >> 1)][2 * (i & 1) + 1] = pk_bf16(s.t[TB(q) + 1][e], s.t[TB(q)][e]);
        } else if (i >= 4 && i < 6 && q < 4) {
            const int e = 3 - 2 * (i - 4);
            s.R[r + 2][2 * (i - 4)] = pk_bf16(s.t[TB(q) + 5][e], s.t[TB(q) + 4][e]);
            s.R[r + 2][2 * (i - 4) + 1] = pk_bf16(s.t[TB(q) + 5][e - 1], s.t[TB(q) + 4][e - 1]);
        }
    }
#pragma unroll
    for (int t = 0; t < 4; ++t)
        if (P == ASC.epi[0] + t) { epi_a(s.acc[t], s.sg + 512 * (7 - t)); if (t == 3) flag_set(s.sf); }
#pragma unroll
    for (int q = 1; q < 4; ++q)
        if (P == ASC.epi[q]) { epi_a(s.acc[q + 3], s.sg + 512 * (4 - q)); flag_set(s.sf + 32 * q); }
    if constexpr (P == APS) { if (s.hiw) __builtin_amdgcn_s_setprio(0); else __builtin_amdgcn_s_setprio(APP); }
    SB();
}
template <int... P>
__device__ __forceinline__ void a_slots(AState& s, std::integer_sequence<int, P...>) { (a_slot<P>(s), ...); }

__global__ __launch_bounds__(1024) void fused_kernel(
    const uint16_t* __restrict__ u, const uint16_t* __restrict__ logA, const uint16_t* __restrict__ Aim,
    const uint16_t* __restrict__ Cre, const uint16_t* __restrict__ Cim, const uint16_t* __restrict__ logdt,
    const uint16_t* __restrict__ Dsk, const uint16_t* __restrict__ qin, const uint16_t* __restrict__ kin,
    const uint16_t* __restrict__ vin, const uint16_t* __restrict__ shk, const uint16_t* __restrict__ ssmk,
    const uint16_t* __restrict__ sD, uint16_t* __restrict__ out)
{
    __shared__ __attribute__((aligned(16))) char lds[LDS_TOTAL];
    uint64_t pc0;
    asm volatile("s_getpc_b64 %0" : "=s"(pc0));
    uint32_t pf = 0;
    if (threadIdx.x < CODE_BYTES / 128) pf = GLD(uint32_t, (gc_t)(pc0 & ~63ull), threadIdx.x * 128u, 0);
    SB();
    STDECL;
    RT(0, 0);
    STAMP(0, 0);
    STAMP(1, 0);
    const int b = blockIdx.x;
#define XSH 0
    const int xcd = (PERMA >> (4 * (b & 7))) & 7, idx = b >> 3, part = idx >> 4, gi = idx & 15, xcb = (PERMB >> (4 * (b & 7))) & 7, grp = (part ? xcb : xcd) * 16 + gi;
    const int lane = threadIdx.x & 63;
    const int w = __builtin_amdgcn_readfirstlane(threadIdx.x >> 6);
    const lds_t flags = (lds_t)lds + FLAG_OFF;
    const int g = lane >> 4, fr = lane & 15;
    {
        const u32x4 z = {0, 0, 0, 0};
        char* zp = w < 8 ? lds + w * CH_STRIDE + (lane < 4 ? XA_OFF + 4096 : XB_OFF + 4080 - 64) + 16 * lane : lds + FLAG_OFF + 16 * lane;
        if (lane < (w < 8 ? 9 : 19)) *(u32x4*)zp = z;
    }
    SB();
    KEEPS(u); KEEPS(logA); KEEPS(Aim); KEEPS(Cre); KEEPS(Cim); KEEPS(logdt); KEEPS(Dsk);
    KEEPS(qin); KEEPS(kin); KEEPS(vin); KEEPS(shk); KEEPS(ssmk); KEEPS(sD); KEEPS(out);
    const uint32_t chl = grp * 8 + w;
    u32x4 kq[4];
    uint32_t pr[4], dra, drb, dtr;
    if (w < 8) {
        if (part) {
            const gc_t ksrc = (gc_t)ssmk + (size_t)chl * (2 * LSEQ);
#pragma unroll
            for (int i = 0; i < 4; ++i) kq[i] = GLDN(u32x4, ksrc, 16u * lane, 1024 * i);
            drb = GLD(uint16_t, (gc_t)sD, 2 * chl, 0);
        } else {
            const uint32_t n = chl * (2 * NMODE) + 4 * lane;
            pr[0] = GLDN(uint32_t, (gc_t)logA, n, 0); pr[1] = GLDN(uint32_t, (gc_t)Aim, n, 0);
            pr[2] = GLDN(uint32_t, (gc_t)Cre, n, 0); pr[3] = GLDN(uint32_t, (gc_t)Cim, n, 0);
            dtr = GLD(uint16_t, (gc_t)logdt, 2 * chl, 0);
            dra = GLD(uint16_t, (gc_t)Dsk, 2 * chl, 0);
        }
    }
    SB();
    lds_barrier();
    SB();
    if (w >= 12) {
        const int rw = w - 12;
        __builtin_amdgcn_s_setprio(P_OUT);
        const int qq = (lane >> 2) & 3, pp = lane & 3;
        const char* rg = lds + (part ? qq * CH_STRIDE + KA_OFF + 464 : qq * SA) + 3584 + (64 * rw + 16 * g + 4 * pp) * 2;
        const uint32_t r0 = 255 - (64 * rw + lane);
        uint32_t oo = r0 * (4 * DCH) + part * (2 * DCH) + grp * 16;
        OPAQUE(oo);
#define ROW_OUT(T) (*(__attribute__((address_space(1))) u32x4*)((gw_t)out + (uint32_t)(oo + (T) * (256u * 4 * DCH))))
#define ROW_ST(T, v) ROW_OUT(T) = v
#define ROW_WAIT(S) flag_wait(flags + 128 + 32 * (S) + 4 * (lane & 7))
        if (part) {
            uint32_t qo = r0 * (2 * DCH) + grp * 16;
            OPAQUE(qo);
            u32x4 qv[8];
#define LDQ(T) qv[T] = GLDD(u32x4, (gc_t)qin, (uint32_t)(qo + (T) * (256u * 2 * DCH)), 0)
#define ROWB(T) ROW_ST(T, gate(row_read<CH_STRIDE>(rg - 512 * (T)), qv[T]))
#define KV_WAIT(b) flag_wait(flags + 16 * (b) + 4 * (lane & 3))
            KV_WAIT(G1); LDQ(0); LDQ(1); SB();
            STAMP(0, 1);
            ROW_WAIT(0); ROWB(0); SB();
            STAMP(0, 2);
            ROW_WAIT(1);
            ROWB(1); SB();
            KV_WAIT(G2); LDQ(2); LDQ(3); if (TS > 4) LDQ(4); SB();
            ROWB(2); SB();
            STAMP(0, 3);
            ROW_WAIT(2); ROWB(3); if (TS > 4) ROWB(4);
        } else {
            const uint32_t t = gi * 64 + lane;
            const gc_t pkv = (gc_t)((t & 2) ? vin : kin) + ((t >> 2) * (2 * DCH) + 256 * xcb + 128 * (t & 1));
            const uint32_t ouq = (t >> 1) * (2 * DCH) + 128 * (t & 1);
            const gc_t pu = (gc_t)u + (ouq + 256 * xcd), pq = (gc_t)qin + (ouq + 256 * xcb);
            uint64_t seq = rw == 0 ? SEQ0 : rw == 1 ? SEQ1 : rw == 2 ? SEQ2 : SEQ3;
            for (int d = rw == 0 ? DL0 : rw == 1 ? DL1 : rw == 2 ? DL2 : DL3; d > 0; --d) __builtin_amdgcn_s_sleep(8);
            gc_t la[5];
            uint32_t lf[5], lt[5];
#pragma unroll
            for (int i = 0; i < 5; ++i) {
                const uint32_t c = (uint32_t)(seq >> (8 * i)) & 0xffu;
                la[i] = c < 8 ? pkv + (size_t)c * (256 * 2 * DCH) : (c < 12 ? pu : pq) + (size_t)(c & 3) * (512 * 2 * DCH);
                lf[i] = (uint32_t)(size_t)(flags + ((c >> 2) == 2 ? 288 + 4 * (c & 3) : 304));
            }
#define LNK(i) "global_load_dword %" #i ", %1" #i ", off\n\t"
            const uint32_t sa = (uint32_t)(size_t)kin + 256 * xcb;
#define THM THR
            const uint32_t su = (uint32_t)(size_t)u + 256 * xcd;
            const int nsl = (((sa >> 7) & 7) == 3 || (((sa + 128) >> 7) & 7) == 3) ? THS : (((su >> 7) & 7) == 3 || (((su + 128) >> 7) & 7) == 3) ? THM : THR;
            const int s1 = 256 * ((gi >> 2) & 1) + 64 * (gi & 3) + 4 * (lane >> 2);
            uint32_t lo = (uint32_t)(s1 + 2) * (2 * DCH) + grp * 16 + 4 * pp;
            char* lx = lds + XR_OFF + 2 * pp * SX + 2 * (2044 - s1);
#pragma unroll 1
            for (int i = 0; i < 4; ++i, seq >>= 8) {
                if (i >= THF) for (int d = nsl; d > 0; --d) __builtin_amdgcn_s_sleep(2);
                const uint32_t c = (uint32_t)seq & 0xffu, m = c & 3;
                if ((c >> 2) == 2) {
                    uint32_t uu[4];
#pragma unroll
                    for (int r = 0; r < 4; ++r) uu[r] = GLD(uint32_t, (gc_t)u, (uint32_t)(lo + m * (512u * 2 * DCH)), (r - 2) * (2 * DCH));
#pragma unroll
                    for (int h = 0; h < 2; ++h) {
                        const uint32_t sel = h ? 0x07060302u : 0x05040100u;
                        *(u32x2*)(lx + h * SX - 1024 * m) = (u32x2){__builtin_amdgcn_perm(uu[2], uu[3], sel), __builtin_amdgcn_perm(uu[0], uu[1], sel)};
                    }
                    flag_set(flags + 288 + 4 * m);
                    flag_set(flags + 32 * m + 4 * (gi & 7));
                } else {
                    const gc_t a = c < 8 ? pkv + (size_t)c * (256 * 2 * DCH) : pq + (size_t)m * (512 * 2 * DCH);
                    uint32_t t_;
                    asm volatile("global_load_dword %0, %1, off\n\ts_waitcnt vmcnt(0)" : "=&v"(t_) : "v"(a) : "memory");
                }
            }
#define ROWA(T) ROW_ST(T, row_read<SA>(rg - 512 * (T)))
            STAMP(0, 1);
            ROW_WAIT(0); ROWA(0); ROWA(1); ROWA(2); ROWA(3); SB();
            STAMP(0, 2);
            ROW_WAIT(1); ROWA(4); SB();
            STAMP(0, 3);
            ROW_WAIT(2); ROWA(5); SB();
            STAMP(0, 4);
            ROW_WAIT(3); ROWA(6); SB();
            STAMP(0, 5);
            ROW_WAIT(4); ROWA(7);
        }
        STAMP(1, 6);
        STAMPW(1, 7);
        RT(0, 1);
        return;
    }
    if (w >= 8) {
        const int pw = w - 8, c2 = lane & 3, rq = lane >> 2;
        __builtin_amdgcn_s_setprio(P_IN);
        const int s0 = 64 * pw + 4 * rq;
        uint32_t eo = (uint32_t)(s0 + 2) * (2 * DCH) + grp * 16 + 4 * c2;
        OPAQUE(eo);
        char* xp = lds + (part ? 2 * c2 * CH_STRIDE : XR_OFF + 2 * c2 * SX) + 2 * (2044 - s0);
#define BLK(o, P) ((uint32_t)((o) + (P) * (256u * 2 * DCH)))
        if (part) {
            uint32_t kk[8][4], vv[8][4], hk[8][3];
#define HB 0
            uint32_t ho = (uint32_t)(64 * pw + 16 * g - 2) * (2 * DCH) + grp * 16 + 4 * c2;
            OPAQUE(ho);
            const bool hz = (pw | g) == 0;
            const uint32_t ho0 = hz ? ho + 4 * (2 * DCH) : ho;
#define LDK(P) do { _Pragma("unroll") for (int i = 0; i < 4; ++i) kk[P][i] = GLDD(uint32_t, (gc_t)kin, BLK(eo, P), (i - 2) * (2 * DCH)); } while (0)
#define LDV(P) do { _Pragma("unroll") for (int i = 0; i < 4; ++i) vv[P][i] = GLDD(uint32_t, (gc_t)vin, BLK(eo, P), (i - 2) * (2 * DCH)); } while (0)
#define LDH(P) do { _Pragma("unroll") for (int i = 0; i < 3; ++i) hk[P][i] = GLDD(uint32_t, (gc_t)kin, ((P) ? BLK(ho, P) : ho0), (i - 1) * (2 * DCH)); } while (0)
            const u32x4 wv = GLD(u32x4, (gc_t)shk, (uint32_t)(grp * 8 + 2 * c2) * 8, 0);
#pragma unroll
            for (int p = 0; p < PD; ++p) { LDK(p); LDH(p); LDV(p); SB(); }
            const float wt[2][4] = {{lo_f(wv[0]), hi_f(wv[0]), lo_f(wv[1]), hi_f(wv[1])}, {lo_f(wv[2]), hi_f(wv[2]), lo_f(wv[3]), hi_f(wv[3])}};
            STAMP(0, 1);
            u32x4 qv[8];
            uint32_t qo = (255 - (64 * pw + lane)) * (2 * DCH) + grp * 16;
            OPAQUE(qo);
#define LDQT() do { if (TS <= 4) LDQ(4); LDQ(5); LDQ(6); LDQ(7); SB(); } while (0)
#pragma unroll
            for (int p = 0; p < 8; ++p) {
                if (p == QP) LDQT();
                if (p + PD < 8 && PM == 0) { LDK(p + PD); LDH(p + PD); }
                if (p + PD < 8 && PM == 2) { LDK(p + PD); }
                SB();
                if (p == 0) {
#pragma unroll
                    for (int i = 0; i < (HB ? 1 : 3); ++i) hk[0][i] = hz ? 0u : hk[0][i];
                }
                uint32_t kp[7];
#pragma unroll
                for (int i = 0; i < 3; ++i)
                    kp[i] = (uint32_t)__builtin_amdgcn_update_dpp((int)hk[p][i], (int)kk[p][1 + i], 0x114  , 0xf, 0xf, false);
#pragma unroll
                for (int i = 0; i < 4; ++i) kp[3 + i] = kk[p][i];
#pragma unroll
                for (int h = 0; h < 2; ++h) {
                    float kf[7], vf[4];
#pragma unroll
                    for (int i = 0; i < 7; ++i) kf[i] = h ? hi_f(kp[i]) : lo_f(kp[i]);
#pragma unroll
                    for (int i = 0; i < 4; ++i) vf[i] = h ? hi_f(vv[p][i]) : lo_f(vv[p][i]);
                    uint32_t o0, o1;
                    kv4(kf, wt[h], vf, o0, o1);
                    put_x4(xp + h * CH_STRIDE - 512 * p, o0, o1);
                    SB();
                    if (h == 0 && p + PD < 8 && PM == 0) LDV(p + PD);
                    if (h == 0 && p + PD < 8 && PM == 1) { LDK(p + PD); LDH(p + PD); }
                    if (h == 0 && p + PD < 8 && PM == 2) { LDH(p + PD); }
                    SB();
                }
                flag_set(flags + 16 * p + 4 * pw);
                SB();
                if (p + PD < 8 && PM >= 1) LDV(p + PD);
                SB();
                if (p < 6) STAMP(0, 2 + p); else STAMP(1, p - 5);
            }
            if (QP == 8) LDQT();
            const char* rg = lds + ((lane >> 2) & 3) * CH_STRIDE + KA_OFF + 464 + 3584 + (64 * pw + 16 * g + 4 * (lane & 3)) * 2;
            const uint32_t oo = 2 * qo + (2 * DCH - grp * 16);
            if (TS <= 4) { ROW_WAIT(2); ROWB(4); SB(); }
            STAMP(1, 3);
            ROW_WAIT(3); ROWB(5); ROWB(6); SB();
            STAMP(1, 4);
            ROW_WAIT(4); ROWB(7);
            STAMP(1, 6);
            STAMPW(1, 7);
        } else {
            uint32_t uu[8][4];
#define LDU(P) do { _Pragma("unroll") for (int i = 0; i < 4; ++i) uu[P][i] = GLDD(uint32_t, (gc_t)u, BLK(eo, P), (i - 2) * (2 * DCH)); SB(); } while (0)
#define PUTU(P) do { _Pragma("unroll") for (int h = 0; h < 2; ++h) { \
                    const uint32_t sel = h ? 0x07060302u : 0x05040100u; \
                    *(u32x2*)(xp + h * SX - 512 * (P)) = (u32x2){__builtin_amdgcn_perm(uu[P][2], uu[P][3], sel), __builtin_amdgcn_perm(uu[P][0], uu[P][1], sel)}; } \
                flag_set(flags + 16 * (P) + 4 * pw); SB(); \
                if ((P) < 6) STAMP(0, 2 + (P)); else STAMP(1, (P) - 5); } while (0)
#pragma unroll 1
            for (int ci = pw; ci < 5; ci += 4) {
                const uint32_t lo = (uint32_t)((0x121413151aull >> (8 * ci)) & 0xff), span = (uint32_t)((0x0106080402ull >> (8 * ci)) & 0xff);
                const uint32_t one = ci < 4 ? 0x3f80u : 0xffffu, z0 = fr + 8 * g - lo;
                u32x4 cv;
#pragma unroll
                for (int q = 0; q < 4; ++q) cv[q] = (z0 + 2 * q <= span ? one : 0u) | (z0 + 2 * q + 1 <= span ? one << 16 : 0u);
                *(u32x4*)(lds + CW_OFF + 1024 * ci + 16 * lane) = cv;
            }
            const bool d0 = (gi & 7) != pw, d1 = (gi & 7) != pw + 4;
#define UPAIR(m) do { flag_wait(flags + 288 + 4 * (m)); if ((m) == 0) STAMP(0, 1); \
                if (d0) LDU(2 * (m)); if (d1) LDU(2 * (m) + 1); if (d0) PUTU(2 * (m)); if (d1) PUTU(2 * (m) + 1); } while (0)
            UPAIR(0); UPAIR(1); UPAIR(2); UPAIR(3);
        }
        RT(0, 1);
        return;
    }

    char* cb = lds + w * (part ? CH_STRIDE : SA);
    if (part) {
        __builtin_amdgcn_s_setprio(2);
        SB();
        KEEP(pf);
        OPAQUE(drb);
        kq[0][0] = pk_bf16(lo_f(kq[0][0]) + (lane ? 0.f : lo_f(drb)), hi_f(kq[0][0]));
    } else {
#define KSW 0
#define KSWP(n) do { if (KSW == (n) && w >= 4) __builtin_amdgcn_s_setprio(1); } while (0)
        if (KSW && w >= 4) __builtin_amdgcn_s_setprio(3); else __builtin_amdgcn_s_setprio(P_KG);
        char* cx = lds + XR_OFF + w * SX;
        SB();
        {
            const u32x4 z = {0, 0, 0, 0};
            *(u32x4*)(cx + 4096 + 16 * lane) = z;
            if (lane < 18) *(u32x4*)(cx + 5120 + 16 * lane) = z;
        }
        SB();
        OPAQUE(dtr);
#define FEXP(x) __builtin_amdgcn_exp2f((x) * 1.4426950408889634f)
        const float dt = rbf(FEXP(lo_f(dtr)));
        float pa[2], pb[2], pcr[2], pci[2];
        float mx = 0.f;
#define PR(i) (bt ? hi_f(pr[i]) : lo_f(pr[i]))
#pragma unroll
        for (int bt = 0; bt < 2; ++bt) {
            const float ar = -rbf(FEXP(PR(0)));
            pa[bt] = (ar * dt) * 1.4426950408889634f;
            pb[bt] = (PR(1) * dt) * 0.15915494309189535f;
            pcr[bt] = 2.f * (PR(2) * dt);
            pci[bt] = 2.f * (PR(3) * dt);
            mx = fmaxf(mx, fmaxf(fabsf(pcr[bt]), fabsf(pci[bt])));
        }
        KEEP(pf);
        OPAQUE(dra);
        const float Dv = lane ? 0.f : lo_f(dra);
        int mi = (int)__float_as_uint(mx);
#define DPPMAX(ctrl) mi = max(mi, __builtin_amdgcn_update_dpp(0, mi, ctrl, 0xf, 0xf, false))
        DPPMAX(0x111); DPPMAX(0x112); DPPMAX(0x114); DPPMAX(0x118); DPPMAX(0x142); DPPMAX(0x143);
        int sf = 266 - ((__builtin_amdgcn_readlane(mi, 63) >> 23) & 0xff);
        sf = sf < 1 ? 1 : (sf > 239 ? 239 : sf);
        const float scl = __uint_as_float((uint32_t)sf << 23);
        const float inv = __uint_as_float((uint32_t)(254 - sf) << 23);
        STAMP(0, 1);

        const char* Tr = cb + fr * KG_ROWB + g * 16;
        lds_t Tw = (lds_t)cb + 8 * lane;
        OPAQUE(Tw);
#define TW(row, v0, v1) asm volatile("ds_write_b64 %0, %1 offset:%2" :: "v"(Tw), "v"((u32x2){v0, v1}), "n"((row) * KG_ROWB) : "memory")
#define B2(X) do { _Pragma("unroll") for (int bt = 0; bt < 2; ++bt) { X; } } while (0)
#define FOR(i, n, X) _Pragma("unroll") for (int i = 0; i < (n); ++i) { X; }
        f16x8 bfr[8], af[8];
        f32x2 s[2][4], l[2][4], h[2][8], zz[2][7], cg[2][4], t[2][8];
        uint32_t s16[2][4], l16[2][4], h16[2][8], z16[2][7], c16[2][4], u[2][8];
        B2(s[bt][1] = anchor(pa[bt] * 8.f, pb[bt] * -8.f));
        B2(h[bt][1] = anchor(pa[bt], pb[bt]));
        B2(zz[bt][0] = anchor(pa[bt] * 128.f, pb[bt] * 128.f));
        B2(cg[bt][0] = ((f32x2){pcr[bt] * scl, pci[bt] * scl}));
#define M1(i, x, y) t[bt][i] = cm1(x, y)
#define M2(d, i, x, y) d = cm2(x, y, t[bt][i])
        B2(M1(0, s[bt][1], s[bt][1]); M1(1, zz[bt][0], zz[bt][0]); M1(2, h[bt][1], h[bt][1]); M1(3, cg[bt][0], zz[bt][0]));
        SB();
        B2(M2(s[bt][2], 0, s[bt][1], s[bt][1]); M2(zz[bt][1], 1, zz[bt][0], zz[bt][0]); M2(h[bt][2], 2, h[bt][1], h[bt][1]); M2(cg[bt][1], 3, cg[bt][0], zz[bt][0]));
        B2(s16[bt][1] = pk16(s[bt][1]));
        SB();
        B2(M1(0, s[bt][2], s[bt][2]); M1(1, s[bt][2], s[bt][1]); M1(2, zz[bt][1], zz[bt][1]); M1(3, h[bt][2], h[bt][2]));
        B2(s16[bt][2] = pk16(s[bt][2]));
        TW(0, 0x3c00u, 0x3c00u);
        TW(1, s16[0][1], s16[1][1]);
        SB();
        B2(M2(l[bt][1], 0, s[bt][2], s[bt][2]); M2(s[bt][3], 1, s[bt][2], s[bt][1]); M2(zz[bt][2], 2, zz[bt][1], zz[bt][1]); M2(h[bt][4], 3, h[bt][2], h[bt][2]));
        TW(2, s16[0][2], s16[1][2]);
        SB();
        B2(M1(0, l[bt][1], l[bt][1]); M1(1, h[bt][2], h[bt][1]); M1(2, cg[bt][0], zz[bt][1]); M1(3, cg[bt][1], zz[bt][1]));
        B2(s16[bt][3] = pk16(s[bt][3]); l16[bt][1] = pk16(l[bt][1]));
        SB();
        B2(M2(l[bt][2], 0, l[bt][1], l[bt][1]); M2(h[bt][3], 1, h[bt][2], h[bt][1]); M2(cg[bt][2], 2, cg[bt][0], zz[bt][1]); M2(cg[bt][3], 3, cg[bt][1], zz[bt][1]));
        TW(3, s16[0][3], s16[1][3]);
        TW(4, l16[0][1], l16[1][1]);
        SB();
        B2(M1(0, l[bt][2], l[bt][1]); FOR(i, 3, u[bt][i] = hm1(l16[bt][1], s16[bt][1 + i])); l16[bt][2] = pk16(l[bt][2]));
        SB();
        B2(M2(l[bt][3], 0, l[bt][2], l[bt][1]); FOR(i, 3, u[bt][i] = hm2(l16[bt][1], s16[bt][1 + i], u[bt][i])));
        B2(FOR(i, 3, u[bt][3 + i] = hm1(l16[bt][2], s16[bt][1 + i])));
        TW(8, l16[0][2], l16[1][2]);
        SB();
        B2(l16[bt][3] = pk16(l[bt][3]); FOR(i, 3, u[bt][3 + i] = hm2(l16[bt][2], s16[bt][1 + i], u[bt][3 + i])));
        FOR(i, 3, TW(5 + i, u[0][i], u[1][i]));
        SB();
        B2(FOR(i, 3, u[bt][i] = hm1(l16[bt][3], s16[bt][1 + i])));
        B2(M1(0, h[bt][4], h[bt][1]); M1(1, h[bt][4], h[bt][2]); M1(2, h[bt][4], h[bt][3]));
        FOR(i, 3, TW(9 + i, u[0][3 + i], u[1][3 + i]));
        TW(12, l16[0][3], l16[1][3]);
        SB();
        B2(FOR(i, 3, u[bt][i] = hm2(l16[bt][3], s16[bt][1 + i], u[bt][i])));
        B2(M2(h[bt][5], 0, h[bt][4], h[bt][1]); M2(h[bt][6], 1, h[bt][4], h[bt][2]); M2(h[bt][7], 2, h[bt][4], h[bt][3]));
        SB();
        FOR(i, 3, TW(13 + i, u[0][i], u[1][i]));
#pragma unroll
        for (int ks = 0; ks < 8; ++ks) bfr[ks] = *(const f16x8*)(Tr + ks * 64);
        B2(FOR(i, 4, c16[bt][i] = pk16(cg[bt][i]); h16[bt][1 + i] = pk16(h[bt][1 + i])));
        B2(M1(0, zz[bt][2], zz[bt][2]); M1(1, zz[bt][2], h[bt][4]));
        SB();
        B2(FOR(i, 3, h16[bt][5 + i] = pk16(h[bt][5 + i])));
        B2(M2(zz[bt][3], 0, zz[bt][2], zz[bt][2]); M2(zz[bt][4], 1, zz[bt][2], h[bt][4]));
        SB();
        KSWP(1);
        f32x4 ac[3][2];
#define TRD() do { _Pragma("unroll") for (int ks = 0; ks < 8; ++ks) af[ks] = *(const f16x8*)(Tr + ks * 64); SB(); } while (0)
#define TMM(rt) do { \
            ac[rt][0] = (f32x4){0, 0, 0, 0}; ac[rt][1] = (f32x4){0, 0, 0, 0}; \
            _Pragma("unroll") for (int ks = 0; ks < 8; ++ks) \
                ac[rt][ks & 1] = __builtin_amdgcn_mfma_f32_16x16x32_f16(af[ks], bfr[ks], ac[rt][ks & 1], 0, 0, 0); SB(); } while (0)
#define KVAL(v, rt) do { \
            _Pragma("unroll") for (int r = 0; r < 4; ++r) { \
                float t_ = ac[rt][0][r] + ac[rt][1][r]; asm volatile("" : "+v"(t_)); \
                t_ *= inv; asm volatile("" : "+v"(t_)); v[r] = t_; } } while (0)
#pragma unroll
        for (int aa = 0; aa < 4; aa += 2) {
#define HE(a, e) h16[bt][(a) < 2 ? (e) : 2 * (e)]
            B2(FOR(i, 6, u[bt][i] = hm1(c16[bt][aa + i / 3], HE(aa, 1 + i % 3))));
            TW(4 * aa, c16[0][aa], c16[1][aa]);
            TW(4 * aa + 4, c16[0][aa + 1], c16[1][aa + 1]);
            SB();
            B2(FOR(i, 6, u[bt][i] = hm2(c16[bt][aa + i / 3], HE(aa, 1 + i % 3), u[bt][i])));
            SB();
            FOR(i, 6, TW(4 * (aa + i / 3) + 1 + i % 3, u[0][i], u[1][i]));
            SB();
        }
        TRD();
        KSWP(2);
        B2(z16[bt][2] = pk16(zz[bt][2]); z16[bt][4] = pk16(zz[bt][4]); zz[bt][5] = cmp(zz[bt][3], zz[bt][2]));
        B2(FOR(i, 8, u[bt][i] = hm1(c16[bt][i >> 2], h16[bt][4 + (i & 3)])));
        SB();
        B2(FOR(i, 8, u[bt][i] = hm2(c16[bt][i >> 2], h16[bt][4 + (i & 3)], u[bt][i])));
        SB();
        FOR(i, 8, TW(i, u[0][i], u[1][i]));
        SB();
        TMM(0);
        KSWP(3);
        B2(FOR(i, 4, u[bt][2 * i] = hm1(c16[bt][i], z16[bt][2]); u[bt][2 * i + 1] = hm1(c16[bt][i], z16[bt][4])));
        B2(zz[bt][6] = cmp(zz[bt][3], zz[bt][3]); z16[bt][3] = pk16(zz[bt][3]));
        SB();
        B2(FOR(i, 4, u[bt][2 * i] = hm2(c16[bt][i], z16[bt][2], u[bt][2 * i]); u[bt][2 * i + 1] = hm2(c16[bt][i], z16[bt][4], u[bt][2 * i + 1])));
        B2(z16[bt][5] = pk16(zz[bt][5]); z16[bt][6] = pk16(zz[bt][6]));
        SB();
        FOR(i, 8, TW(8 + i, u[0][i], u[1][i]));
        SB();
        TRD();
        KSWP(4);
        f32x4 v0, v1, v2;
        KVAL(v0, 0);
        v0[0] += Dv;
        const uint32_t p01 = pk_bf16(v0[0], v0[1]), p23 = pk_bf16(v0[2], v0[3]);
        SB();
        B2(FOR(i, 4, u[bt][i] = hm1(c16[bt][i], z16[bt][3]); u[bt][4 + i] = hm1(c16[bt][i], z16[bt][5])); s16[bt][0] = hm1(c16[bt][0], z16[bt][6]));
        SB();
        B2(FOR(i, 4, u[bt][i] = hm2(c16[bt][i], z16[bt][3], u[bt][i]); u[bt][4 + i] = hm2(c16[bt][i], z16[bt][5], u[bt][4 + i]));
           s16[bt][0] = hm2(c16[bt][0], z16[bt][6], s16[bt][0]));
        SB();
        FOR(i, 8, TW(i, u[0][i], u[1][i]));
        TW(8, s16[0][0], s16[1][0]);
        SB();
        TMM(1);
        TRD();
        int tid = threadIdx.x;
        asm volatile("" : "+v"(tid));
        uint32_t kk;
        {
            const int lane = tid & 63, g = lane >> 4, fr = lane & 15;
            const bool hi = lane >= 32;
            char* dm = lds + KD_OFF + w * 1536;
            KVAL(v1, 1);
            const uint32_t q01 = pk_bf16(v1[0], v1[1]), q23 = pk_bf16(v1[2], v1[3]);
            char* a2 = cx + L2_OFF + 128 * g + 4 * fr - 140;
            *(uint32_t*)(lane >= 34 ? a2 : dm) = q01;
            *(uint32_t*)((hi ? a2 : dm) + 64) = q23;
            SB();
            TMM(2);
            STAMP(0, 2);
            const uint32_t nx = (uint32_t)__builtin_amdgcn_update_dpp(__builtin_amdgcn_readlane((int)q01, 32), (int)p01, 0x130  , 0xf, 0xf, false);
            const uint32_t nq = (uint32_t)__builtin_amdgcn_update_dpp(0, (int)q01, 0x101  , 0xf, 0xf, false);
            kq[0][0] = __builtin_amdgcn_perm(p01, p01, hi ? 0x01000100u : 0x03020100u);
            kq[0][1] = __builtin_amdgcn_perm(p23, p01, hi ? 0x05040302u : 0x07060504u);
            kq[0][2] = __builtin_amdgcn_perm(q01, p23, hi ? 0x01000100u : 0x07060504u);
            kq[0][3] = hi ? __builtin_amdgcn_alignbyte(nx, p23, 2) : q23;
            const uint32_t qz = lane < 34 ? q01 : 0u;
            kq[1][0] = __builtin_amdgcn_perm(qz, qz, 0x01000100u);
            kq[1][1] = qz;
            kq[1][2] = __builtin_amdgcn_perm(qz, qz, 0x03020302u);
            kq[1][3] = __builtin_amdgcn_perm(nq, nq, lane == 32 ? 0x01000100u : 0x0c0c0c0cu);
            put_k4(cb + LA_OFF + 32 + 16 * lane, kq[0], prev_lane(kq[0][2], 0), prev_lane(kq[0][3], 0));
            const uint32_t pv2 = (uint32_t)__builtin_amdgcn_update_dpp(__builtin_amdgcn_readlane((int)kq[0][2], 63), (int)kq[1][2], 0x111, 0xf, 0xf, false);
            const uint32_t pv3 = (uint32_t)__builtin_amdgcn_update_dpp(__builtin_amdgcn_readlane((int)kq[0][3], 63), (int)kq[1][3], 0x111, 0xf, 0xf, false);
            if ((unsigned)(lane - 32) < 6u) put_k4(cb + LA_OFF + 32 + 16 * (lane < 36 ? 32 + lane : lane - 38), kq[1], pv2, pv3);
            SB();
            KVAL(v2, 2);
            const uint32_t r01 = pk_bf16(v2[0], v2[1]), r23 = pk_bf16(v2[2], v2[3]);
            char* a3 = lane < 32 ? cx + L3_OFF + 58 + 128 * g + 2 * fr : dm;
            kk = (uint32_t)__builtin_amdgcn_readlane((int)r01, 0) & 0xffffu; kk |= kk << 16;
            *(uint16_t*)(lane == 0 ? cx + L2_OFF + 372 : lane == 32 ? cx + L3_OFF + 314 : a3) = (uint16_t)r01;
            *(uint16_t*)(a3 + 32) = (uint16_t)(r01 >> 16);
            *(uint16_t*)(a3 + 64) = (uint16_t)r23;
            *(uint16_t*)(a3 + 96) = (uint16_t)(r23 >> 16);
        }
        STAMP(0, 3);
        {
            const lds_t lb = (lds_t)lds + w * SA, lx = (lds_t)lds + XR_OFF + w * SX, sf = flags + 128 + 4 * w;
            if (w < 4) __builtin_amdgcn_s_setprio(P_CA0); else __builtin_amdgcn_s_setprio(P_CA);
            OPAQUE(tid);
            AState as;
            {
                const int g = (tid >> 4) & 3, fr = tid & 15;
                as.xb = lx + 480 - 32 * fr + 16 * g; as.cw = (lds_t)lds + CW_OFF + 16 * (tid & 63);
                as.k2a = lx + L2_OFF + 96 + 8 * fr - 32 * g; as.k3a = lx + L3_OFF + 48 + 4 * fr - 16 * g;
                as.kfa = lb + LA_OFF + (~fr & 3) * LA_STR + 8 + 8 * (fr >> 2) + 16 * g;
                as.sg = lb + 504 - 32 * fr - 8 * g; as.fl8 = flags + 4 * (tid & 7); as.sf = sf; as.kk = kk; as.hiw = w >= 4;
                OPAQUE(as.xb); OPAQUE(as.cw); OPAQUE(as.k2a); OPAQUE(as.k3a); OPAQUE(as.kfa); OPAQUE(as.sg); OPAQUE(as.fl8);
            }
#pragma unroll
            for (int i = 0; i < 9; ++i) as.acc[i] = (f32x4){0, 0, 0, 0};
            flag_wait(as.fl8);
            STAMP(0, 4);
            a_loads<-1>(as);
            SB();
            a_slots(as, std::make_integer_sequence<int, NAO>{});
            epi_a(add4(as.acc[7], as.acc[8]), as.sg);
            flag_set(sf + 128);
            STAMP(1, 6);
            RT(0, 1);
        }
        return;
    }
    int tid2 = threadIdx.x;
    asm volatile("" : "+v"(tid2));
#define lane (tid2 & 63)
#define g ((tid2 >> 4) & 3)
#define fr (tid2 & 15)
    {
        const u32x4 z = {0, 0, 0, 0};
        if (lane < 29) {
            *(u32x4*)(cb + KA_OFF + 16 * lane) = z;
            *(u32x4*)(cb + KB_OFF + 16 * lane) = z;
        }
        uint32_t c = 0;
#pragma unroll
        for (int i = 0; i < 4; ++i) {
            put_k<KA_OFF + 464, KB_OFF + 464>(cb, 64 * i + lane, kq[i], prev_lane(kq[i][3], c));
            c = __builtin_amdgcn_readlane(kq[i][3], 63);
        }
    }
    STAMP(0, 3);

    const lds_t lb = (lds_t)lds + w * CH_STRIDE;
    const lds_t kaddr = lb + ((fr & 1) ? KA_OFF : KB_OFF) + 2 * (32 * (fr >> 1) + 8 * g + 8);
    lds_t xaddr = lb + ((fr & 1) ? (XA_OFF + 2 * (1822 - 2 * fr + 8 * g)) : (XB_OFF + 2 * (1820 - 2 * fr + 8 * g))) - 3584;
    asm volatile("" : "+v"(xaddr));
    const int lo_ = 64 * g + 2 * fr;
    char* sg = cb + KA_OFF + 464 + 2 * (2046 - lo_);
#define EPI(T) epi_acc(acc[T], sg - 512 * (T))
#define SFLAG(S) flag_set(flags + 128 + 32 * (S) + 4 * w)

    BState bs;
    bs.kaddr = kaddr; bs.xaddr = xaddr; bs.sg = sg; bs.fl8 = flags + 4 * (lane & 7); bs.sfl = flags + 128 + 4 * w;
#pragma unroll
    for (int i = 0; i < 10; ++i) bs.acc[i] = (f32x4){0, 0, 0, 0};
    b_loads<-1>(bs);
    SB();
    flag_wait(flags + 4 * (lane & 3));
    STAMP(0, 4);
    SB();
    b_slots(bs, std::make_integer_sequence<int, NMF>{});
    epi_acc(add4(bs.acc[7], bs.acc[9]), sg - 3584);
    flag_set(bs.sfl + 128);
    STAMP(1, 6);
    RT(0, 1);
}

extern "C" void kernel_launch(void* const* d_in, const int* in_sizes, int n_in,
                              void* d_out, int out_size, void* d_ws, size_t ws_size,
                              hipStream_t stream) {
    fused_kernel<<<256, 1024, 0, stream>>>(
        (const uint16_t*)d_in[0], (const uint16_t*)d_in[1], (const uint16_t*)d_in[2], (const uint16_t*)d_in[3],
        (const uint16_t*)d_in[4], (const uint16_t*)d_in[5], (const uint16_t*)d_in[6], (const uint16_t*)d_in[7],
        (const uint16_t*)d_in[8], (const uint16_t*)d_in[9], (const uint16_t*)d_in[10], (const uint16_t*)d_in[11],
        (const uint16_t*)d_in[12], (uint16_t*)d_out);
}
```
